```python
import torch

def _softmax(x, axis=-1):
    return torch.softmax(x, dim=axis)
def _gelu(x):
    return torch.nn.functional.gelu(x, approximate="tanh")
def _silu(x):
    return torch.nn.functional.silu(x)
def _sigmoid(x):
    return torch.sigmoid(x)
def _layernorm(x, axis=-1, eps=1e-5):
    m = x.mean(dim=axis, keepdim=True)
    v = x.var(dim=axis, keepdim=True, unbiased=False)
    return (x - m) / torch.sqrt(v + eps)
def _scatter_add(tgt, idx, src, axis=0):
    return tgt.clone().index_add_(axis, idx, src)
def _segment_sum(src, seg, n):
    out = torch.zeros((n,) + src.shape[1:], dtype=src.dtype)
    return out.index_add_(0, seg, src)
def _pad(x, pw):
    flat = tuple(v for pair in reversed(pw) for v in pair)
    return torch.nn.functional.pad(x, flat)

kernel_name = 'recurrent_scan__sliding_block_cached__glu_gated_bilinear__prompt'

def setup_inputs():

    _w = (lambda a: (lambda t: t.to(torch.bfloat16) if t.is_floating_point() else t)(torch.as_tensor(a)))
    def _A_setup():
        B, Sq, D = 1, 2048, 448
        Hq, Hkv, Sk, Dh = 16, 8, 2048, 256
        assert Hq % Hkv == 0, (Hq, Hkv)
        return {
            "x": _w((lambda *s: torch.randn(*s, dtype=torch.bfloat16) * 0.05)(B, Sq, D)),
            "k_cache": _w((lambda *s: torch.randn(*s, dtype=torch.bfloat16) * 1.6)(B, Hkv, Sk, Dh)),
            "v_cache": _w((lambda *s: torch.randn(*s, dtype=torch.bfloat16) * 1.6)(B, Hkv, Sk, Dh)),
            "wq": _w((lambda *s: torch.randn(*s, dtype=torch.bfloat16) * 1.6)(D, Hq * Dh) * (D ** -0.5)),
            "wo": _w((lambda *s: torch.randn(*s, dtype=torch.bfloat16) * 1.6)(Hq * Dh, D) * ((Hq * Dh) ** -0.5)),
        }
    _A_in = _A_setup()

    def _B_setup():
        B, S, D, F = 1, 2048, 448, 1792
        return {
            "x": _w((lambda *s: torch.randn(*s, dtype=torch.bfloat16) * 0.05)(B, S, D)),
            "w_gate": _w((lambda *s: torch.randn(*s, dtype=torch.bfloat16) * 0.2)(D, F) * (D ** -0.5)),
            "w_up": _w((lambda *s: torch.randn(*s, dtype=torch.bfloat16) * 0.2)(D, F) * (D ** -0.5)),
            "w_down": _w((lambda *s: torch.randn(*s, dtype=torch.bfloat16) * 0.2)(F, D) * (F ** -0.5)),
        }
    _B_in = _B_setup()

    return {
        "A_x": _A_in["x"],
        "A_k_cache": _A_in["k_cache"],
        "A_v_cache": _A_in["v_cache"],
        "A_wq": _A_in["wq"],
        "A_wo": _A_in["wo"],
        "B_x": _B_in["x"],
        "B_w_gate": _B_in["w_gate"],
        "B_w_up": _B_in["w_up"],
        "B_w_down": _B_in["w_down"],
    }

def reference(A_x, A_k_cache, A_v_cache, A_wq, A_wo, B_x, B_w_gate, B_w_up, B_w_down):
    def _A_ref(x, k_cache, v_cache, wq, wo):
        B, Sq, D = x.shape
        Hq, Dh = 16, 256
        Hkv, Sk = k_cache.shape[1], k_cache.shape[2]
        W = min(4096, max(256, Sk // 4))
        rep = Hq // Hkv
        q = (lambda x, *d: x.permute(*d))((x @ wq).reshape(B, Sq, Hq, Dh), 0, 2, 1, 3)
        k = (lambda x, n, axis: torch.repeat_interleave(x, n, dim=axis))(k_cache[:, :, Sk - W:, :], rep, axis=1)
        v = (lambda x, n, axis: torch.repeat_interleave(x, n, dim=axis))(v_cache[:, :, Sk - W:, :], rep, axis=1)
        scores = torch.einsum("bhqd,bhkd->bhqk", q, k) * (Dh ** -0.5)
        p = _softmax(scores, axis=-1)
        o = torch.einsum("bhqk,bhkd->bhqd", p, v)
        return (lambda x, *d: x.permute(*d))(o, 0, 2, 1, 3).reshape(B, Sq, Hq * Dh) @ wo

    def _B_ref(x, w_gate, w_up, w_down):
        g = torch.einsum("bsd,df->bsf", x, w_gate)
        u = torch.einsum("bsd,df->bsf", x, w_up)
        h = g * u
        return torch.einsum("bsf,fd->bsd", h, w_down)

    x = A_x
    xn = _layernorm(x, axis=-1)
    s = _A_ref(xn, A_k_cache, A_v_cache, A_wq, A_wo)
    g = _silu(_B_ref(xn, B_w_gate, B_w_up, B_w_down))
    return x + s * g
```

```cpp
#include <hip/hip_runtime.h>
#include <hip/hip_bf16.h>
typedef __hip_bfloat16 bf16;
static __device__ __forceinline__ float b2f(bf16 x){return __bfloat162float(x);}
static __device__ __forceinline__ bf16 f2b(float x){return __float2bfloat16(x);}
static __device__ __forceinline__ float rb(float x){return b2f(f2b(x));}

__global__ void ln_k(const bf16* x, bf16* xn){
  int r=blockIdx.x; __shared__ float red[64];
  float s=0; for(int i=threadIdx.x;i<448;i+=64) s+=b2f(x[r*448+i]);
  for(int o=32;o;o>>=1) s+=__shfl_xor(s,o);
  float m=s/448.f; float v=0; for(int i=threadIdx.x;i<448;i+=64){float d=b2f(x[r*448+i])-m; v+=d*d;}
  for(int o=32;o;o>>=1) v+=__shfl_xor(v,o);
  v/=448.f; float mb=rb(m), vb=rb(v); float den=rb(sqrtf(rb(vb+1e-5f)));
  for(int i=threadIdx.x;i<448;i+=64) xn[r*448+i]=f2b(rb(b2f(x[r*448+i])-mb)/den);
}
template<bool BT>
__global__ void gemm_k(const bf16*A,int lda,long sa,const bf16*B,int ldb,long sb,int bdiv,bf16*C,int ldc,long sc,int M,int N,int K,float alpha){
  __shared__ float As[32][33],Bs[32][33];
  int z=blockIdx.z; A+=sa*z; B+=sb*(z/bdiv); C+=sc*z;
  int tx=threadIdx.x,ty=threadIdx.y; int row=blockIdx.y*32+ty,col=blockIdx.x*32+tx; float acc=0;
  for(int k0=0;k0<K;k0+=32){
    As[ty][tx]=b2f(A[(long)row*lda+k0+tx]);
    if(BT) Bs[tx][ty]=b2f(B[(long)(blockIdx.x*32+ty)*ldb+k0+tx]); else Bs[ty][tx]=b2f(B[(long)(k0+ty)*ldb+col]);
    __syncthreads();
    for(int k=0;k<32;k++) acc+=As[ty][k]*Bs[k][tx];
    __syncthreads();
  }
  float o=rb(acc); if(alpha!=1.f) o=o*alpha;
  C[(long)row*ldc+col]=f2b(o);
}
__global__ void sm_k(bf16* s){
  long r=blockIdx.x; bf16*p=s+r*512; float v[8]; float m=-1e30f;
  for(int i=0;i<8;i++){v[i]=b2f(p[threadIdx.x+64*i]); m=fmaxf(m,v[i]);}
  for(int o=32;o;o>>=1) m=fmaxf(m,__shfl_xor(m,o));
  float t=0; for(int i=0;i<8;i++){v[i]=__expf(v[i]-m); t+=v[i];}
  for(int o=32;o;o>>=1) t+=__shfl_xor(t,o);
  for(int i=0;i<8;i++) p[threadIdx.x+64*i]=f2b(v[i]/t);
}
__global__ void mul_k(bf16*g,const bf16*u,int n){int i=blockIdx.x*256+threadIdx.x; if(i<n) g[i]=f2b(b2f(g[i])*b2f(u[i]));}
__global__ void fin_k(const bf16*x,const bf16*s,const bf16*m,bf16*o,int n){int i=blockIdx.x*256+threadIdx.x; if(i<n){float mm=b2f(m[i]); float g=rb(mm/(1.f+__expf(-mm))); o[i]=f2b(b2f(x[i])+rb(b2f(s[i])*g));}}

extern "C" void kernel_launch(void* const* d_in, const int* in_sizes, int n_in, void* d_out, int out_size, void* d_ws, size_t ws_size, hipStream_t stream) {
  const bf16* x=(const bf16*)d_in[0]; const bf16* kc=(const bf16*)d_in[1]; const bf16* vc=(const bf16*)d_in[2];
  const bf16* wq=(const bf16*)d_in[3]; const bf16* wo=(const bf16*)d_in[4];
  const bf16* wg=(const bf16*)d_in[6]; const bf16* wu=(const bf16*)d_in[7]; const bf16* wd=(const bf16*)d_in[8];
  bf16* ws=(bf16*)d_ws; bf16* xn=ws; bf16* q=xn+2048*448; bf16* sc=q+2048L*4096; bf16* o=sc+16L*2048*512; bf16* g=o+2048L*4096; bf16* u=g+2048L*1792; bf16* s=u+2048L*1792; bf16* m=s+2048*448;
  dim3 b(32,32);
  ln_k<<<2048,64,0,stream>>>(x,xn);
  gemm_k<false><<<dim3(4096/32,64,1),b,0,stream>>>(xn,448,0,wq,4096,0,1,q,4096,0,2048,4096,448,1.f);
  gemm_k<true><<<dim3(512/32,64,16),b,0,stream>>>(q,4096,256,kc+1536*256,256,2048L*256,2,sc,512,2048L*512,2048,512,256,0.0625f);
  sm_k<<<16*2048,64,0,stream>>>(sc);
  gemm_k<false><<<dim3(256/32,64,16),b,0,stream>>>(sc,512,2048L*512,vc+1536*256,256,2048L*256,2,o,4096,256,2048,256,512,1.f);
  gemm_k<false><<<dim3(448/32,64,1),b,0,stream>>>(o,4096,0,wo,448,0,1,s,448,0,2048,448,4096,1.f);
  gemm_k<false><<<dim3(1792/32,64,1),b,0,stream>>>(xn,448,0,wg,1792,0,1,g,1792,0,2048,1792,448,1.f);
  gemm_k<false><<<dim3(1792/32,64,1),b,0,stream>>>(xn,448,0,wu,1792,0,1,u,1792,0,2048,1792,448,1.f);
  mul_k<<<2048*1792/256,256,0,stream>>>(g,u,2048*1792);
  gemm_k<false><<<dim3(448/32,64,1),b,0,stream>>>(g,1792,0,wd,448,0,1,m,448,0,2048,448,1792,1.f);
  fin_k<<<2048*448/256,256,0,stream>>>(x,s,m,(bf16*)d_out,2048*448);
}
```

```cpp
#include <hip/hip_runtime.h>
extern "C" __global__ __launch_bounds__(256,1) void fused_block(const void* x,const void* k,const void* v,const void* wq,const void* wo,const void* wg,const void* wu,const void* wd,void* ws,void* out) {
  unsigned tid=threadIdx.x;
  asm volatile(R"ASM(
v_mov_b32 v255, %0
L_begin:
s_load_dwordx16 s[4:19], %1, 0x0
s_load_dwordx4 s[20:23], %1, 0x40
s_getpc_b64 s[64:65]
s_and_b32 s65, s65, 0xffff
s_and_b32 s64, s64, 0xffffff80
s_mov_b32 s66, L_end-L_begin
s_mov_b32 s67, 0x20000
s_lshr_b32 s87, %2, 3
s_mul_i32 s87, s87, 2560
v_lshlrev_b32 v208, 7, v255
v_cmp_gt_u32 vcc, 20, v255
s_nop 3
s_mov_b64 exec, vcc
s_cbranch_execz L_nopf
buffer_load_dword a174, v208, s[64:67], s87 offen offset:0
L_nopf:
s_mov_b64 exec, -1
v_lshrrev_b32 v207, 6, v255
v_and_b32 v200, 63, v255
v_readfirstlane_b32 s68, v207
s_and_b32 s87, %2, 7
s_lshr_b32 s88, %2, 3
s_and_b32 s69, s87, 1
s_lshl_b32 s69, s69, 3
s_and_b32 s31, s88, 7
s_add_u32 s69, s69, s31
s_lshr_b32 s70, s87, 1
s_lshl_b32 s70, s70, 2
s_lshr_b32 s89, s88, 3
s_add_u32 s70, s70, s89
s_lshr_b32 s28, s70, 1
s_and_b32 s29, s70, 1
v_cmp_lt_u32 vcc, 27, v200
s_lshl_b32 s77, s68, 13
s_lshl_b32 s87, s69, 7
v_cndmask_b32 v207, 0, 1, vcc
v_mul_u32_u24 v208, 28, v207
v_sub_u32 v208, v200, v208
v_xor_b32 v208, v208, v207
v_mul_u32_u24 v207, 896, v207
v_lshl_add_u32 v192, v208, 4, v207
v_add_u32 v193, 864, v192
v_add_u32 v194, 1728, v192
v_add_u32 v195, 2592, v192
s_lshl_b32 s88, s68, 5
s_add_u32 s87, s87, s88
s_waitcnt lgkmcnt(0)
s_mul_i32 s35, s87, 896
s_mov_b64 s[36:37], s[4:5]
s_and_b32 s37, s37, 0xffff
s_mov_b32 s38, 0x1c0000
s_mov_b32 s39, 0x20000
s_add_u32 m0, s77, 0
s_add_u32 s71, s35, 0
s_mov_b32 exec_hi, 0x00ffffff
buffer_load_dwordx4 v192, s[36:39], s71 offen offset:0 lds
s_mov_b32 exec_hi, -1
v_and_b32 v201, 31, v200
v_lshrrev_b32 v202, 5, v200
v_and_b32 v203, 15, v200
v_bfe_u32 v204, v200, 2, 2
v_and_b32 v205, 3, v200
v_lshrrev_b32 v206, 4, v200
s_mov_b64 s[64:65], s[20:21]
s_and_b32 s65, s65, 0xffff
s_mov_b32 s66, 0x10000000
s_mov_b32 s67, 0x20000
s_lshl_b32 s82, s69, 4
s_add_u32 s82, s82, s70
s_mov_b32 exec_hi, 0x00ffffff
buffer_load_dwordx4 v193, s[36:39], s71 offen offset:928 lds
s_mov_b32 exec_hi, -1
s_lshl_b32 s82, s82, 2
s_add_u32 s82, s82, s68
s_lshl_b32 s82, s82, 10
s_add_u32 s82, s82, 33554432
v_lshlrev_b32 v184, 5, v203
v_accvgpr_write_b32 a181, v184
s_and_b32 s87, s68, 1
s_lshr_b32 s30, s68, 1
s_cmp_eq_u32 s87, 0
s_cselect_b64 s[44:45], s[14:15], s[16:17]
s_and_b32 s45, s45, 0xffff
s_mov_b32 s46, 0x188000
s_mov_b32 exec_hi, 0x00ffffff
buffer_load_dwordx4 v194, s[36:39], s71 offen offset:1856 lds
s_mov_b32 exec_hi, -1
s_mov_b32 s47, 0x20000
s_mul_i32 s78, s87, 14464
s_mul_i32 s89, s30, 7168
s_add_u32 s78, s78, s89
s_mul_i32 s73, s70, 224
s_mul_i32 s89, s30, 114688
s_add_u32 s73, s73, s89
v_add_u32 v207, 0, v200
v_mul_u32_u24 v208, 4682, v207
v_lshrrev_b32 v208, 16, v208
v_mul_u32_u24 v208, 3360, v208
v_lshl_add_u32 v207, v207, 4, v208
s_mov_b32 exec_hi, 0x00ffffff
buffer_load_dwordx4 v195, s[36:39], s71 offen offset:2784 lds
s_mov_b32 exec_hi, -1
v_subrev_u32 v236, 0, v207
v_add_u32 v207, 64, v200
v_mul_u32_u24 v208, 4682, v207
v_lshrrev_b32 v208, 16, v208
v_mul_u32_u24 v208, 3360, v208
v_lshl_add_u32 v207, v207, 4, v208
v_subrev_u32 v237, 1024, v207
v_add_u32 v207, 128, v200
v_mul_u32_u24 v208, 4682, v207
v_lshrrev_b32 v208, 16, v208
v_mul_u32_u24 v208, 3360, v208
v_lshl_add_u32 v207, v207, 4, v208
s_add_u32 m0, s77, 3712
s_add_u32 s71, s35, 7168
s_mov_b32 exec_hi, 0x00ffffff
buffer_load_dwordx4 v192, s[36:39], s71 offen offset:0 lds
s_mov_b32 exec_hi, -1
v_subrev_u32 v238, 2048, v207
v_add_u32 v207, 192, v200
v_mul_u32_u24 v208, 4682, v207
v_lshrrev_b32 v208, 16, v208
v_mul_u32_u24 v208, 3360, v208
v_lshl_add_u32 v207, v207, 4, v208
v_subrev_u32 v239, 3072, v207
v_add_u32 v207, 256, v200
v_mul_u32_u24 v208, 4682, v207
v_lshrrev_b32 v208, 16, v208
v_mul_u32_u24 v208, 3360, v208
v_lshl_add_u32 v207, v207, 4, v208
s_mov_b32 exec_hi, 0x00ffffff
buffer_load_dwordx4 v193, s[36:39], s71 offen offset:928 lds
s_mov_b32 exec_hi, -1
v_subrev_u32 v240, 0, v207
v_add_u32 v207, 320, v200
v_mul_u32_u24 v208, 4682, v207
v_lshrrev_b32 v208, 16, v208
v_mul_u32_u24 v208, 3360, v208
v_lshl_add_u32 v207, v207, 4, v208
v_subrev_u32 v241, 1024, v207
v_add_u32 v207, 384, v200
v_mul_u32_u24 v208, 4682, v207
v_lshrrev_b32 v208, 16, v208
v_mul_u32_u24 v208, 3360, v208
v_lshl_add_u32 v207, v207, 4, v208
s_mov_b32 exec_hi, 0x00ffffff
buffer_load_dwordx4 v194, s[36:39], s71 offen offset:1856 lds
s_mov_b32 exec_hi, -1
v_subrev_u32 v242, 2048, v207
s_mov_b64 s[40:41], s[10:11]
s_and_b32 s41, s41, 0xffff
s_mov_b32 s42, 0x380000
s_mov_b32 s43, 0x20000
s_lshl_b32 s31, s69, 1
s_add_u32 s31, s31, s29
s_lshl_b32 s31, s31, 2
s_add_u32 s31, s31, s68
s_lshl_b32 s88, s28, 10
s_mul_i32 s87, s31, 16
v_add_u32 v207, s87, v200
s_mov_b32 exec_hi, 0x00ffffff
buffer_load_dwordx4 v195, s[36:39], s71 offen offset:2784 lds
s_mov_b32 exec_hi, -1
v_lshrrev_b32 v208, 3, v207
v_and_b32 v207, 7, v207
v_lshlrev_b32 v207, 7, v207
v_lshl_add_u32 v191, v208, 13, v207
v_add_u32 v191, s88, v191
s_mul_i32 s87, s31, 12
v_add_u32 v207, s87, v200
v_lshrrev_b32 v208, 3, v207
v_and_b32 v207, 7, v207
v_lshlrev_b32 v207, 7, v207
v_lshl_add_u32 v188, v208, 13, v207
s_add_u32 s88, s88, 2097152
s_add_u32 m0, s77, 32768
s_add_u32 s71, s35, 14336
s_mov_b32 exec_hi, 0x00ffffff
buffer_load_dwordx4 v192, s[36:39], s71 offen offset:0 lds
s_mov_b32 exec_hi, -1
v_add_u32 v188, s88, v188
v_accvgpr_write_b32 a208, v188
s_lshr_b32 s87, s31, 2
s_lshl_b32 s87, s87, 1
s_add_u32 s87, s87, s30
s_mul_i32 s89, s87, 3584
s_mul_i32 s88, s28, 448
s_and_b32 s88, s88, 0xffffff80
s_add_u32 s89, s89, s88
v_lshlrev_b32 v189, 7, v200
v_add_u32 v189, s89, v189
s_mov_b64 exec, 0xf
buffer_load_dword a175, v189, s[44:47], 0 offen offset:0
s_mov_b64 exec, -1
s_mov_b32 exec_hi, 0x00ffffff
buffer_load_dwordx4 v193, s[36:39], s71 offen offset:928 lds
s_mov_b32 exec_hi, -1
s_mul_i32 s87, s87, 21504
s_mul_i32 s88, s28, 448
s_and_b32 s88, s88, 0xffffff80
s_add_u32 s87, s87, s88
s_add_u32 s87, s87, 229376
v_lshrrev_b32 v207, 2, v200
v_mul_u32_u24 v207, 3584, v207
v_lshl_add_u32 v207, v205, 7, v207
v_add_u32 v207, s87, v207
v_accvgpr_write_b32 a209, v207
v_lshlrev_b32 v207, 7, v200
s_mul_i32 s87, s31, 2048
s_mov_b32 exec_hi, 0x00ffffff
buffer_load_dwordx4 v194, s[36:39], s71 offen offset:1856 lds
s_mov_b32 exec_hi, -1
s_mul_i32 s88, s28, 1048576
s_add_u32 s87, s87, s88
s_add_u32 s87, s87, 786432
v_add_u32 v208, s87, v207
v_accvgpr_write_b32 a200, v208
s_mul_i32 s87, s31, 2048
s_mul_i32 s88, s28, 1048576
s_add_u32 s87, s87, s88
s_add_u32 s87, s87, 786432
v_add_u32 v208, s87, v207
v_accvgpr_write_b32 a201, v208
s_mul_i32 s87, s31, 1664
s_mov_b32 exec_hi, 0x00ffffff
buffer_load_dwordx4 v195, s[36:39], s71 offen offset:2784 lds
s_mov_b32 exec_hi, -1
s_mul_i32 s88, s28, 200704
s_add_u32 s87, s87, s88
v_add_u32 v208, s87, v207
v_accvgpr_write_b32 a202, v208
s_mul_i32 s87, s31, 3584
s_mul_i32 s88, s28, 458752
s_add_u32 s87, s87, s88
v_add_u32 v208, s87, v207
v_accvgpr_write_b32 a203, v208
v_lshrrev_b32 v207, 1, v203
v_mul_u32_u24 v210, 928, v207
v_and_b32 v207, 1, v203
s_add_u32 m0, s77, 36480
s_add_u32 s71, s35, 21504
s_mov_b32 exec_hi, 0x00ffffff
buffer_load_dwordx4 v192, s[36:39], s71 offen offset:0 lds
s_mov_b32 exec_hi, -1
v_mul_u32_u24 v208, 448, v207
v_xor_b32 v207, v207, v202
v_lshl_add_u32 v207, v207, 4, v208
v_add3_u32 v210, v210, v207, s77
v_and_b32 v207, 1, v206
v_lshl_add_u32 v210, v207, 3, v210
v_add_u32 v211, 32768, v210
v_add_u32 v217, 65536, v210
v_add_u32 v218, 98304, v210
v_mov_b32 v212, 0x3f803f80
v_mov_b32 v213, 0
v_mov_b32 v214, 0
s_mov_b32 exec_hi, 0x00ffffff
buffer_load_dwordx4 v193, s[36:39], s71 offen offset:928 lds
s_mov_b32 exec_hi, -1
v_mov_b32 v215, 0
v_mov_b32 v216, 0
v_lshlrev_b32 v207, 1, v202
v_xor_b32 v207, v207, v201
v_lshlrev_b32 v207, 4, v207
v_lshl_add_u32 v232, v202, 13, v207
v_xor_b32 v208, 64, v232
v_add_u32 v233, 15360, v208
v_xor_b32 v208, 128, v232
v_add_u32 v234, 30720, v208
v_xor_b32 v208, 192, v232
v_add_u32 v235, 46080, v208
s_mov_b32 exec_hi, 0x00ffffff
buffer_load_dwordx4 v194, s[36:39], s71 offen offset:1856 lds
s_mov_b32 exec_hi, -1
v_lshl_add_u32 v208, v202, 9, v207
v_accvgpr_write_b32 a177, v208
s_lshl_b32 s72, s70, 9
s_lshl_b32 s88, s68, 17
s_add_u32 s72, s72, s88
v_lshrrev_b32 v190, 2, v200
v_and_b32 v186, 7, v190
v_lshlrev_b32 v187, 3, v205
v_lshl_add_u32 v209, v186, 5, v187
v_lshl_add_u32 v207, v190, 9, v209
v_add_u32 v224, 0, v207
v_xor_b32 v225, 32, v224
s_mov_b32 exec_hi, 0x00ffffff
buffer_load_dwordx4 v195, s[36:39], s71 offen offset:2784 lds
s_mov_b32 exec_hi, -1
v_xor_b32 v226, 64, v224
v_xor_b32 v227, 96, v224
v_xor_b32 v228, 128, v224
v_xor_b32 v229, 160, v224
v_xor_b32 v230, 192, v224
v_xor_b32 v231, 224, v224
v_add_u32 v207, 0, v207
v_accvgpr_write_b32 a179, v207
v_mul_u32_u24 v207, 224, v190
v_add_u32 v207, v207, v187
v_add_u32 v243, 131072, v207
s_mov_b32 s84, 0
s_add_u32 m0, s77, 65536
s_add_u32 s71, s35, 448
s_mov_b32 exec_hi, 0x00ffffff
buffer_load_dwordx4 v192, s[36:39], s71 offen offset:0 lds
s_mov_b32 exec_hi, -1
s_mov_b32 s85, 0x41a00000
s_mov_b32 s86, 0xffff0000
s_mov_b64 s[52:53], s[6:7]
s_and_b32 s53, s53, 0xffff
s_mov_b32 s54, 0x800000
s_mov_b32 s55, 0x20000
s_mov_b64 s[56:57], s[8:9]
s_and_b32 s57, s57, 0xffff
s_mov_b32 s58, 0x800000
s_mov_b32 s59, 0x20000
s_mov_b64 s[60:61], s[12:13]
s_and_b32 s61, s61, 0xffff
s_mov_b32 exec_hi, 0x00ffffff
buffer_load_dwordx4 v193, s[36:39], s71 offen offset:928 lds
s_mov_b32 exec_hi, -1
s_mov_b32 s62, 0x380000
s_mov_b32 s63, 0x20000
s_mov_b64 s[48:49], s[18:19]
s_and_b32 s49, s49, 0xffff
s_mov_b32 s50, 0x188000
s_mov_b32 s51, 0x20000
s_mul_i32 s79, s68, 7168
s_lshl_b32 s75, s28, 20
s_add_u32 s75, s75, 786432
s_add_u32 s75, s75, s77
s_mul_i32 s76, s70, 229376
s_mul_i32 s89, s68, 57344
s_mov_b32 exec_hi, 0x00ffffff
buffer_load_dwordx4 v194, s[36:39], s71 offen offset:1856 lds
s_mov_b32 exec_hi, -1
s_add_u32 s76, s76, s89
s_and_b32 s27, s69, 7
s_mov_b32 s24, s29
s_cmp_gt_u32 s28, s27
s_subb_u32 s25, s28, 0
s_mul_i32 s98, s24, 7
s_add_u32 s98, s98, s25
s_cmp_eq_u32 s28, s27
s_cselect_b32 s98, 14, s98
s_cmp_ge_u32 s98, 14
s_cselect_b32 s26, 0x40000000, 0
s_and_b32 s99, s68, 1
s_mov_b32 exec_hi, 0x00ffffff
buffer_load_dwordx4 v195, s[36:39], s71 offen offset:2784 lds
s_mov_b32 exec_hi, -1
s_mul_i32 s74, s68, 50176
s_lshl_b32 s89, s25, 7
s_add_u32 s74, s74, s89
s_add_u32 s74, s74, s26
s_mul_i32 s87, s69, 1896448
s_add_u32 s87, s87, 0
s_mul_i32 s80, s30, 946176
s_add_u32 s80, s80, s87
s_lshl_b32 s88, s99, 16
s_add_u32 s80, s80, s88
s_lshl_b32 s88, s70, 11
s_add_u32 s80, s80, s88
s_add_u32 m0, s77, 69248
s_add_u32 s71, s35, 7616
s_mov_b32 exec_hi, 0x00ffffff
buffer_load_dwordx4 v192, s[36:39], s71 offen offset:0 lds
s_mov_b32 exec_hi, -1
s_mul_i32 s95, s98, 135168
s_add_u32 s95, s95, s87
s_lshl_b32 s88, s68, 15
s_add_u32 s95, s95, s88
s_mul_i32 s87, s69, 458752
s_add_u32 s87, s87, 41943040
s_mul_i32 s94, s70, 28672
s_add_u32 s94, s94, s87
s_add_u32 s94, s94, s79
s_lshl_b32 s88, s24, 1
s_add_u32 s88, s88, s30
s_mul_i32 s88, s88, 7168
s_mov_b32 exec_hi, 0x00ffffff
buffer_load_dwordx4 v193, s[36:39], s71 offen offset:928 lds
s_mov_b32 exec_hi, -1
s_add_u32 s87, s87, s88
s_mul_i32 s92, s99, 3072
s_add_u32 s92, s92, s87
s_lshl_b32 s93, s99, 3
s_add_u32 s93, s93, s87
s_add_u32 s93, s93, 6144
s_lshl_b32 s87, s69, 16
s_add_u32 s87, s87, 33554432
s_lshl_b32 s88, s70, 5
s_add_u32 s83, s87, s88
s_lshl_b32 s88, s70, 2
s_add_u32 s96, s87, s88
s_mov_b32 exec_hi, 0x00ffffff
buffer_load_dwordx4 v194, s[36:39], s71 offen offset:1856 lds
s_mov_b32 exec_hi, -1
s_add_u32 s96, s96, 512
v_lshrrev_b32 v207, 1, v186
v_lshl_add_u32 v207, v207, 5, v187
v_lshl_add_u32 v207, v190, 7, v207
v_add_u32 v207, 131072, v207
v_accvgpr_write_b32 a185, v207
v_lshlrev_b32 v186, 4, v200
v_accvgpr_write_b32 a190, v186
v_add_u32 v186, s26, v186
v_accvgpr_write_b32 a183, v186
s_lshl_b32 s88, s99, 1
v_lshrrev_b32 v207, 1, v203
s_mov_b32 exec_hi, 0x00ffffff
buffer_load_dwordx4 v195, s[36:39], s71 offen offset:2784 lds
s_mov_b32 exec_hi, -1
v_cmp_le_u32 vcc, s27, v207
v_and_b32 v208, 1, v203
v_mov_b32 v186, s68
v_addc_co_u32 v207, vcc, 0, v207, vcc
v_lshlrev_b32 v207, 1, v207
v_add_u32 v207, s30, v207
s_lshl_b32 s87, s27, 1
v_add_u32 v209, s87, v208
v_add_u32 v208, s88, v208
v_cmp_lt_u32 vcc, 13, v203
s_nop 1
v_cndmask_b32 v207, v207, v209, vcc
v_cndmask_b32 v208, v208, v186, vcc
s_waitcnt vmcnt(8)
ds_read2_b64 v[0:3], v210 offset0:0 offset1:4
ds_read2_b64 v[4:7], v211 offset0:0 offset1:4
ds_read2_b64 v[8:11], v210 offset0:8 offset1:12
ds_read2_b64 v[12:15], v211 offset0:8 offset1:12
ds_read2_b64 v[16:19], v210 offset0:16 offset1:20
ds_read2_b64 v[20:23], v211 offset0:16 offset1:20
ds_read2_b64 v[24:27], v210 offset0:24 offset1:28
ds_read2_b64 v[28:31], v211 offset0:24 offset1:28
ds_read2_b64 v[32:35], v210 offset0:32 offset1:36
ds_read2_b64 v[36:39], v211 offset0:32 offset1:36
ds_read2_b64 v[40:43], v210 offset0:40 offset1:44
ds_read2_b64 v[44:47], v211 offset0:40 offset1:44
ds_read2_b64 v[48:51], v210 offset0:48 offset1:52
ds_read2_b64 v[52:55], v211 offset0:48 offset1:52
s_add_u32 m0, s77, 98304
s_add_u32 s71, s35, 14784
s_mov_b32 exec_hi, 0x00ffffff
buffer_load_dwordx4 v192, s[36:39], s71 offen offset:0 lds
s_mov_b32 exec_hi, -1
s_waitcnt lgkmcnt(13)
v_dot2c_f32_bf16 v213, v0, v212
v_dot2c_f32_bf16 v215, v0, v0
s_waitcnt lgkmcnt(12)
v_dot2c_f32_bf16 v214, v4, v212
v_dot2c_f32_bf16 v216, v4, v4
v_dot2c_f32_bf16 v213, v1, v212
v_dot2c_f32_bf16 v215, v1, v1
v_dot2c_f32_bf16 v214, v5, v212
v_dot2c_f32_bf16 v216, v5, v5
v_lshl_add_u32 v207, v207, 2, v208
v_lshlrev_b32 v208, 28, v206
v_lshl_add_u32 v208, v207, 10, v208
v_accvgpr_write_b32 a182, v208
s_mov_b32 exec_hi, 0x00ffffff
buffer_load_dwordx4 v193, s[36:39], s71 offen offset:928 lds
s_mov_b32 exec_hi, -1
v_dot2c_f32_bf16 v213, v2, v212
v_dot2c_f32_bf16 v215, v2, v2
v_dot2c_f32_bf16 v214, v6, v212
v_dot2c_f32_bf16 v216, v6, v6
v_dot2c_f32_bf16 v213, v3, v212
v_dot2c_f32_bf16 v215, v3, v3
v_dot2c_f32_bf16 v214, v7, v212
v_dot2c_f32_bf16 v216, v7, v7
v_xor_b32 v207, v202, v201
v_lshlrev_b32 v207, 4, v207
v_lshl_add_u32 v207, v202, 9, v207
v_accvgpr_write_b32 a176, v207
s_mov_b32 exec_hi, 0x00ffffff
buffer_load_dwordx4 v194, s[36:39], s71 offen offset:1856 lds
s_mov_b32 exec_hi, -1
s_waitcnt lgkmcnt(11)
v_dot2c_f32_bf16 v213, v8, v212
v_dot2c_f32_bf16 v215, v8, v8
s_waitcnt lgkmcnt(10)
v_dot2c_f32_bf16 v214, v12, v212
v_dot2c_f32_bf16 v216, v12, v12
v_dot2c_f32_bf16 v213, v9, v212
v_dot2c_f32_bf16 v215, v9, v9
v_dot2c_f32_bf16 v214, v13, v212
v_dot2c_f32_bf16 v216, v13, v13
v_and_b32 v207, 1, v206
v_lshl_add_u32 v207, v207, 1, v202
v_xor_b32 v207, v207, v203
v_lshlrev_b32 v207, 4, v207
s_mov_b32 exec_hi, 0x00ffffff
buffer_load_dwordx4 v195, s[36:39], s71 offen offset:2784 lds
s_mov_b32 exec_hi, -1
v_dot2c_f32_bf16 v213, v10, v212
v_dot2c_f32_bf16 v215, v10, v10
v_dot2c_f32_bf16 v214, v14, v212
v_dot2c_f32_bf16 v216, v14, v14
v_dot2c_f32_bf16 v213, v11, v212
v_dot2c_f32_bf16 v215, v11, v11
v_dot2c_f32_bf16 v214, v15, v212
v_dot2c_f32_bf16 v216, v15, v15
v_lshl_add_u32 v207, v203, 9, v207
v_add_u32 v207, 98304, v207
v_accvgpr_write_b32 a178, v207
v_lshrrev_b32 v207, 3, v200
s_add_u32 m0, s77, 102016
s_add_u32 s71, s35, 21952
s_mov_b32 exec_hi, 0x00ffffff
buffer_load_dwordx4 v192, s[36:39], s71 offen offset:0 lds
s_mov_b32 exec_hi, -1
s_waitcnt lgkmcnt(9)
v_dot2c_f32_bf16 v213, v16, v212
v_dot2c_f32_bf16 v215, v16, v16
s_waitcnt lgkmcnt(8)
v_dot2c_f32_bf16 v214, v20, v212
v_dot2c_f32_bf16 v216, v20, v20
v_dot2c_f32_bf16 v213, v17, v212
v_dot2c_f32_bf16 v215, v17, v17
v_dot2c_f32_bf16 v214, v21, v212
v_dot2c_f32_bf16 v216, v21, v21
v_lshlrev_b32 v208, 1, v206
v_and_b32 v186, 7, v200
v_xor_b32 v208, v208, v186
v_mul_u32_u24 v207, 896, v207
s_mov_b32 exec_hi, 0x00ffffff
buffer_load_dwordx4 v193, s[36:39], s71 offen offset:928 lds
s_mov_b32 exec_hi, -1
v_dot2c_f32_bf16 v213, v18, v212
v_dot2c_f32_bf16 v215, v18, v18
v_dot2c_f32_bf16 v214, v22, v212
v_dot2c_f32_bf16 v216, v22, v22
v_dot2c_f32_bf16 v213, v19, v212
v_dot2c_f32_bf16 v215, v19, v19
v_dot2c_f32_bf16 v214, v23, v212
v_dot2c_f32_bf16 v216, v23, v23
v_lshl_add_u32 v207, v208, 4, v207
v_accvgpr_write_b32 a184, v207
s_lshl_b32 s87, s69, 1
s_add_u32 s87, s87, s24
s_mov_b32 exec_hi, 0x00ffffff
buffer_load_dwordx4 v194, s[36:39], s71 offen offset:1856 lds
s_mov_b32 exec_hi, -1
s_waitcnt lgkmcnt(7)
v_dot2c_f32_bf16 v213, v24, v212
v_dot2c_f32_bf16 v215, v24, v24
s_waitcnt lgkmcnt(6)
v_dot2c_f32_bf16 v214, v28, v212
v_dot2c_f32_bf16 v216, v28, v28
v_dot2c_f32_bf16 v213, v25, v212
v_dot2c_f32_bf16 v215, v25, v25
v_dot2c_f32_bf16 v214, v29, v212
v_dot2c_f32_bf16 v216, v29, v29
s_lshl_b32 s87, s87, 2
s_add_u32 s87, s87, s68
s_mul_i32 s87, s87, 14336
s_lshl_b32 s88, s25, 7
s_mov_b32 exec_hi, 0x00ffffff
buffer_load_dwordx4 v195, s[36:39], s71 offen offset:2784 lds
s_mov_b32 exec_hi, -1
v_dot2c_f32_bf16 v213, v26, v212
v_dot2c_f32_bf16 v215, v26, v26
v_dot2c_f32_bf16 v214, v30, v212
v_dot2c_f32_bf16 v216, v30, v30
v_dot2c_f32_bf16 v213, v27, v212
v_dot2c_f32_bf16 v215, v27, v27
v_dot2c_f32_bf16 v214, v31, v212
v_dot2c_f32_bf16 v216, v31, v31
s_add_u32 s87, s87, s88
s_add_u32 s87, s87, s26
v_mul_u32_u24 v207, 896, v203
v_lshl_add_u32 v207, v202, 4, v207
s_add_u32 m0, s78, 131072
s_add_u32 s71, s73, 0
buffer_load_dwordx4 v236, s[44:47], s71 offen offset:0 lds
s_waitcnt lgkmcnt(5)
v_dot2c_f32_bf16 v213, v32, v212
v_dot2c_f32_bf16 v215, v32, v32
s_waitcnt lgkmcnt(4)
v_dot2c_f32_bf16 v214, v36, v212
v_dot2c_f32_bf16 v216, v36, v36
v_dot2c_f32_bf16 v213, v33, v212
v_dot2c_f32_bf16 v215, v33, v33
v_dot2c_f32_bf16 v214, v37, v212
v_dot2c_f32_bf16 v216, v37, v37
v_and_b32 v208, 1, v206
v_lshl_add_u32 v207, v208, 5, v207
v_add_u32 v207, s87, v207
v_accvgpr_write_b32 a186, v207
buffer_load_dwordx4 v237, s[44:47], s71 offen offset:1024 lds
v_dot2c_f32_bf16 v213, v34, v212
v_dot2c_f32_bf16 v215, v34, v34
v_dot2c_f32_bf16 v214, v38, v212
v_dot2c_f32_bf16 v216, v38, v38
v_dot2c_f32_bf16 v213, v35, v212
v_dot2c_f32_bf16 v215, v35, v35
v_dot2c_f32_bf16 v214, v39, v212
v_dot2c_f32_bf16 v216, v39, v39
s_mov_b32 s25, s87
s_and_b32 s88, s68, 1
s_lshl_b32 s88, s88, 11
s_lshl_b32 s24, s30, 15
buffer_load_dwordx4 v238, s[44:47], s71 offen offset:2048 lds
s_waitcnt lgkmcnt(3)
v_dot2c_f32_bf16 v213, v40, v212
v_dot2c_f32_bf16 v215, v40, v40
s_waitcnt lgkmcnt(2)
v_dot2c_f32_bf16 v214, v44, v212
v_dot2c_f32_bf16 v216, v44, v44
v_dot2c_f32_bf16 v213, v41, v212
v_dot2c_f32_bf16 v215, v41, v41
v_dot2c_f32_bf16 v214, v45, v212
v_dot2c_f32_bf16 v216, v45, v45
s_add_u32 s24, s24, s88
s_add_u32 s24, s24, 61440
v_and_b32 v207, 1, v200
v_lshlrev_b32 v207, 4, v207
buffer_load_dwordx4 v239, s[44:47], s71 offen offset:3072 lds
v_dot2c_f32_bf16 v213, v42, v212
v_dot2c_f32_bf16 v215, v42, v42
v_dot2c_f32_bf16 v214, v46, v212
v_dot2c_f32_bf16 v216, v46, v46
v_dot2c_f32_bf16 v213, v43, v212
v_dot2c_f32_bf16 v215, v43, v43
v_dot2c_f32_bf16 v214, v47, v212
v_dot2c_f32_bf16 v216, v47, v47
v_mov_b32 v208, 0x3f80
v_lshlrev_b32 v207, v207, v208
v_xor_b32 v208, v206, v204
v_bfe_u32 v209, v200, 1, 1
s_add_u32 m0, s78, 135168
s_nop 0
buffer_load_dwordx4 v240, s[44:47], s71 offen offset:0 lds
s_waitcnt lgkmcnt(1)
v_dot2c_f32_bf16 v213, v48, v212
v_dot2c_f32_bf16 v215, v48, v48
s_waitcnt lgkmcnt(0)
v_dot2c_f32_bf16 v214, v52, v212
v_dot2c_f32_bf16 v216, v52, v52
v_dot2c_f32_bf16 v213, v49, v212
v_dot2c_f32_bf16 v215, v49, v49
v_dot2c_f32_bf16 v214, v53, v212
v_dot2c_f32_bf16 v216, v53, v53
v_lshl_add_u32 v208, v208, 1, v209
v_xor_b32 v186, 0, v208
v_min_u32 v186, 1, v186
v_subrev_u32 v186, 1, v186
buffer_load_dwordx4 v241, s[44:47], s71 offen offset:1024 lds
v_dot2c_f32_bf16 v213, v50, v212
v_dot2c_f32_bf16 v215, v50, v50
v_dot2c_f32_bf16 v214, v54, v212
v_dot2c_f32_bf16 v216, v54, v54
v_dot2c_f32_bf16 v213, v51, v212
v_dot2c_f32_bf16 v215, v51, v51
v_dot2c_f32_bf16 v214, v55, v212
v_dot2c_f32_bf16 v216, v55, v55
v_and_b32 v186, v186, v207
v_accvgpr_write_b32 a170, v186
v_xor_b32 v186, 1, v208
v_min_u32 v186, 1, v186
buffer_load_dwordx4 v242, s[44:47], s71 offen offset:2048 lds
v_subrev_u32 v186, 1, v186
v_and_b32 v186, v186, v207
v_accvgpr_write_b32 a171, v186
v_accvgpr_write_b32 a168, 0
s_mov_b64 exec, 0xffff
buffer_load_dword a174, v191, s[40:43], 0 offen offset:0
s_mov_b64 exec, -1
v_accvgpr_write_b32 a169, 0
v_accvgpr_write_b32 a172, 0
v_accvgpr_write_b32 a173, 0
s_waitcnt vmcnt(8)
ds_read2_b64 v[56:59], v217 offset0:0 offset1:4
ds_read2_b64 v[60:63], v218 offset0:0 offset1:4
ds_read2_b64 v[64:67], v217 offset0:8 offset1:12
ds_read2_b64 v[68:71], v218 offset0:8 offset1:12
ds_read2_b64 v[72:75], v217 offset0:16 offset1:20
ds_read2_b64 v[76:79], v218 offset0:16 offset1:20
ds_read2_b64 v[80:83], v217 offset0:24 offset1:28
ds_read2_b64 v[84:87], v218 offset0:24 offset1:28
ds_read2_b64 v[88:91], v217 offset0:32 offset1:36
ds_read2_b64 v[92:95], v218 offset0:32 offset1:36
ds_read2_b64 v[96:99], v217 offset0:40 offset1:44
ds_read2_b64 v[100:103], v218 offset0:40 offset1:44
ds_read2_b64 v[104:107], v217 offset0:48 offset1:52
ds_read2_b64 v[108:111], v218 offset0:48 offset1:52
s_waitcnt lgkmcnt(13)
v_dot2c_f32_bf16 v213, v56, v212
v_dot2c_f32_bf16 v215, v56, v56
s_waitcnt lgkmcnt(12)
v_dot2c_f32_bf16 v214, v60, v212
v_dot2c_f32_bf16 v216, v60, v60
v_dot2c_f32_bf16 v213, v57, v212
v_dot2c_f32_bf16 v215, v57, v57
v_dot2c_f32_bf16 v214, v61, v212
v_dot2c_f32_bf16 v216, v61, v61
s_add_u32 m0, s77, 0
s_add_u32 s71, s72, 0
buffer_load_dwordx4 v232, s[40:43], s71 offen offset:0 lds
v_dot2c_f32_bf16 v213, v58, v212
v_dot2c_f32_bf16 v215, v58, v58
v_dot2c_f32_bf16 v214, v62, v212
v_dot2c_f32_bf16 v216, v62, v62
v_dot2c_f32_bf16 v213, v59, v212
v_dot2c_f32_bf16 v215, v59, v59
v_dot2c_f32_bf16 v214, v63, v212
v_dot2c_f32_bf16 v216, v63, v63
buffer_load_dwordx4 v233, s[40:43], s71 offen offset:1024 lds
s_waitcnt lgkmcnt(11)
v_dot2c_f32_bf16 v213, v64, v212
v_dot2c_f32_bf16 v215, v64, v64
s_waitcnt lgkmcnt(10)
v_dot2c_f32_bf16 v214, v68, v212
v_dot2c_f32_bf16 v216, v68, v68
v_dot2c_f32_bf16 v213, v65, v212
v_dot2c_f32_bf16 v215, v65, v65
v_dot2c_f32_bf16 v214, v69, v212
v_dot2c_f32_bf16 v216, v69, v69
buffer_load_dwordx4 v234, s[40:43], s71 offen offset:2048 lds
v_dot2c_f32_bf16 v213, v66, v212
v_dot2c_f32_bf16 v215, v66, v66
v_dot2c_f32_bf16 v214, v70, v212
v_dot2c_f32_bf16 v216, v70, v70
v_dot2c_f32_bf16 v213, v67, v212
v_dot2c_f32_bf16 v215, v67, v67
v_dot2c_f32_bf16 v214, v71, v212
v_dot2c_f32_bf16 v216, v71, v71
buffer_load_dwordx4 v235, s[40:43], s71 offen offset:3072 lds
s_waitcnt lgkmcnt(9)
v_dot2c_f32_bf16 v213, v72, v212
v_dot2c_f32_bf16 v215, v72, v72
s_waitcnt lgkmcnt(8)
v_dot2c_f32_bf16 v214, v76, v212
v_dot2c_f32_bf16 v216, v76, v76
v_dot2c_f32_bf16 v213, v73, v212
v_dot2c_f32_bf16 v215, v73, v73
v_dot2c_f32_bf16 v214, v77, v212
v_dot2c_f32_bf16 v216, v77, v77
s_add_u32 m0, s77, 4096
s_add_u32 s71, s72, 65536
buffer_load_dwordx4 v232, s[40:43], s71 offen offset:0 lds
v_dot2c_f32_bf16 v213, v74, v212
v_dot2c_f32_bf16 v215, v74, v74
v_dot2c_f32_bf16 v214, v78, v212
v_dot2c_f32_bf16 v216, v78, v78
v_dot2c_f32_bf16 v213, v75, v212
v_dot2c_f32_bf16 v215, v75, v75
v_dot2c_f32_bf16 v214, v79, v212
v_dot2c_f32_bf16 v216, v79, v79
buffer_load_dwordx4 v233, s[40:43], s71 offen offset:1024 lds
s_waitcnt lgkmcnt(7)
v_dot2c_f32_bf16 v213, v80, v212
v_dot2c_f32_bf16 v215, v80, v80
s_waitcnt lgkmcnt(6)
v_dot2c_f32_bf16 v214, v84, v212
v_dot2c_f32_bf16 v216, v84, v84
v_dot2c_f32_bf16 v213, v81, v212
v_dot2c_f32_bf16 v215, v81, v81
v_dot2c_f32_bf16 v214, v85, v212
v_dot2c_f32_bf16 v216, v85, v85
buffer_load_dwordx4 v234, s[40:43], s71 offen offset:2048 lds
v_dot2c_f32_bf16 v213, v82, v212
v_dot2c_f32_bf16 v215, v82, v82
v_dot2c_f32_bf16 v214, v86, v212
v_dot2c_f32_bf16 v216, v86, v86
v_dot2c_f32_bf16 v213, v83, v212
v_dot2c_f32_bf16 v215, v83, v83
v_dot2c_f32_bf16 v214, v87, v212
v_dot2c_f32_bf16 v216, v87, v87
buffer_load_dwordx4 v235, s[40:43], s71 offen offset:3072 lds
s_waitcnt lgkmcnt(5)
v_dot2c_f32_bf16 v213, v88, v212
v_dot2c_f32_bf16 v215, v88, v88
s_waitcnt lgkmcnt(4)
v_dot2c_f32_bf16 v214, v92, v212
v_dot2c_f32_bf16 v216, v92, v92
v_dot2c_f32_bf16 v213, v89, v212
v_dot2c_f32_bf16 v215, v89, v89
v_dot2c_f32_bf16 v214, v93, v212
v_dot2c_f32_bf16 v216, v93, v93
v_dot2c_f32_bf16 v213, v90, v212
v_dot2c_f32_bf16 v215, v90, v90
v_dot2c_f32_bf16 v214, v94, v212
v_dot2c_f32_bf16 v216, v94, v94
v_dot2c_f32_bf16 v213, v91, v212
v_dot2c_f32_bf16 v215, v91, v91
v_dot2c_f32_bf16 v214, v95, v212
v_dot2c_f32_bf16 v216, v95, v95
s_waitcnt lgkmcnt(3)
v_dot2c_f32_bf16 v213, v96, v212
v_dot2c_f32_bf16 v215, v96, v96
s_waitcnt lgkmcnt(2)
v_dot2c_f32_bf16 v214, v100, v212
v_dot2c_f32_bf16 v216, v100, v100
v_dot2c_f32_bf16 v213, v97, v212
v_dot2c_f32_bf16 v215, v97, v97
v_dot2c_f32_bf16 v214, v101, v212
v_dot2c_f32_bf16 v216, v101, v101
v_dot2c_f32_bf16 v213, v98, v212
v_dot2c_f32_bf16 v215, v98, v98
v_dot2c_f32_bf16 v214, v102, v212
v_dot2c_f32_bf16 v216, v102, v102
v_dot2c_f32_bf16 v213, v99, v212
v_dot2c_f32_bf16 v215, v99, v99
v_dot2c_f32_bf16 v214, v103, v212
v_dot2c_f32_bf16 v216, v103, v103
s_waitcnt lgkmcnt(1)
v_dot2c_f32_bf16 v213, v104, v212
v_dot2c_f32_bf16 v215, v104, v104
s_waitcnt lgkmcnt(0)
v_dot2c_f32_bf16 v214, v108, v212
v_dot2c_f32_bf16 v216, v108, v108
v_dot2c_f32_bf16 v213, v105, v212
v_dot2c_f32_bf16 v215, v105, v105
v_dot2c_f32_bf16 v214, v109, v212
v_dot2c_f32_bf16 v216, v109, v109
v_dot2c_f32_bf16 v213, v106, v212
v_dot2c_f32_bf16 v215, v106, v106
v_dot2c_f32_bf16 v214, v110, v212
v_dot2c_f32_bf16 v216, v110, v110
v_dot2c_f32_bf16 v213, v107, v212
v_dot2c_f32_bf16 v215, v107, v107
v_dot2c_f32_bf16 v214, v111, v212
v_dot2c_f32_bf16 v216, v111, v111
s_waitcnt vmcnt(9)
s_barrier
ds_read_b64_tr_b16 a[128:129], v243 offset:0
ds_read_b64_tr_b16 a[130:131], v243 offset:3584
ds_read_b64_tr_b16 a[132:133], v243 offset:14464
ds_read_b64_tr_b16 a[134:135], v243 offset:18048
ds_read_b64_tr_b16 a[136:137], v243 offset:32
ds_read_b64_tr_b16 a[138:139], v243 offset:3616
ds_read_b64_tr_b16 a[140:141], v243 offset:14496
ds_read_b64_tr_b16 a[142:143], v243 offset:18080
ds_read_b64_tr_b16 a[144:145], v243 offset:64
ds_read_b64_tr_b16 a[146:147], v243 offset:3648
ds_read_b64_tr_b16 a[148:149], v243 offset:14528
ds_read_b64_tr_b16 a[150:151], v243 offset:18112
ds_read_b64_tr_b16 a[152:153], v243 offset:96
ds_read_b64_tr_b16 a[154:155], v243 offset:3680
buffer_load_dwordx2 a[192:193], v184, s[64:67], s82 offen offset:0 sc1
v_mov_b32 v207, v213
v_mov_b32 v208, v214
v_mov_b32 v209, v215
v_mov_b32 v186, v216
v_permlane16_swap_b32 v213, v207
v_permlane16_swap_b32 v214, v208
v_permlane16_swap_b32 v215, v209
v_permlane16_swap_b32 v216, v186
v_add_f32 v213, v213, v207
v_add_f32 v214, v214, v208
v_add_f32 v215, v215, v209
v_add_f32 v216, v216, v186
v_mov_b32 v207, v213
v_mov_b32 v208, v214
v_mov_b32 v209, v215
v_mov_b32 v186, v216
v_permlane32_swap_b32 v213, v207
v_permlane32_swap_b32 v214, v208
v_permlane32_swap_b32 v215, v209
v_permlane32_swap_b32 v216, v186
v_add_f32 v213, v213, v207
v_add_f32 v214, v214, v208
v_add_f32 v215, v215, v209
v_add_f32 v216, v216, v186
v_mul_f32 v213, 0x3b124925, v213
v_mul_f32 v214, 0x3b124925, v214
v_mul_f32 v215, 0x3b124925, v215
v_mul_f32 v216, 0x3b124925, v216
v_fma_f32 v215, -v213, v213, v215
v_fma_f32 v216, -v214, v214, v216
v_max_f32 v215, 0, v215
v_max_f32 v216, 0, v216
v_add_f32 v215, 0x3727c5ac, v215
v_add_f32 v216, 0x3727c5ac, v216
v_rsq_f32 v215, v215
v_rsq_f32 v216, v216
v_mul_f32 v244, 0x3db8aa3b, v215
v_mul_f32 v246, 0x3db8aa3b, v216
v_mul_f32 v245, -v213, v244
v_mul_f32 v247, -v214, v246
v_lshlrev_b32 v248, 16, v0
v_and_b32 v249, s86, v0
v_fma_f32 v248, v248, v244, v245
v_fma_f32 v249, v249, v244, v245
v_cvt_pk_bf16_f32 v0, v248, v249
v_lshlrev_b32 v250, 16, v1
v_and_b32 v251, s86, v1
v_fma_f32 v250, v250, v244, v245
v_fma_f32 v251, v251, v244, v245
v_cvt_pk_bf16_f32 v1, v250, v251
v_lshlrev_b32 v248, 16, v2
v_and_b32 v249, s86, v2
v_fma_f32 v248, v248, v244, v245
v_fma_f32 v249, v249, v244, v245
v_cvt_pk_bf16_f32 v2, v248, v249
v_lshlrev_b32 v250, 16, v3
v_and_b32 v251, s86, v3
v_fma_f32 v250, v250, v244, v245
v_fma_f32 v251, v251, v244, v245
v_cvt_pk_bf16_f32 v3, v250, v251
v_lshlrev_b32 v248, 16, v4
v_and_b32 v249, s86, v4
v_fma_f32 v248, v248, v246, v247
v_fma_f32 v249, v249, v246, v247
v_cvt_pk_bf16_f32 v4, v248, v249
v_lshlrev_b32 v250, 16, v5
v_and_b32 v251, s86, v5
v_fma_f32 v250, v250, v246, v247
v_fma_f32 v251, v251, v246, v247
v_cvt_pk_bf16_f32 v5, v250, v251
v_lshlrev_b32 v248, 16, v6
v_and_b32 v249, s86, v6
v_fma_f32 v248, v248, v246, v247
v_fma_f32 v249, v249, v246, v247
v_cvt_pk_bf16_f32 v6, v248, v249
v_lshlrev_b32 v250, 16, v7
v_and_b32 v251, s86, v7
v_fma_f32 v250, v250, v246, v247
v_fma_f32 v251, v251, v246, v247
v_cvt_pk_bf16_f32 v7, v250, v251
s_waitcnt lgkmcnt(6)
v_mfma_f32_16x16x32_bf16 v[112:115], a[128:131], v[0:3], 0
ds_read_b64_tr_b16 a[156:157], v243 offset:14560
v_lshlrev_b32 v248, 16, v8
v_mfma_f32_16x16x32_bf16 v[116:119], a[128:131], v[4:7], 0
ds_read_b64_tr_b16 a[158:159], v243 offset:18144
v_and_b32 v249, s86, v8
v_mfma_f32_16x16x32_bf16 v[120:123], a[132:135], v[0:3], 0
ds_read_b64_tr_b16 a[128:129], v243 offset:128
v_fma_f32 v248, v248, v244, v245
v_mfma_f32_16x16x32_bf16 v[124:127], a[132:135], v[4:7], 0
s_add_u32 s71, s72, 524288
ds_read_b64_tr_b16 a[130:131], v243 offset:3712
v_fma_f32 v249, v249, v244, v245
v_mfma_f32_16x16x32_bf16 v[128:131], a[136:139], v[0:3], 0
s_add_u32 m0, s77, 32768
ds_read_b64_tr_b16 a[132:133], v243 offset:14592
v_cvt_pk_bf16_f32 v8, v248, v249
v_mfma_f32_16x16x32_bf16 v[132:135], a[136:139], v[4:7], 0
ds_read_b64_tr_b16 a[134:135], v243 offset:18176
buffer_load_dwordx4 v232, s[40:43], s71 offen offset:0 lds
v_mfma_f32_16x16x32_bf16 v[136:139], a[140:143], v[0:3], 0
ds_read_b64_tr_b16 a[136:137], v243 offset:160
v_lshlrev_b32 v250, 16, v9
v_mfma_f32_16x16x32_bf16 v[140:143], a[140:143], v[4:7], 0
s_waitcnt lgkmcnt(5)
ds_read_b64_tr_b16 a[138:139], v243 offset:3744
v_and_b32 v251, s86, v9
v_mfma_f32_16x16x32_bf16 v[144:147], a[144:147], v[0:3], 0
ds_read_b64_tr_b16 a[140:141], v243 offset:14624
v_fma_f32 v250, v250, v244, v245
v_mfma_f32_16x16x32_bf16 v[148:151], a[144:147], v[4:7], 0
ds_read_b64_tr_b16 a[142:143], v243 offset:18208
buffer_load_dwordx4 v233, s[40:43], s71 offen offset:1024 lds
v_mfma_f32_16x16x32_bf16 v[152:155], a[148:151], v[0:3], 0
ds_read_b64_tr_b16 a[144:145], v243 offset:192
v_fma_f32 v251, v251, v244, v245
v_mfma_f32_16x16x32_bf16 v[156:159], a[148:151], v[4:7], 0
ds_read_b64_tr_b16 a[146:147], v243 offset:3776
v_cvt_pk_bf16_f32 v9, v250, v251
v_mfma_f32_16x16x32_bf16 v[160:163], a[152:155], v[0:3], 0
ds_read_b64_tr_b16 a[148:149], v243 offset:14656
v_lshlrev_b32 v248, 16, v10
v_mfma_f32_16x16x32_bf16 v[164:167], a[152:155], v[4:7], 0
ds_read_b64_tr_b16 a[150:151], v243 offset:18240
buffer_load_dwordx4 v234, s[40:43], s71 offen offset:2048 lds
v_mfma_f32_16x16x32_bf16 v[168:171], a[156:159], v[0:3], 0
ds_read_b64_tr_b16 a[152:153], v243 offset:7168
v_and_b32 v249, s86, v10
v_mfma_f32_16x16x32_bf16 v[172:175], a[156:159], v[4:7], 0
s_waitcnt lgkmcnt(5)
ds_read_b64_tr_b16 a[154:155], v243 offset:10752
v_fma_f32 v248, v248, v244, v245
v_mfma_f32_16x16x32_bf16 v[176:179], a[128:131], v[0:3], 0
ds_read_b64_tr_b16 a[156:157], v243 offset:21632
v_fma_f32 v249, v249, v244, v245
v_mfma_f32_16x16x32_bf16 v[180:183], a[128:131], v[4:7], 0
ds_read_b64_tr_b16 a[158:159], v243 offset:25216
buffer_load_dwordx4 v235, s[40:43], s71 offen offset:3072 lds
v_mfma_f32_16x16x32_bf16 v[184:187], a[132:135], v[0:3], 0
ds_read_b64_tr_b16 a[128:129], v243 offset:7200
v_cvt_pk_bf16_f32 v10, v248, v249
v_mfma_f32_16x16x32_bf16 v[188:191], a[132:135], v[4:7], 0
s_add_u32 s71, s72, 589824
ds_read_b64_tr_b16 a[130:131], v243 offset:10784
v_lshlrev_b32 v250, 16, v11
v_mfma_f32_16x16x32_bf16 v[192:195], a[136:139], v[0:3], 0
s_add_u32 m0, s77, 36864
ds_read_b64_tr_b16 a[132:133], v243 offset:21664
v_and_b32 v251, s86, v11
v_mfma_f32_16x16x32_bf16 v[196:199], a[136:139], v[4:7], 0
ds_read_b64_tr_b16 a[134:135], v243 offset:25248
buffer_load_dwordx4 v232, s[40:43], s71 offen offset:0 lds
v_mfma_f32_16x16x32_bf16 v[200:203], a[140:143], v[0:3], 0
ds_read_b64_tr_b16 a[136:137], v243 offset:7232
v_fma_f32 v250, v250, v244, v245
v_mfma_f32_16x16x32_bf16 v[204:207], a[140:143], v[4:7], 0
s_waitcnt lgkmcnt(5)
ds_read_b64_tr_b16 a[138:139], v243 offset:10816
v_fma_f32 v251, v251, v244, v245
v_mfma_f32_16x16x32_bf16 v[208:211], a[144:147], v[0:3], 0
ds_read_b64_tr_b16 a[140:141], v243 offset:21696
v_cvt_pk_bf16_f32 v11, v250, v251
v_mfma_f32_16x16x32_bf16 v[212:215], a[144:147], v[4:7], 0
ds_read_b64_tr_b16 a[142:143], v243 offset:25280
buffer_load_dwordx4 v233, s[40:43], s71 offen offset:1024 lds
v_mfma_f32_16x16x32_bf16 v[216:219], a[148:151], v[0:3], 0
ds_read_b64_tr_b16 a[144:145], v243 offset:7264
v_lshlrev_b32 v248, 16, v12
v_and_b32 v249, s86, v12
v_fma_f32 v248, v248, v246, v247
v_fma_f32 v249, v249, v246, v247
v_cvt_pk_bf16_f32 v12, v248, v249
v_lshlrev_b32 v250, 16, v13
v_and_b32 v251, s86, v13
v_fma_f32 v250, v250, v246, v247
v_fma_f32 v251, v251, v246, v247
v_cvt_pk_bf16_f32 v13, v250, v251
v_lshlrev_b32 v248, 16, v14
v_and_b32 v249, s86, v14
v_fma_f32 v248, v248, v246, v247
v_fma_f32 v249, v249, v246, v247
v_cvt_pk_bf16_f32 v14, v248, v249
v_lshlrev_b32 v250, 16, v15
v_and_b32 v251, s86, v15
v_fma_f32 v250, v250, v246, v247
v_fma_f32 v251, v251, v246, v247
v_cvt_pk_bf16_f32 v15, v250, v251
v_mfma_f32_16x16x32_bf16 v[220:223], a[148:151], v[4:7], 0
ds_read_b64_tr_b16 a[146:147], v243 offset:10848
v_lshlrev_b32 v248, 16, v16
v_mfma_f32_16x16x32_bf16 v[112:115], a[152:155], v[8:11], v[112:115]
ds_read_b64_tr_b16 a[148:149], v243 offset:21728
v_and_b32 v249, s86, v16
v_mfma_f32_16x16x32_bf16 v[116:119], a[152:155], v[12:15], v[116:119]
ds_read_b64_tr_b16 a[150:151], v243 offset:25312
buffer_load_dwordx4 v234, s[40:43], s71 offen offset:2048 lds
v_mfma_f32_16x16x32_bf16 v[120:123], a[156:159], v[8:11], v[120:123]
ds_read_b64_tr_b16 a[152:153], v243 offset:7296
v_fma_f32 v248, v248, v244, v245
v_mfma_f32_16x16x32_bf16 v[124:127], a[156:159], v[12:15], v[124:127]
s_waitcnt lgkmcnt(5)
ds_read_b64_tr_b16 a[154:155], v243 offset:10880
v_fma_f32 v249, v249, v244, v245
v_mfma_f32_16x16x32_bf16 v[128:131], a[128:131], v[8:11], v[128:131]
ds_read_b64_tr_b16 a[156:157], v243 offset:21760
v_cvt_pk_bf16_f32 v16, v248, v249
v_mfma_f32_16x16x32_bf16 v[132:135], a[128:131], v[12:15], v[132:135]
ds_read_b64_tr_b16 a[158:159], v243 offset:25344
buffer_load_dwordx4 v235, s[40:43], s71 offen offset:3072 lds
v_mfma_f32_16x16x32_bf16 v[136:139], a[132:135], v[8:11], v[136:139]
ds_read_b64_tr_b16 a[128:129], v243 offset:7328
v_lshlrev_b32 v250, 16, v17
v_mfma_f32_16x16x32_bf16 v[140:143], a[132:135], v[12:15], v[140:143]
ds_read_b64_tr_b16 a[130:131], v243 offset:10912
v_and_b32 v251, s86, v17
v_mfma_f32_16x16x32_bf16 v[144:147], a[136:139], v[8:11], v[144:147]
ds_read_b64_tr_b16 a[132:133], v243 offset:21792
v_fma_f32 v250, v250, v244, v245
v_mfma_f32_16x16x32_bf16 v[148:151], a[136:139], v[12:15], v[148:151]
s_add_u32 s71, s72, 1048576
ds_read_b64_tr_b16 a[134:135], v243 offset:25376
v_fma_f32 v251, v251, v244, v245
v_mfma_f32_16x16x32_bf16 v[152:155], a[140:143], v[8:11], v[152:155]
s_add_u32 m0, s77, 65536
ds_read_b64_tr_b16 a[136:137], v243 offset:7360
v_cvt_pk_bf16_f32 v17, v250, v251
v_mfma_f32_16x16x32_bf16 v[156:159], a[140:143], v[12:15], v[156:159]
s_waitcnt lgkmcnt(5)
ds_read_b64_tr_b16 a[138:139], v243 offset:10944
buffer_load_dwordx4 v232, s[40:43], s71 offen offset:0 lds
v_mfma_f32_16x16x32_bf16 v[160:163], a[144:147], v[8:11], v[160:163]
ds_read_b64_tr_b16 a[140:141], v243 offset:21824
v_lshlrev_b32 v248, 16, v18
v_mfma_f32_16x16x32_bf16 v[164:167], a[144:147], v[12:15], v[164:167]
s_waitcnt vmcnt(10)
ds_read_b64_tr_b16 a[142:143], v243 offset:25408
v_and_b32 v249, s86, v18
v_mfma_f32_16x16x32_bf16 v[168:171], a[148:151], v[8:11], v[168:171]
s_barrier
ds_read_b64_tr_b16 a[144:145], v224 offset:0
v_fma_f32 v248, v248, v244, v245
v_mfma_f32_16x16x32_bf16 v[172:175], a[148:151], v[12:15], v[172:175]
ds_read_b64_tr_b16 a[146:147], v224 offset:8192
buffer_load_dwordx4 v233, s[40:43], s71 offen offset:1024 lds
v_mfma_f32_16x16x32_bf16 v[176:179], a[152:155], v[8:11], v[176:179]
ds_read_b64_tr_b16 a[148:149], v225 offset:0
v_fma_f32 v249, v249, v244, v245
v_mfma_f32_16x16x32_bf16 v[180:183], a[152:155], v[12:15], v[180:183]
ds_read_b64_tr_b16 a[150:151], v225 offset:8192
v_cvt_pk_bf16_f32 v18, v248, v249
v_mfma_f32_16x16x32_bf16 v[184:187], a[156:159], v[8:11], v[184:187]
ds_read_b64_tr_b16 a[152:153], v226 offset:0
v_lshlrev_b32 v250, 16, v19
v_mfma_f32_16x16x32_bf16 v[188:191], a[156:159], v[12:15], v[188:191]
s_waitcnt lgkmcnt(5)
ds_read_b64_tr_b16 a[154:155], v226 offset:8192
buffer_load_dwordx4 v234, s[40:43], s71 offen offset:2048 lds
v_mfma_f32_16x16x32_bf16 v[192:195], a[128:131], v[8:11], v[192:195]
ds_read_b64_tr_b16 a[156:157], v227 offset:0
v_and_b32 v251, s86, v19
v_mfma_f32_16x16x32_bf16 v[196:199], a[128:131], v[12:15], v[196:199]
ds_read_b64_tr_b16 a[158:159], v227 offset:8192
v_fma_f32 v250, v250, v244, v245
v_mfma_f32_16x16x32_bf16 v[200:203], a[132:135], v[8:11], v[200:203]
ds_read_b64_tr_b16 a[128:129], v228 offset:0
v_fma_f32 v251, v251, v244, v245
v_mfma_f32_16x16x32_bf16 v[204:207], a[132:135], v[12:15], v[204:207]
ds_read_b64_tr_b16 a[130:131], v228 offset:8192
buffer_load_dwordx4 v235, s[40:43], s71 offen offset:3072 lds
v_mfma_f32_16x16x32_bf16 v[208:211], a[136:139], v[8:11], v[208:211]
ds_read_b64_tr_b16 a[132:133], v229 offset:0
v_cvt_pk_bf16_f32 v19, v250, v251
v_mfma_f32_16x16x32_bf16 v[212:215], a[136:139], v[12:15], v[212:215]
s_add_u32 s71, s72, 1114112
ds_read_b64_tr_b16 a[134:135], v229 offset:8192
v_lshlrev_b32 v248, 16, v20
v_mfma_f32_16x16x32_bf16 v[216:219], a[140:143], v[8:11], v[216:219]
s_add_u32 m0, s77, 69632
ds_read_b64_tr_b16 a[136:137], v230 offset:0
v_and_b32 v249, s86, v20
v_mfma_f32_16x16x32_bf16 v[220:223], a[140:143], v[12:15], v[220:223]
s_waitcnt lgkmcnt(5)
ds_read_b64_tr_b16 a[138:139], v230 offset:8192
buffer_load_dwordx4 v232, s[40:43], s71 offen offset:0 lds
v_mfma_f32_16x16x32_bf16 a[0:3], a[144:147], v[0:3], 0
ds_read_b64_tr_b16 a[140:141], v231 offset:0
v_fma_f32 v248, v248, v246, v247
v_mfma_f32_16x16x32_bf16 a[4:7], a[144:147], v[4:7], 0
ds_read_b64_tr_b16 a[142:143], v231 offset:8192
v_fma_f32 v249, v249, v246, v247
v_mfma_f32_16x16x32_bf16 a[8:11], a[148:151], v[0:3], 0
ds_read_b64_tr_b16 a[144:145], v224 offset:256
v_cvt_pk_bf16_f32 v20, v248, v249
v_mfma_f32_16x16x32_bf16 a[12:15], a[148:151], v[4:7], 0
ds_read_b64_tr_b16 a[146:147], v224 offset:8448
buffer_load_dwordx4 v233, s[40:43], s71 offen offset:1024 lds
v_mfma_f32_16x16x32_bf16 a[16:19], a[152:155], v[0:3], 0
ds_read_b64_tr_b16 a[148:149], v225 offset:256
v_lshlrev_b32 v250, 16, v21
v_mfma_f32_16x16x32_bf16 a[20:23], a[152:155], v[4:7], 0
ds_read_b64_tr_b16 a[150:151], v225 offset:8448
v_and_b32 v251, s86, v21
v_mfma_f32_16x16x32_bf16 a[24:27], a[156:159], v[0:3], 0
ds_read_b64_tr_b16 a[152:153], v226 offset:256
v_fma_f32 v250, v250, v246, v247
v_mfma_f32_16x16x32_bf16 a[28:31], a[156:159], v[4:7], 0
s_waitcnt lgkmcnt(5)
ds_read_b64_tr_b16 a[154:155], v226 offset:8448
buffer_load_dwordx4 v234, s[40:43], s71 offen offset:2048 lds
v_mfma_f32_16x16x32_bf16 a[32:35], a[128:131], v[0:3], 0
ds_read_b64_tr_b16 a[156:157], v227 offset:256
v_fma_f32 v251, v251, v246, v247
v_mfma_f32_16x16x32_bf16 a[36:39], a[128:131], v[4:7], 0
ds_read_b64_tr_b16 a[158:159], v227 offset:8448
v_cvt_pk_bf16_f32 v21, v250, v251
v_mfma_f32_16x16x32_bf16 a[40:43], a[132:135], v[0:3], 0
ds_read_b64_tr_b16 a[128:129], v228 offset:256
v_lshlrev_b32 v248, 16, v22
v_mfma_f32_16x16x32_bf16 a[44:47], a[132:135], v[4:7], 0
ds_read_b64_tr_b16 a[130:131], v228 offset:8448
buffer_load_dwordx4 v235, s[40:43], s71 offen offset:3072 lds
v_mfma_f32_16x16x32_bf16 a[48:51], a[136:139], v[0:3], 0
ds_read_b64_tr_b16 a[132:133], v229 offset:256
v_and_b32 v249, s86, v22
v_mfma_f32_16x16x32_bf16 a[52:55], a[136:139], v[4:7], 0
s_add_u32 s71, s72, 1572864
ds_read_b64_tr_b16 a[134:135], v229 offset:8448
v_fma_f32 v248, v248, v246, v247
v_mfma_f32_16x16x32_bf16 a[56:59], a[140:143], v[0:3], 0
s_waitcnt lgkmcnt(4)
ds_read_b64_tr_b16 a[136:137], v230 offset:256
v_accvgpr_read_b32 v254, a208
v_mfma_f32_16x16x32_bf16 a[60:63], a[140:143], v[4:7], 0
s_mov_b64 exec, 0xfff
buffer_load_dword a174, v254, s[40:43], 0 offen offset:0
s_mov_b64 exec, -1
ds_read_b64_tr_b16 a[138:139], v230 offset:8448
v_mfma_f32_16x16x32_bf16 a[64:67], a[144:147], v[0:3], 0
s_add_u32 m0, s77, 98304
ds_read_b64_tr_b16 a[140:141], v231 offset:256
v_fma_f32 v249, v249, v246, v247
v_mfma_f32_16x16x32_bf16 a[68:71], a[144:147], v[4:7], 0
ds_read_b64_tr_b16 a[142:143], v231 offset:8448
buffer_load_dwordx4 v232, s[40:43], s71 offen offset:0 lds
v_mfma_f32_16x16x32_bf16 a[72:75], a[148:151], v[0:3], 0
ds_read_b64_tr_b16 a[144:145], v224 offset:16384
v_cvt_pk_bf16_f32 v22, v248, v249
v_mfma_f32_16x16x32_bf16 a[76:79], a[148:151], v[4:7], 0
ds_read_b64_tr_b16 a[146:147], v224 offset:24576
v_lshlrev_b32 v250, 16, v23
v_mfma_f32_16x16x32_bf16 a[80:83], a[152:155], v[0:3], 0
ds_read_b64_tr_b16 a[148:149], v225 offset:16384
v_and_b32 v251, s86, v23
v_mfma_f32_16x16x32_bf16 a[84:87], a[152:155], v[4:7], 0
ds_read_b64_tr_b16 a[150:151], v225 offset:24576
buffer_load_dwordx4 v233, s[40:43], s71 offen offset:1024 lds
v_mfma_f32_16x16x32_bf16 a[88:91], a[156:159], v[0:3], 0
ds_read_b64_tr_b16 a[152:153], v226 offset:16384
v_fma_f32 v250, v250, v246, v247
v_mfma_f32_16x16x32_bf16 a[92:95], a[156:159], v[4:7], 0
s_waitcnt lgkmcnt(5)
ds_read_b64_tr_b16 a[154:155], v226 offset:24576
v_fma_f32 v251, v251, v246, v247
v_mfma_f32_16x16x32_bf16 a[96:99], a[128:131], v[0:3], 0
ds_read_b64_tr_b16 a[156:157], v227 offset:16384
v_cvt_pk_bf16_f32 v23, v250, v251
v_mfma_f32_16x16x32_bf16 a[100:103], a[128:131], v[4:7], 0
ds_read_b64_tr_b16 a[158:159], v227 offset:24576
buffer_load_dwordx4 v234, s[40:43], s71 offen offset:2048 lds
v_mfma_f32_16x16x32_bf16 a[104:107], a[132:135], v[0:3], 0
ds_read_b64_tr_b16 a[128:129], v228 offset:16384
v_lshlrev_b32 v248, 16, v24
v_mfma_f32_16x16x32_bf16 a[108:111], a[132:135], v[4:7], 0
ds_read_b64_tr_b16 a[130:131], v228 offset:24576
v_and_b32 v249, s86, v24
v_mfma_f32_16x16x32_bf16 a[112:115], a[136:139], v[0:3], 0
ds_read_b64_tr_b16 a[132:133], v229 offset:16384
v_fma_f32 v248, v248, v244, v245
v_mfma_f32_16x16x32_bf16 a[116:119], a[136:139], v[4:7], 0
ds_read_b64_tr_b16 a[134:135], v229 offset:24576
buffer_load_dwordx4 v235, s[40:43], s71 offen offset:3072 lds
v_mfma_f32_16x16x32_bf16 a[120:123], a[140:143], v[0:3], 0
s_add_u32 s71, s72, 1638400
ds_read_b64_tr_b16 a[136:137], v230 offset:16384
v_fma_f32 v249, v249, v244, v245
v_mfma_f32_16x16x32_bf16 a[124:127], a[140:143], v[4:7], 0
s_waitcnt lgkmcnt(5)
ds_read_b64_tr_b16 a[138:139], v230 offset:24576
v_cvt_pk_bf16_f32 v24, v248, v249
v_mfma_f32_16x16x32_bf16 a[0:3], a[144:147], v[8:11], a[0:3]
s_add_u32 m0, s77, 102400
ds_read_b64_tr_b16 a[140:141], v231 offset:16384
v_lshlrev_b32 v250, 16, v25
v_mfma_f32_16x16x32_bf16 a[4:7], a[144:147], v[12:15], a[4:7]
ds_read_b64_tr_b16 a[142:143], v231 offset:24576
buffer_load_dwordx4 v232, s[40:43], s71 offen offset:0 lds
v_mfma_f32_16x16x32_bf16 a[8:11], a[148:151], v[8:11], a[8:11]
ds_read_b64_tr_b16 a[144:145], v224 offset:16640
v_and_b32 v251, s86, v25
v_mfma_f32_16x16x32_bf16 a[12:15], a[148:151], v[12:15], a[12:15]
ds_read_b64_tr_b16 a[146:147], v224 offset:24832
v_fma_f32 v250, v250, v244, v245
v_mfma_f32_16x16x32_bf16 a[16:19], a[152:155], v[8:11], a[16:19]
ds_read_b64_tr_b16 a[148:149], v225 offset:16640
v_fma_f32 v251, v251, v244, v245
v_mfma_f32_16x16x32_bf16 a[20:23], a[152:155], v[12:15], a[20:23]
ds_read_b64_tr_b16 a[150:151], v225 offset:24832
buffer_load_dwordx4 v233, s[40:43], s71 offen offset:1024 lds
v_mfma_f32_16x16x32_bf16 a[24:27], a[156:159], v[8:11], a[24:27]
ds_read_b64_tr_b16 a[152:153], v226 offset:16640
v_cvt_pk_bf16_f32 v25, v250, v251
v_mfma_f32_16x16x32_bf16 a[28:31], a[156:159], v[12:15], a[28:31]
s_waitcnt lgkmcnt(5)
ds_read_b64_tr_b16 a[154:155], v226 offset:24832
v_lshlrev_b32 v248, 16, v26
v_mfma_f32_16x16x32_bf16 a[32:35], a[128:131], v[8:11], a[32:35]
ds_read_b64_tr_b16 a[156:157], v227 offset:16640
v_and_b32 v249, s86, v26
v_mfma_f32_16x16x32_bf16 a[36:39], a[128:131], v[12:15], a[36:39]
ds_read_b64_tr_b16 a[158:159], v227 offset:24832
buffer_load_dwordx4 v234, s[40:43], s71 offen offset:2048 lds
v_mfma_f32_16x16x32_bf16 a[40:43], a[132:135], v[8:11], a[40:43]
ds_read_b64_tr_b16 a[128:129], v228 offset:16640
v_fma_f32 v248, v248, v244, v245
v_mfma_f32_16x16x32_bf16 a[44:47], a[132:135], v[12:15], a[44:47]
ds_read_b64_tr_b16 a[130:131], v228 offset:24832
v_fma_f32 v249, v249, v244, v245
v_mfma_f32_16x16x32_bf16 a[48:51], a[136:139], v[8:11], a[48:51]
ds_read_b64_tr_b16 a[132:133], v229 offset:16640
v_cvt_pk_bf16_f32 v26, v248, v249
v_mfma_f32_16x16x32_bf16 a[52:55], a[136:139], v[12:15], a[52:55]
ds_read_b64_tr_b16 a[134:135], v229 offset:24832
buffer_load_dwordx4 v235, s[40:43], s71 offen offset:3072 lds
v_mfma_f32_16x16x32_bf16 a[56:59], a[140:143], v[8:11], a[56:59]
ds_read_b64_tr_b16 a[136:137], v230 offset:16640
v_lshlrev_b32 v250, 16, v27
v_mfma_f32_16x16x32_bf16 a[60:63], a[140:143], v[12:15], a[60:63]
s_waitcnt lgkmcnt(5)
ds_read_b64_tr_b16 a[138:139], v230 offset:24832
v_and_b32 v251, s86, v27
v_mfma_f32_16x16x32_bf16 a[64:67], a[144:147], v[8:11], a[64:67]
ds_read_b64_tr_b16 a[140:141], v231 offset:16640
v_fma_f32 v250, v250, v244, v245
v_mfma_f32_16x16x32_bf16 a[68:71], a[144:147], v[12:15], a[68:71]
s_waitcnt vmcnt(17)
ds_read_b64_tr_b16 a[142:143], v231 offset:24832
v_fma_f32 v251, v251, v244, v245
v_mfma_f32_16x16x32_bf16 a[72:75], a[148:151], v[8:11], a[72:75]
s_barrier
ds_read_b64_tr_b16 a[144:145], v224 offset:32768
v_cvt_pk_bf16_f32 v27, v250, v251
v_mfma_f32_16x16x32_bf16 a[76:79], a[148:151], v[12:15], a[76:79]
ds_read_b64_tr_b16 a[146:147], v224 offset:40960
v_lshlrev_b32 v248, 16, v28
v_mfma_f32_16x16x32_bf16 a[80:83], a[152:155], v[8:11], a[80:83]
ds_read_b64_tr_b16 a[148:149], v225 offset:32768
v_and_b32 v249, s86, v28
v_mfma_f32_16x16x32_bf16 a[84:87], a[152:155], v[12:15], a[84:87]
s_add_u32 s71, s72, 2097152
ds_read_b64_tr_b16 a[150:151], v225 offset:40960
v_fma_f32 v248, v248, v246, v247
v_mfma_f32_16x16x32_bf16 a[88:91], a[156:159], v[8:11], a[88:91]
s_add_u32 m0, s77, 131072
ds_read_b64_tr_b16 a[152:153], v226 offset:32768
v_fma_f32 v249, v249, v246, v247
v_mfma_f32_16x16x32_bf16 a[92:95], a[156:159], v[12:15], a[92:95]
s_waitcnt lgkmcnt(5)
ds_read_b64_tr_b16 a[154:155], v226 offset:40960
buffer_load_dwordx4 v232, s[40:43], s71 offen offset:0 lds
v_mfma_f32_16x16x32_bf16 a[96:99], a[128:131], v[8:11], a[96:99]
ds_read_b64_tr_b16 a[156:157], v227 offset:32768
v_cvt_pk_bf16_f32 v28, v248, v249
v_mfma_f32_16x16x32_bf16 a[100:103], a[128:131], v[12:15], a[100:103]
ds_read_b64_tr_b16 a[158:159], v227 offset:40960
v_lshlrev_b32 v250, 16, v29
v_mfma_f32_16x16x32_bf16 a[104:107], a[132:135], v[8:11], a[104:107]
ds_read_b64_tr_b16 a[128:129], v228 offset:32768
v_and_b32 v251, s86, v29
v_mfma_f32_16x16x32_bf16 a[108:111], a[132:135], v[12:15], a[108:111]
ds_read_b64_tr_b16 a[130:131], v228 offset:40960
v_fma_f32 v250, v250, v246, v247
v_mfma_f32_16x16x32_bf16 a[112:115], a[136:139], v[8:11], a[112:115]
ds_read_b64_tr_b16 a[132:133], v229 offset:32768
v_fma_f32 v251, v251, v246, v247
v_mfma_f32_16x16x32_bf16 a[116:119], a[136:139], v[12:15], a[116:119]
ds_read_b64_tr_b16 a[134:135], v229 offset:40960
buffer_load_dwordx4 v233, s[40:43], s71 offen offset:1024 lds
v_mfma_f32_16x16x32_bf16 a[120:123], a[140:143], v[8:11], a[120:123]
ds_read_b64_tr_b16 a[136:137], v230 offset:32768
v_cvt_pk_bf16_f32 v29, v250, v251
v_mfma_f32_16x16x32_bf16 a[124:127], a[140:143], v[12:15], a[124:127]
s_waitcnt lgkmcnt(5)
ds_read_b64_tr_b16 a[138:139], v230 offset:40960
v_lshlrev_b32 v248, 16, v30
v_mfma_f32_16x16x32_bf16 a[0:3], a[144:147], v[16:19], a[0:3]
ds_read_b64_tr_b16 a[140:141], v231 offset:32768
v_and_b32 v249, s86, v30
v_mfma_f32_16x16x32_bf16 a[4:7], a[144:147], v[20:23], a[4:7]
ds_read_b64_tr_b16 a[142:143], v231 offset:40960
v_fma_f32 v248, v248, v246, v247
v_mfma_f32_16x16x32_bf16 a[8:11], a[148:151], v[16:19], a[8:11]
ds_read_b64_tr_b16 a[144:145], v224 offset:33024
v_fma_f32 v249, v249, v246, v247
v_mfma_f32_16x16x32_bf16 a[12:15], a[148:151], v[20:23], a[12:15]
ds_read_b64_tr_b16 a[146:147], v224 offset:41216
buffer_load_dwordx4 v234, s[40:43], s71 offen offset:2048 lds
v_mfma_f32_16x16x32_bf16 a[16:19], a[152:155], v[16:19], a[16:19]
ds_read_b64_tr_b16 a[148:149], v225 offset:33024
v_cvt_pk_bf16_f32 v30, v248, v249
v_mfma_f32_16x16x32_bf16 a[20:23], a[152:155], v[20:23], a[20:23]
ds_read_b64_tr_b16 a[150:151], v225 offset:41216
v_lshlrev_b32 v250, 16, v31
v_mfma_f32_16x16x32_bf16 a[24:27], a[156:159], v[16:19], a[24:27]
ds_read_b64_tr_b16 a[152:153], v226 offset:33024
v_and_b32 v251, s86, v31
v_mfma_f32_16x16x32_bf16 a[28:31], a[156:159], v[20:23], a[28:31]
s_waitcnt lgkmcnt(5)
ds_read_b64_tr_b16 a[154:155], v226 offset:41216
v_fma_f32 v250, v250, v246, v247
v_mfma_f32_16x16x32_bf16 a[32:35], a[128:131], v[16:19], a[32:35]
ds_read_b64_tr_b16 a[156:157], v227 offset:33024
v_fma_f32 v251, v251, v246, v247
v_mfma_f32_16x16x32_bf16 a[36:39], a[128:131], v[20:23], a[36:39]
ds_read_b64_tr_b16 a[158:159], v227 offset:41216
buffer_load_dwordx4 v235, s[40:43], s71 offen offset:3072 lds
v_mfma_f32_16x16x32_bf16 a[40:43], a[132:135], v[16:19], a[40:43]
ds_read_b64_tr_b16 a[128:129], v228 offset:33024
v_cvt_pk_bf16_f32 v31, v250, v251
v_mfma_f32_16x16x32_bf16 a[44:47], a[132:135], v[20:23], a[44:47]
ds_read_b64_tr_b16 a[130:131], v228 offset:41216
v_lshlrev_b32 v248, 16, v32
v_mfma_f32_16x16x32_bf16 a[48:51], a[136:139], v[16:19], a[48:51]
ds_read_b64_tr_b16 a[132:133], v229 offset:33024
v_and_b32 v249, s86, v32
v_mfma_f32_16x16x32_bf16 a[52:55], a[136:139], v[20:23], a[52:55]
s_add_u32 s71, s72, 2162688
ds_read_b64_tr_b16 a[134:135], v229 offset:41216
v_fma_f32 v248, v248, v244, v245
v_mfma_f32_16x16x32_bf16 a[56:59], a[140:143], v[16:19], a[56:59]
s_add_u32 m0, s77, 135168
ds_read_b64_tr_b16 a[136:137], v230 offset:33024
v_fma_f32 v249, v249, v244, v245
v_mfma_f32_16x16x32_bf16 a[60:63], a[140:143], v[20:23], a[60:63]
s_waitcnt lgkmcnt(5)
ds_read_b64_tr_b16 a[138:139], v230 offset:41216
buffer_load_dwordx4 v232, s[40:43], s71 offen offset:0 lds
v_mfma_f32_16x16x32_bf16 a[64:67], a[144:147], v[16:19], a[64:67]
ds_read_b64_tr_b16 a[140:141], v231 offset:33024
v_cvt_pk_bf16_f32 v32, v248, v249
v_mfma_f32_16x16x32_bf16 a[68:71], a[144:147], v[20:23], a[68:71]
ds_read_b64_tr_b16 a[142:143], v231 offset:41216
v_lshlrev_b32 v250, 16, v33
v_mfma_f32_16x16x32_bf16 a[72:75], a[148:151], v[16:19], a[72:75]
ds_read_b64_tr_b16 a[144:145], v224 offset:49152
v_and_b32 v251, s86, v33
v_mfma_f32_16x16x32_bf16 a[76:79], a[148:151], v[20:23], a[76:79]
ds_read_b64_tr_b16 a[146:147], v224 offset:57344
v_fma_f32 v250, v250, v244, v245
v_mfma_f32_16x16x32_bf16 a[80:83], a[152:155], v[16:19], a[80:83]
ds_read_b64_tr_b16 a[148:149], v225 offset:49152
v_accvgpr_read_b32 v254, a209
v_mfma_f32_16x16x32_bf16 a[84:87], a[152:155], v[20:23], a[84:87]
s_mov_b64 exec, 0xffffff
buffer_load_dword a174, v254, s[44:47], 0 offen offset:0
s_mov_b64 exec, -1
ds_read_b64_tr_b16 a[150:151], v225 offset:57344
buffer_load_dwordx4 v233, s[40:43], s71 offen offset:1024 lds
v_mfma_f32_16x16x32_bf16 a[88:91], a[156:159], v[16:19], a[88:91]
ds_read_b64_tr_b16 a[152:153], v226 offset:49152
v_fma_f32 v251, v251, v244, v245
v_mfma_f32_16x16x32_bf16 a[92:95], a[156:159], v[20:23], a[92:95]
s_waitcnt lgkmcnt(5)
ds_read_b64_tr_b16 a[154:155], v226 offset:57344
v_cvt_pk_bf16_f32 v33, v250, v251
v_mfma_f32_16x16x32_bf16 a[96:99], a[128:131], v[16:19], a[96:99]
ds_read_b64_tr_b16 a[156:157], v227 offset:49152
v_lshlrev_b32 v248, 16, v34
v_mfma_f32_16x16x32_bf16 a[100:103], a[128:131], v[20:23], a[100:103]
ds_read_b64_tr_b16 a[158:159], v227 offset:57344
v_and_b32 v249, s86, v34
v_mfma_f32_16x16x32_bf16 a[104:107], a[132:135], v[16:19], a[104:107]
ds_read_b64_tr_b16 a[128:129], v228 offset:49152
v_fma_f32 v248, v248, v244, v245
v_mfma_f32_16x16x32_bf16 a[108:111], a[132:135], v[20:23], a[108:111]
ds_read_b64_tr_b16 a[130:131], v228 offset:57344
buffer_load_dwordx4 v234, s[40:43], s71 offen offset:2048 lds
v_mfma_f32_16x16x32_bf16 a[112:115], a[136:139], v[16:19], a[112:115]
ds_read_b64_tr_b16 a[132:133], v229 offset:49152
v_fma_f32 v249, v249, v244, v245
v_mfma_f32_16x16x32_bf16 a[116:119], a[136:139], v[20:23], a[116:119]
ds_read_b64_tr_b16 a[134:135], v229 offset:57344
v_cvt_pk_bf16_f32 v34, v248, v249
v_mfma_f32_16x16x32_bf16 a[120:123], a[140:143], v[16:19], a[120:123]
ds_read_b64_tr_b16 a[136:137], v230 offset:49152
v_lshlrev_b32 v250, 16, v35
v_mfma_f32_16x16x32_bf16 a[124:127], a[140:143], v[20:23], a[124:127]
s_waitcnt lgkmcnt(5)
ds_read_b64_tr_b16 a[138:139], v230 offset:57344
v_and_b32 v251, s86, v35
v_mfma_f32_16x16x32_bf16 a[0:3], a[144:147], v[24:27], a[0:3]
ds_read_b64_tr_b16 a[140:141], v231 offset:49152
v_fma_f32 v250, v250, v244, v245
v_mfma_f32_16x16x32_bf16 a[4:7], a[144:147], v[28:31], a[4:7]
ds_read_b64_tr_b16 a[142:143], v231 offset:57344
buffer_load_dwordx4 v235, s[40:43], s71 offen offset:3072 lds
v_mfma_f32_16x16x32_bf16 a[8:11], a[148:151], v[24:27], a[8:11]
ds_read_b64_tr_b16 a[144:145], v224 offset:49408
v_fma_f32 v251, v251, v244, v245
v_mfma_f32_16x16x32_bf16 a[12:15], a[148:151], v[28:31], a[12:15]
ds_read_b64_tr_b16 a[146:147], v224 offset:57600
v_add_u32 v224, 0x10000, v224
v_mfma_f32_16x16x32_bf16 a[16:19], a[152:155], v[24:27], a[16:19]
ds_read_b64_tr_b16 a[148:149], v225 offset:49408
v_cvt_pk_bf16_f32 v35, v250, v251
v_mfma_f32_16x16x32_bf16 a[20:23], a[152:155], v[28:31], a[20:23]
ds_read_b64_tr_b16 a[150:151], v225 offset:57600
v_add_u32 v225, 0x10000, v225
v_mfma_f32_16x16x32_bf16 a[24:27], a[156:159], v[24:27], a[24:27]
ds_read_b64_tr_b16 a[152:153], v226 offset:49408
v_lshlrev_b32 v248, 16, v36
v_mfma_f32_16x16x32_bf16 a[28:31], a[156:159], v[28:31], a[28:31]
s_waitcnt lgkmcnt(5)
ds_read_b64_tr_b16 a[154:155], v226 offset:57600
v_add_u32 v226, 0x10000, v226
v_mfma_f32_16x16x32_bf16 a[32:35], a[128:131], v[24:27], a[32:35]
ds_read_b64_tr_b16 a[156:157], v227 offset:49408
v_and_b32 v249, s86, v36
v_mfma_f32_16x16x32_bf16 a[36:39], a[128:131], v[28:31], a[36:39]
ds_read_b64_tr_b16 a[158:159], v227 offset:57600
v_add_u32 v227, 0x10000, v227
v_mfma_f32_16x16x32_bf16 a[40:43], a[132:135], v[24:27], a[40:43]
ds_read_b64_tr_b16 a[128:129], v228 offset:49408
v_fma_f32 v248, v248, v246, v247
v_mfma_f32_16x16x32_bf16 a[44:47], a[132:135], v[28:31], a[44:47]
ds_read_b64_tr_b16 a[130:131], v228 offset:57600
v_add_u32 v228, 0x10000, v228
v_mfma_f32_16x16x32_bf16 a[48:51], a[136:139], v[24:27], a[48:51]
ds_read_b64_tr_b16 a[132:133], v229 offset:49408
v_fma_f32 v249, v249, v246, v247
v_mfma_f32_16x16x32_bf16 a[52:55], a[136:139], v[28:31], a[52:55]
ds_read_b64_tr_b16 a[134:135], v229 offset:57600
v_add_u32 v229, 0x10000, v229
v_mfma_f32_16x16x32_bf16 a[56:59], a[140:143], v[24:27], a[56:59]
ds_read_b64_tr_b16 a[136:137], v230 offset:49408
v_cvt_pk_bf16_f32 v36, v248, v249
v_mfma_f32_16x16x32_bf16 a[60:63], a[140:143], v[28:31], a[60:63]
s_waitcnt lgkmcnt(5)
ds_read_b64_tr_b16 a[138:139], v230 offset:57600
v_add_u32 v230, 0x10000, v230
v_mfma_f32_16x16x32_bf16 a[64:67], a[144:147], v[24:27], a[64:67]
ds_read_b64_tr_b16 a[140:141], v231 offset:49408
v_lshlrev_b32 v250, 16, v37
v_mfma_f32_16x16x32_bf16 a[68:71], a[144:147], v[28:31], a[68:71]
s_waitcnt vmcnt(18)
ds_read_b64_tr_b16 a[142:143], v231 offset:57600
v_and_b32 v251, s86, v37
v_mfma_f32_16x16x32_bf16 a[72:75], a[148:151], v[24:27], a[72:75]
s_barrier
ds_read_b64_tr_b16 a[144:145], v224 offset:0
v_fma_f32 v250, v250, v246, v247
v_mfma_f32_16x16x32_bf16 a[76:79], a[148:151], v[28:31], a[76:79]
ds_read_b64_tr_b16 a[146:147], v224 offset:8192
v_fma_f32 v251, v251, v246, v247
v_mfma_f32_16x16x32_bf16 a[80:83], a[152:155], v[24:27], a[80:83]
ds_read_b64_tr_b16 a[148:149], v225 offset:0
v_cvt_pk_bf16_f32 v37, v250, v251
v_mfma_f32_16x16x32_bf16 a[84:87], a[152:155], v[28:31], a[84:87]
s_add_u32 s71, s72, 2621440
ds_read_b64_tr_b16 a[150:151], v225 offset:8192
v_lshlrev_b32 v248, 16, v38
v_mfma_f32_16x16x32_bf16 a[88:91], a[156:159], v[24:27], a[88:91]
s_add_u32 m0, s77, 0
ds_read_b64_tr_b16 a[152:153], v226 offset:0
v_and_b32 v249, s86, v38
v_mfma_f32_16x16x32_bf16 a[92:95], a[156:159], v[28:31], a[92:95]
s_waitcnt lgkmcnt(5)
ds_read_b64_tr_b16 a[154:155], v226 offset:8192
buffer_load_dwordx4 v232, s[40:43], s71 offen offset:0 lds
v_mfma_f32_16x16x32_bf16 a[96:99], a[128:131], v[24:27], a[96:99]
ds_read_b64_tr_b16 a[156:157], v227 offset:0
v_fma_f32 v248, v248, v246, v247
v_mfma_f32_16x16x32_bf16 a[100:103], a[128:131], v[28:31], a[100:103]
ds_read_b64_tr_b16 a[158:159], v227 offset:8192
v_fma_f32 v249, v249, v246, v247
v_mfma_f32_16x16x32_bf16 a[104:107], a[132:135], v[24:27], a[104:107]
ds_read_b64_tr_b16 a[128:129], v228 offset:0
v_cvt_pk_bf16_f32 v38, v248, v249
v_mfma_f32_16x16x32_bf16 a[108:111], a[132:135], v[28:31], a[108:111]
ds_read_b64_tr_b16 a[130:131], v228 offset:8192
v_lshlrev_b32 v250, 16, v39
v_mfma_f32_16x16x32_bf16 a[112:115], a[136:139], v[24:27], a[112:115]
ds_read_b64_tr_b16 a[132:133], v229 offset:0
v_and_b32 v251, s86, v39
v_mfma_f32_16x16x32_bf16 a[116:119], a[136:139], v[28:31], a[116:119]
ds_read_b64_tr_b16 a[134:135], v229 offset:8192
buffer_load_dwordx4 v233, s[40:43], s71 offen offset:1024 lds
v_mfma_f32_16x16x32_bf16 a[120:123], a[140:143], v[24:27], a[120:123]
ds_read_b64_tr_b16 a[136:137], v230 offset:0
v_fma_f32 v250, v250, v246, v247
v_fma_f32 v251, v251, v246, v247
v_cvt_pk_bf16_f32 v39, v250, v251
v_mfma_f32_16x16x32_bf16 a[124:127], a[140:143], v[28:31], a[124:127]
s_waitcnt lgkmcnt(5)
ds_read_b64_tr_b16 a[138:139], v230 offset:8192
v_add_u32 v231, 0x10000, v231
v_mfma_f32_16x16x32_bf16 a[0:3], a[144:147], v[32:35], a[0:3]
ds_read_b64_tr_b16 a[140:141], v231 offset:0
v_lshlrev_b32 v248, 16, v40
v_mfma_f32_16x16x32_bf16 a[4:7], a[144:147], v[36:39], a[4:7]
ds_read_b64_tr_b16 a[142:143], v231 offset:8192
v_and_b32 v249, s86, v40
v_mfma_f32_16x16x32_bf16 a[8:11], a[148:151], v[32:35], a[8:11]
ds_read_b64_tr_b16 a[144:145], v224 offset:256
v_fma_f32 v248, v248, v244, v245
v_mfma_f32_16x16x32_bf16 a[12:15], a[148:151], v[36:39], a[12:15]
ds_read_b64_tr_b16 a[146:147], v224 offset:8448
buffer_load_dwordx4 v234, s[40:43], s71 offen offset:2048 lds
v_mfma_f32_16x16x32_bf16 a[16:19], a[152:155], v[32:35], a[16:19]
ds_read_b64_tr_b16 a[148:149], v225 offset:256
v_fma_f32 v249, v249, v244, v245
v_mfma_f32_16x16x32_bf16 a[20:23], a[152:155], v[36:39], a[20:23]
ds_read_b64_tr_b16 a[150:151], v225 offset:8448
v_cvt_pk_bf16_f32 v40, v248, v249
v_mfma_f32_16x16x32_bf16 a[24:27], a[156:159], v[32:35], a[24:27]
ds_read_b64_tr_b16 a[152:153], v226 offset:256
v_lshlrev_b32 v250, 16, v41
v_mfma_f32_16x16x32_bf16 a[28:31], a[156:159], v[36:39], a[28:31]
s_waitcnt lgkmcnt(5)
ds_read_b64_tr_b16 a[154:155], v226 offset:8448
v_and_b32 v251, s86, v41
v_mfma_f32_16x16x32_bf16 a[32:35], a[128:131], v[32:35], a[32:35]
ds_read_b64_tr_b16 a[156:157], v227 offset:256
v_fma_f32 v250, v250, v244, v245
v_mfma_f32_16x16x32_bf16 a[36:39], a[128:131], v[36:39], a[36:39]
ds_read_b64_tr_b16 a[158:159], v227 offset:8448
buffer_load_dwordx4 v235, s[40:43], s71 offen offset:3072 lds
v_mfma_f32_16x16x32_bf16 a[40:43], a[132:135], v[32:35], a[40:43]
ds_read_b64_tr_b16 a[128:129], v228 offset:256
v_fma_f32 v251, v251, v244, v245
v_mfma_f32_16x16x32_bf16 a[44:47], a[132:135], v[36:39], a[44:47]
ds_read_b64_tr_b16 a[130:131], v228 offset:8448
v_cvt_pk_bf16_f32 v41, v250, v251
v_mfma_f32_16x16x32_bf16 a[48:51], a[136:139], v[32:35], a[48:51]
ds_read_b64_tr_b16 a[132:133], v229 offset:256
v_lshlrev_b32 v248, 16, v42
v_mfma_f32_16x16x32_bf16 a[52:55], a[136:139], v[36:39], a[52:55]
s_add_u32 s71, s72, 2686976
ds_read_b64_tr_b16 a[134:135], v229 offset:8448
v_and_b32 v249, s86, v42
v_mfma_f32_16x16x32_bf16 a[56:59], a[140:143], v[32:35], a[56:59]
s_add_u32 m0, s77, 4096
ds_read_b64_tr_b16 a[136:137], v230 offset:256
v_fma_f32 v248, v248, v244, v245
v_mfma_f32_16x16x32_bf16 a[60:63], a[140:143], v[36:39], a[60:63]
s_waitcnt lgkmcnt(5)
ds_read_b64_tr_b16 a[138:139], v230 offset:8448
buffer_load_dwordx4 v232, s[40:43], s71 offen offset:0 lds
v_mfma_f32_16x16x32_bf16 a[64:67], a[144:147], v[32:35], a[64:67]
ds_read_b64_tr_b16 a[140:141], v231 offset:256
v_fma_f32 v249, v249, v244, v245
v_mfma_f32_16x16x32_bf16 a[68:71], a[144:147], v[36:39], a[68:71]
ds_read_b64_tr_b16 a[142:143], v231 offset:8448
v_cvt_pk_bf16_f32 v42, v248, v249
v_mfma_f32_16x16x32_bf16 a[72:75], a[148:151], v[32:35], a[72:75]
ds_read_b64_tr_b16 a[144:145], v224 offset:16384
v_lshlrev_b32 v250, 16, v43
v_mfma_f32_16x16x32_bf16 a[76:79], a[148:151], v[36:39], a[76:79]
ds_read_b64_tr_b16 a[146:147], v224 offset:24576
v_and_b32 v251, s86, v43
v_mfma_f32_16x16x32_bf16 a[80:83], a[152:155], v[32:35], a[80:83]
ds_read_b64_tr_b16 a[148:149], v225 offset:16384
v_fma_f32 v250, v250, v244, v245
v_mfma_f32_16x16x32_bf16 a[84:87], a[152:155], v[36:39], a[84:87]
ds_read_b64_tr_b16 a[150:151], v225 offset:24576
buffer_load_dwordx4 v233, s[40:43], s71 offen offset:1024 lds
v_mfma_f32_16x16x32_bf16 a[88:91], a[156:159], v[32:35], a[88:91]
ds_read_b64_tr_b16 a[152:153], v226 offset:16384
v_fma_f32 v251, v251, v244, v245
v_mfma_f32_16x16x32_bf16 a[92:95], a[156:159], v[36:39], a[92:95]
s_waitcnt lgkmcnt(5)
ds_read_b64_tr_b16 a[154:155], v226 offset:24576
v_cvt_pk_bf16_f32 v43, v250, v251
v_mfma_f32_16x16x32_bf16 a[96:99], a[128:131], v[32:35], a[96:99]
ds_read_b64_tr_b16 a[156:157], v227 offset:16384
v_lshlrev_b32 v248, 16, v44
v_mfma_f32_16x16x32_bf16 a[100:103], a[128:131], v[36:39], a[100:103]
ds_read_b64_tr_b16 a[158:159], v227 offset:24576
v_and_b32 v249, s86, v44
v_mfma_f32_16x16x32_bf16 a[104:107], a[132:135], v[32:35], a[104:107]
ds_read_b64_tr_b16 a[128:129], v228 offset:16384
v_fma_f32 v248, v248, v246, v247
v_mfma_f32_16x16x32_bf16 a[108:111], a[132:135], v[36:39], a[108:111]
ds_read_b64_tr_b16 a[130:131], v228 offset:24576
buffer_load_dwordx4 v234, s[40:43], s71 offen offset:2048 lds
v_mfma_f32_16x16x32_bf16 a[112:115], a[136:139], v[32:35], a[112:115]
ds_read_b64_tr_b16 a[132:133], v229 offset:16384
v_fma_f32 v249, v249, v246, v247
v_mfma_f32_16x16x32_bf16 a[116:119], a[136:139], v[36:39], a[116:119]
ds_read_b64_tr_b16 a[134:135], v229 offset:24576
v_cvt_pk_bf16_f32 v44, v248, v249
v_mfma_f32_16x16x32_bf16 a[120:123], a[140:143], v[32:35], a[120:123]
ds_read_b64_tr_b16 a[136:137], v230 offset:16384
v_lshlrev_b32 v250, 16, v45
v_and_b32 v251, s86, v45
v_fma_f32 v250, v250, v246, v247
v_fma_f32 v251, v251, v246, v247
v_cvt_pk_bf16_f32 v45, v250, v251
v_lshlrev_b32 v248, 16, v46
v_and_b32 v249, s86, v46
v_fma_f32 v248, v248, v246, v247
v_fma_f32 v249, v249, v246, v247
v_cvt_pk_bf16_f32 v46, v248, v249
v_lshlrev_b32 v250, 16, v47
v_and_b32 v251, s86, v47
v_fma_f32 v250, v250, v246, v247
v_fma_f32 v251, v251, v246, v247
v_cvt_pk_bf16_f32 v47, v250, v251
v_mfma_f32_16x16x32_bf16 a[124:127], a[140:143], v[36:39], a[124:127]
s_waitcnt lgkmcnt(5)
ds_read_b64_tr_b16 a[138:139], v230 offset:24576
v_lshlrev_b32 v248, 16, v48
v_mfma_f32_16x16x32_bf16 a[0:3], a[144:147], v[40:43], a[0:3]
ds_read_b64_tr_b16 a[140:141], v231 offset:16384
v_and_b32 v249, s86, v48
v_mfma_f32_16x16x32_bf16 a[4:7], a[144:147], v[44:47], a[4:7]
ds_read_b64_tr_b16 a[142:143], v231 offset:24576
buffer_load_dwordx4 v235, s[40:43], s71 offen offset:3072 lds
v_mfma_f32_16x16x32_bf16 a[8:11], a[148:151], v[40:43], a[8:11]
ds_read_b64_tr_b16 a[144:145], v224 offset:16640
v_fma_f32 v248, v248, v244, v245
v_mfma_f32_16x16x32_bf16 a[12:15], a[148:151], v[44:47], a[12:15]
ds_read_b64_tr_b16 a[146:147], v224 offset:24832
v_fma_f32 v249, v249, v244, v245
v_mfma_f32_16x16x32_bf16 a[16:19], a[152:155], v[40:43], a[16:19]
ds_read_b64_tr_b16 a[148:149], v225 offset:16640
v_cvt_pk_bf16_f32 v48, v248, v249
v_mfma_f32_16x16x32_bf16 a[20:23], a[152:155], v[44:47], a[20:23]
ds_read_b64_tr_b16 a[150:151], v225 offset:24832
v_lshlrev_b32 v250, 16, v49
v_mfma_f32_16x16x32_bf16 a[24:27], a[156:159], v[40:43], a[24:27]
ds_read_b64_tr_b16 a[152:153], v226 offset:16640
v_and_b32 v251, s86, v49
v_mfma_f32_16x16x32_bf16 a[28:31], a[156:159], v[44:47], a[28:31]
s_waitcnt lgkmcnt(5)
ds_read_b64_tr_b16 a[154:155], v226 offset:24832
v_fma_f32 v250, v250, v244, v245
v_mfma_f32_16x16x32_bf16 a[32:35], a[128:131], v[40:43], a[32:35]
ds_read_b64_tr_b16 a[156:157], v227 offset:16640
v_fma_f32 v251, v251, v244, v245
v_mfma_f32_16x16x32_bf16 a[36:39], a[128:131], v[44:47], a[36:39]
ds_read_b64_tr_b16 a[158:159], v227 offset:24832
v_cvt_pk_bf16_f32 v49, v250, v251
v_mfma_f32_16x16x32_bf16 a[40:43], a[132:135], v[40:43], a[40:43]
ds_read_b64_tr_b16 a[128:129], v228 offset:16640
v_lshlrev_b32 v248, 16, v50
v_mfma_f32_16x16x32_bf16 a[44:47], a[132:135], v[44:47], a[44:47]
ds_read_b64_tr_b16 a[130:131], v228 offset:24832
v_and_b32 v249, s86, v50
v_mfma_f32_16x16x32_bf16 a[48:51], a[136:139], v[40:43], a[48:51]
ds_read_b64_tr_b16 a[132:133], v229 offset:16640
v_fma_f32 v248, v248, v244, v245
v_mfma_f32_16x16x32_bf16 a[52:55], a[136:139], v[44:47], a[52:55]
ds_read_b64_tr_b16 a[134:135], v229 offset:24832
v_fma_f32 v249, v249, v244, v245
v_mfma_f32_16x16x32_bf16 a[56:59], a[140:143], v[40:43], a[56:59]
ds_read_b64_tr_b16 a[136:137], v230 offset:16640
v_cvt_pk_bf16_f32 v50, v248, v249
v_mfma_f32_16x16x32_bf16 a[60:63], a[140:143], v[44:47], a[60:63]
s_waitcnt lgkmcnt(5)
ds_read_b64_tr_b16 a[138:139], v230 offset:24832
v_lshlrev_b32 v250, 16, v51
v_mfma_f32_16x16x32_bf16 a[64:67], a[144:147], v[40:43], a[64:67]
ds_read_b64_tr_b16 a[140:141], v231 offset:16640
v_and_b32 v251, s86, v51
v_mfma_f32_16x16x32_bf16 a[68:71], a[144:147], v[44:47], a[68:71]
s_waitcnt vmcnt(17)
ds_read_b64_tr_b16 a[142:143], v231 offset:24832
v_fma_f32 v250, v250, v244, v245
v_mfma_f32_16x16x32_bf16 a[72:75], a[148:151], v[40:43], a[72:75]
s_barrier
ds_read_b64_tr_b16 a[144:145], v224 offset:32768
v_fma_f32 v251, v251, v244, v245
v_mfma_f32_16x16x32_bf16 a[76:79], a[148:151], v[44:47], a[76:79]
ds_read_b64_tr_b16 a[146:147], v224 offset:40960
v_cvt_pk_bf16_f32 v51, v250, v251
v_mfma_f32_16x16x32_bf16 a[80:83], a[152:155], v[40:43], a[80:83]
ds_read_b64_tr_b16 a[148:149], v225 offset:32768
v_lshlrev_b32 v248, 16, v52
v_mfma_f32_16x16x32_bf16 a[84:87], a[152:155], v[44:47], a[84:87]
s_add_u32 s71, s72, 3145728
ds_read_b64_tr_b16 a[150:151], v225 offset:40960
v_and_b32 v249, s86, v52
v_mfma_f32_16x16x32_bf16 a[88:91], a[156:159], v[40:43], a[88:91]
s_add_u32 m0, s77, 32768
ds_read_b64_tr_b16 a[152:153], v226 offset:32768
v_fma_f32 v248, v248, v246, v247
v_mfma_f32_16x16x32_bf16 a[92:95], a[156:159], v[44:47], a[92:95]
s_waitcnt lgkmcnt(5)
ds_read_b64_tr_b16 a[154:155], v226 offset:40960
buffer_load_dwordx4 v232, s[40:43], s71 offen offset:0 lds
v_mfma_f32_16x16x32_bf16 a[96:99], a[128:131], v[40:43], a[96:99]
ds_read_b64_tr_b16 a[156:157], v227 offset:32768
v_accvgpr_read_b32 v254, a200
v_mfma_f32_16x16x32_bf16 a[100:103], a[128:131], v[44:47], a[100:103]
s_mov_b64 exec, 0xffff
buffer_load_dword a174, v254, s[52:55], 0 offen offset:0
s_mov_b64 exec, -1
ds_read_b64_tr_b16 a[158:159], v227 offset:40960
v_mfma_f32_16x16x32_bf16 a[104:107], a[132:135], v[40:43], a[104:107]
ds_read_b64_tr_b16 a[128:129], v228 offset:32768
v_fma_f32 v249, v249, v246, v247
v_mfma_f32_16x16x32_bf16 a[108:111], a[132:135], v[44:47], a[108:111]
ds_read_b64_tr_b16 a[130:131], v228 offset:40960
v_cvt_pk_bf16_f32 v52, v248, v249
v_mfma_f32_16x16x32_bf16 a[112:115], a[136:139], v[40:43], a[112:115]
ds_read_b64_tr_b16 a[132:133], v229 offset:32768
v_lshlrev_b32 v250, 16, v53
v_mfma_f32_16x16x32_bf16 a[116:119], a[136:139], v[44:47], a[116:119]
ds_read_b64_tr_b16 a[134:135], v229 offset:40960
buffer_load_dwordx4 v233, s[40:43], s71 offen offset:1024 lds
v_mfma_f32_16x16x32_bf16 a[120:123], a[140:143], v[40:43], a[120:123]
ds_read_b64_tr_b16 a[136:137], v230 offset:32768
v_and_b32 v251, s86, v53
v_fma_f32 v250, v250, v246, v247
v_fma_f32 v251, v251, v246, v247
v_cvt_pk_bf16_f32 v53, v250, v251
v_lshlrev_b32 v248, 16, v54
v_and_b32 v249, s86, v54
v_fma_f32 v248, v248, v246, v247
v_fma_f32 v249, v249, v246, v247
v_cvt_pk_bf16_f32 v54, v248, v249
v_lshlrev_b32 v250, 16, v55
v_and_b32 v251, s86, v55
v_fma_f32 v250, v250, v246, v247
v_fma_f32 v251, v251, v246, v247
v_cvt_pk_bf16_f32 v55, v250, v251
v_mfma_f32_16x16x32_bf16 a[124:127], a[140:143], v[44:47], a[124:127]
s_waitcnt lgkmcnt(5)
ds_read_b64_tr_b16 a[138:139], v230 offset:40960
v_lshlrev_b32 v248, 16, v56
v_mfma_f32_16x16x32_bf16 a[0:3], a[144:147], v[48:51], a[0:3]
ds_read_b64_tr_b16 a[140:141], v231 offset:32768
v_and_b32 v249, s86, v56
v_mfma_f32_16x16x32_bf16 a[4:7], a[144:147], v[52:55], a[4:7]
ds_read_b64_tr_b16 a[142:143], v231 offset:40960
v_fma_f32 v248, v248, v244, v245
v_mfma_f32_16x16x32_bf16 a[8:11], a[148:151], v[48:51], a[8:11]
ds_read_b64_tr_b16 a[144:145], v224 offset:33024
v_fma_f32 v249, v249, v244, v245
v_mfma_f32_16x16x32_bf16 a[12:15], a[148:151], v[52:55], a[12:15]
ds_read_b64_tr_b16 a[146:147], v224 offset:41216
buffer_load_dwordx4 v234, s[40:43], s71 offen offset:2048 lds
v_mfma_f32_16x16x32_bf16 a[16:19], a[152:155], v[48:51], a[16:19]
ds_read_b64_tr_b16 a[148:149], v225 offset:33024
v_cvt_pk_bf16_f32 v56, v248, v249
v_mfma_f32_16x16x32_bf16 a[20:23], a[152:155], v[52:55], a[20:23]
ds_read_b64_tr_b16 a[150:151], v225 offset:41216
v_lshlrev_b32 v250, 16, v57
v_mfma_f32_16x16x32_bf16 a[24:27], a[156:159], v[48:51], a[24:27]
ds_read_b64_tr_b16 a[152:153], v226 offset:33024
v_and_b32 v251, s86, v57
v_mfma_f32_16x16x32_bf16 a[28:31], a[156:159], v[52:55], a[28:31]
s_waitcnt lgkmcnt(5)
ds_read_b64_tr_b16 a[154:155], v226 offset:41216
v_fma_f32 v250, v250, v244, v245
v_mfma_f32_16x16x32_bf16 a[32:35], a[128:131], v[48:51], a[32:35]
ds_read_b64_tr_b16 a[156:157], v227 offset:33024
v_fma_f32 v251, v251, v244, v245
v_mfma_f32_16x16x32_bf16 a[36:39], a[128:131], v[52:55], a[36:39]
ds_read_b64_tr_b16 a[158:159], v227 offset:41216
buffer_load_dwordx4 v235, s[40:43], s71 offen offset:3072 lds
v_mfma_f32_16x16x32_bf16 a[40:43], a[132:135], v[48:51], a[40:43]
ds_read_b64_tr_b16 a[128:129], v228 offset:33024
v_cvt_pk_bf16_f32 v57, v250, v251
v_mfma_f32_16x16x32_bf16 a[44:47], a[132:135], v[52:55], a[44:47]
ds_read_b64_tr_b16 a[130:131], v228 offset:41216
v_lshlrev_b32 v248, 16, v58
v_mfma_f32_16x16x32_bf16 a[48:51], a[136:139], v[48:51], a[48:51]
ds_read_b64_tr_b16 a[132:133], v229 offset:33024
v_and_b32 v249, s86, v58
v_mfma_f32_16x16x32_bf16 a[52:55], a[136:139], v[52:55], a[52:55]
s_add_u32 s71, s72, 3211264
ds_read_b64_tr_b16 a[134:135], v229 offset:41216
v_fma_f32 v248, v248, v244, v245
v_mfma_f32_16x16x32_bf16 a[56:59], a[140:143], v[48:51], a[56:59]
s_add_u32 m0, s77, 36864
ds_read_b64_tr_b16 a[136:137], v230 offset:33024
v_fma_f32 v249, v249, v244, v245
v_mfma_f32_16x16x32_bf16 a[60:63], a[140:143], v[52:55], a[60:63]
s_waitcnt lgkmcnt(5)
ds_read_b64_tr_b16 a[138:139], v230 offset:41216
buffer_load_dwordx4 v232, s[40:43], s71 offen offset:0 lds
v_mfma_f32_16x16x32_bf16 a[64:67], a[144:147], v[48:51], a[64:67]
ds_read_b64_tr_b16 a[140:141], v231 offset:33024
v_cvt_pk_bf16_f32 v58, v248, v249
v_mfma_f32_16x16x32_bf16 a[68:71], a[144:147], v[52:55], a[68:71]
ds_read_b64_tr_b16 a[142:143], v231 offset:41216
v_lshlrev_b32 v250, 16, v59
v_mfma_f32_16x16x32_bf16 a[72:75], a[148:151], v[48:51], a[72:75]
ds_read_b64_tr_b16 a[144:145], v224 offset:49152
v_and_b32 v251, s86, v59
v_mfma_f32_16x16x32_bf16 a[76:79], a[148:151], v[52:55], a[76:79]
ds_read_b64_tr_b16 a[146:147], v224 offset:57344
v_fma_f32 v250, v250, v244, v245
v_mfma_f32_16x16x32_bf16 a[80:83], a[152:155], v[48:51], a[80:83]
ds_read_b64_tr_b16 a[148:149], v225 offset:49152
v_fma_f32 v251, v251, v244, v245
v_mfma_f32_16x16x32_bf16 a[84:87], a[152:155], v[52:55], a[84:87]
ds_read_b64_tr_b16 a[150:151], v225 offset:57344
buffer_load_dwordx4 v233, s[40:43], s71 offen offset:1024 lds
v_mfma_f32_16x16x32_bf16 a[88:91], a[156:159], v[48:51], a[88:91]
ds_read_b64_tr_b16 a[152:153], v226 offset:49152
v_cvt_pk_bf16_f32 v59, v250, v251
v_mfma_f32_16x16x32_bf16 a[92:95], a[156:159], v[52:55], a[92:95]
s_waitcnt lgkmcnt(5)
ds_read_b64_tr_b16 a[154:155], v226 offset:57344
v_lshlrev_b32 v248, 16, v60
v_mfma_f32_16x16x32_bf16 a[96:99], a[128:131], v[48:51], a[96:99]
ds_read_b64_tr_b16 a[156:157], v227 offset:49152
v_and_b32 v249, s86, v60
v_mfma_f32_16x16x32_bf16 a[100:103], a[128:131], v[52:55], a[100:103]
ds_read_b64_tr_b16 a[158:159], v227 offset:57344
v_fma_f32 v248, v248, v246, v247
v_mfma_f32_16x16x32_bf16 a[104:107], a[132:135], v[48:51], a[104:107]
ds_read_b64_tr_b16 a[128:129], v228 offset:49152
v_fma_f32 v249, v249, v246, v247
v_mfma_f32_16x16x32_bf16 a[108:111], a[132:135], v[52:55], a[108:111]
ds_read_b64_tr_b16 a[130:131], v228 offset:57344
buffer_load_dwordx4 v234, s[40:43], s71 offen offset:2048 lds
v_mfma_f32_16x16x32_bf16 a[112:115], a[136:139], v[48:51], a[112:115]
ds_read_b64_tr_b16 a[132:133], v229 offset:49152
v_cvt_pk_bf16_f32 v60, v248, v249
v_mfma_f32_16x16x32_bf16 a[116:119], a[136:139], v[52:55], a[116:119]
ds_read_b64_tr_b16 a[134:135], v229 offset:57344
v_lshlrev_b32 v250, 16, v61
v_mfma_f32_16x16x32_bf16 a[120:123], a[140:143], v[48:51], a[120:123]
ds_read_b64_tr_b16 a[136:137], v230 offset:49152
v_and_b32 v251, s86, v61
v_fma_f32 v250, v250, v246, v247
v_fma_f32 v251, v251, v246, v247
v_cvt_pk_bf16_f32 v61, v250, v251
v_lshlrev_b32 v248, 16, v62
v_and_b32 v249, s86, v62
v_fma_f32 v248, v248, v246, v247
v_fma_f32 v249, v249, v246, v247
v_cvt_pk_bf16_f32 v62, v248, v249
v_lshlrev_b32 v250, 16, v63
v_and_b32 v251, s86, v63
v_fma_f32 v250, v250, v246, v247
v_fma_f32 v251, v251, v246, v247
v_cvt_pk_bf16_f32 v63, v250, v251
v_mfma_f32_16x16x32_bf16 a[124:127], a[140:143], v[52:55], a[124:127]
s_waitcnt lgkmcnt(5)
ds_read_b64_tr_b16 a[138:139], v230 offset:57344
v_lshlrev_b32 v248, 16, v64
v_mfma_f32_16x16x32_bf16 a[0:3], a[144:147], v[56:59], a[0:3]
ds_read_b64_tr_b16 a[140:141], v231 offset:49152
v_and_b32 v249, s86, v64
v_mfma_f32_16x16x32_bf16 a[4:7], a[144:147], v[60:63], a[4:7]
ds_read_b64_tr_b16 a[142:143], v231 offset:57344
buffer_load_dwordx4 v235, s[40:43], s71 offen offset:3072 lds
v_mfma_f32_16x16x32_bf16 a[8:11], a[148:151], v[56:59], a[8:11]
ds_read_b64_tr_b16 a[144:145], v224 offset:49408
v_fma_f32 v248, v248, v244, v245
v_mfma_f32_16x16x32_bf16 a[12:15], a[148:151], v[60:63], a[12:15]
ds_read_b64_tr_b16 a[146:147], v224 offset:57600
v_add_u32 v224, 0x10000, v224
v_mfma_f32_16x16x32_bf16 a[16:19], a[152:155], v[56:59], a[16:19]
ds_read_b64_tr_b16 a[148:149], v225 offset:49408
v_fma_f32 v249, v249, v244, v245
v_mfma_f32_16x16x32_bf16 a[20:23], a[152:155], v[60:63], a[20:23]
ds_read_b64_tr_b16 a[150:151], v225 offset:57600
v_add_u32 v225, 0x10000, v225
v_mfma_f32_16x16x32_bf16 a[24:27], a[156:159], v[56:59], a[24:27]
ds_read_b64_tr_b16 a[152:153], v226 offset:49408
v_cvt_pk_bf16_f32 v64, v248, v249
v_mfma_f32_16x16x32_bf16 a[28:31], a[156:159], v[60:63], a[28:31]
s_waitcnt lgkmcnt(5)
ds_read_b64_tr_b16 a[154:155], v226 offset:57600
v_add_u32 v226, 0x10000, v226
v_mfma_f32_16x16x32_bf16 a[32:35], a[128:131], v[56:59], a[32:35]
ds_read_b64_tr_b16 a[156:157], v227 offset:49408
v_lshlrev_b32 v250, 16, v65
v_mfma_f32_16x16x32_bf16 a[36:39], a[128:131], v[60:63], a[36:39]
ds_read_b64_tr_b16 a[158:159], v227 offset:57600
v_add_u32 v227, 0x10000, v227
v_mfma_f32_16x16x32_bf16 a[40:43], a[132:135], v[56:59], a[40:43]
ds_read_b64_tr_b16 a[128:129], v228 offset:49408
v_and_b32 v251, s86, v65
v_mfma_f32_16x16x32_bf16 a[44:47], a[132:135], v[60:63], a[44:47]
ds_read_b64_tr_b16 a[130:131], v228 offset:57600
v_add_u32 v228, 0x10000, v228
v_mfma_f32_16x16x32_bf16 a[48:51], a[136:139], v[56:59], a[48:51]
ds_read_b64_tr_b16 a[132:133], v229 offset:49408
v_fma_f32 v250, v250, v244, v245
v_mfma_f32_16x16x32_bf16 a[52:55], a[136:139], v[60:63], a[52:55]
ds_read_b64_tr_b16 a[134:135], v229 offset:57600
v_add_u32 v229, 0x10000, v229
v_mfma_f32_16x16x32_bf16 a[56:59], a[140:143], v[56:59], a[56:59]
ds_read_b64_tr_b16 a[136:137], v230 offset:49408
v_fma_f32 v251, v251, v244, v245
v_mfma_f32_16x16x32_bf16 a[60:63], a[140:143], v[60:63], a[60:63]
s_waitcnt lgkmcnt(5)
ds_read_b64_tr_b16 a[138:139], v230 offset:57600
v_add_u32 v230, 0x10000, v230
v_mfma_f32_16x16x32_bf16 a[64:67], a[144:147], v[56:59], a[64:67]
ds_read_b64_tr_b16 a[140:141], v231 offset:49408
v_cvt_pk_bf16_f32 v65, v250, v251
v_mfma_f32_16x16x32_bf16 a[68:71], a[144:147], v[60:63], a[68:71]
s_waitcnt vmcnt(17)
ds_read_b64_tr_b16 a[142:143], v231 offset:57600
v_lshlrev_b32 v248, 16, v66
v_mfma_f32_16x16x32_bf16 a[72:75], a[148:151], v[56:59], a[72:75]
s_barrier
ds_read_b64_tr_b16 a[144:145], v224 offset:0
v_and_b32 v249, s86, v66
v_mfma_f32_16x16x32_bf16 a[76:79], a[148:151], v[60:63], a[76:79]
ds_read_b64_tr_b16 a[146:147], v224 offset:8192
v_fma_f32 v248, v248, v244, v245
v_mfma_f32_16x16x32_bf16 a[80:83], a[152:155], v[56:59], a[80:83]
ds_read_b64_tr_b16 a[148:149], v225 offset:0
v_fma_f32 v249, v249, v244, v245
v_mfma_f32_16x16x32_bf16 a[84:87], a[152:155], v[60:63], a[84:87]
s_add_u32 s71, s73, 229376
ds_read_b64_tr_b16 a[150:151], v225 offset:8192
v_cvt_pk_bf16_f32 v66, v248, v249
v_mfma_f32_16x16x32_bf16 a[88:91], a[156:159], v[56:59], a[88:91]
s_add_u32 m0, s78, 65536
ds_read_b64_tr_b16 a[152:153], v226 offset:0
v_lshlrev_b32 v250, 16, v67
v_mfma_f32_16x16x32_bf16 a[92:95], a[156:159], v[60:63], a[92:95]
s_waitcnt lgkmcnt(5)
ds_read_b64_tr_b16 a[154:155], v226 offset:8192
buffer_load_dwordx4 v236, s[44:47], s71 offen offset:0 lds
v_mfma_f32_16x16x32_bf16 a[96:99], a[128:131], v[56:59], a[96:99]
ds_read_b64_tr_b16 a[156:157], v227 offset:0
v_and_b32 v251, s86, v67
v_mfma_f32_16x16x32_bf16 a[100:103], a[128:131], v[60:63], a[100:103]
ds_read_b64_tr_b16 a[158:159], v227 offset:8192
v_fma_f32 v250, v250, v244, v245
v_mfma_f32_16x16x32_bf16 a[104:107], a[132:135], v[56:59], a[104:107]
ds_read_b64_tr_b16 a[128:129], v228 offset:0
v_fma_f32 v251, v251, v244, v245
v_mfma_f32_16x16x32_bf16 a[108:111], a[132:135], v[60:63], a[108:111]
ds_read_b64_tr_b16 a[130:131], v228 offset:8192
v_cvt_pk_bf16_f32 v67, v250, v251
v_mfma_f32_16x16x32_bf16 a[112:115], a[136:139], v[56:59], a[112:115]
ds_read_b64_tr_b16 a[132:133], v229 offset:0
v_lshlrev_b32 v248, 16, v68
v_mfma_f32_16x16x32_bf16 a[116:119], a[136:139], v[60:63], a[116:119]
ds_read_b64_tr_b16 a[134:135], v229 offset:8192
buffer_load_dwordx4 v237, s[44:47], s71 offen offset:1024 lds
v_mfma_f32_16x16x32_bf16 a[120:123], a[140:143], v[56:59], a[120:123]
ds_read_b64_tr_b16 a[136:137], v230 offset:0
v_and_b32 v249, s86, v68
v_fma_f32 v248, v248, v246, v247
v_fma_f32 v249, v249, v246, v247
v_cvt_pk_bf16_f32 v68, v248, v249
v_lshlrev_b32 v250, 16, v69
v_and_b32 v251, s86, v69
v_fma_f32 v250, v250, v246, v247
v_fma_f32 v251, v251, v246, v247
v_cvt_pk_bf16_f32 v69, v250, v251
v_lshlrev_b32 v248, 16, v70
v_and_b32 v249, s86, v70
v_fma_f32 v248, v248, v246, v247
v_fma_f32 v249, v249, v246, v247
v_cvt_pk_bf16_f32 v70, v248, v249
v_lshlrev_b32 v250, 16, v71
v_and_b32 v251, s86, v71
v_fma_f32 v250, v250, v246, v247
v_fma_f32 v251, v251, v246, v247
v_cvt_pk_bf16_f32 v71, v250, v251
v_mov_b32 v252, 0
v_accvgpr_read_b32 v253, a192
v_bfi_b32 v252, 1, v253, v252
v_accvgpr_read_b32 v253, a193
v_bfi_b32 v252, 2, v253, v252
v_xor_b32 v252, 3, v252
v_accvgpr_read_b32 v253, a181
v_accvgpr_write_b32 a180, v252
v_lshrrev_b32 v253, 3, v253
buffer_store_dword v252, v253, s[64:67], s82 offen offset:512 sc1
v_mfma_f32_16x16x32_bf16 a[124:127], a[140:143], v[60:63], a[124:127]
s_waitcnt lgkmcnt(5)
ds_read_b64_tr_b16 a[138:139], v230 offset:8192
v_add_u32 v231, 0x10000, v231
v_mfma_f32_16x16x32_bf16 a[0:3], a[144:147], v[64:67], a[0:3]
ds_read_b64_tr_b16 a[140:141], v231 offset:0
v_lshlrev_b32 v248, 16, v72
v_mfma_f32_16x16x32_bf16 a[4:7], a[144:147], v[68:71], a[4:7]
ds_read_b64_tr_b16 a[142:143], v231 offset:8192
v_and_b32 v249, s86, v72
v_mfma_f32_16x16x32_bf16 a[8:11], a[148:151], v[64:67], a[8:11]
ds_read_b64_tr_b16 a[144:145], v224 offset:256
v_fma_f32 v248, v248, v244, v245
v_mfma_f32_16x16x32_bf16 a[12:15], a[148:151], v[68:71], a[12:15]
ds_read_b64_tr_b16 a[146:147], v224 offset:8448
buffer_load_dwordx4 v238, s[44:47], s71 offen offset:2048 lds
v_mfma_f32_16x16x32_bf16 a[16:19], a[152:155], v[64:67], a[16:19]
ds_read_b64_tr_b16 a[148:149], v225 offset:256
v_fma_f32 v249, v249, v244, v245
v_mfma_f32_16x16x32_bf16 a[20:23], a[152:155], v[68:71], a[20:23]
ds_read_b64_tr_b16 a[150:151], v225 offset:8448
v_cvt_pk_bf16_f32 v72, v248, v249
v_mfma_f32_16x16x32_bf16 a[24:27], a[156:159], v[64:67], a[24:27]
ds_read_b64_tr_b16 a[152:153], v226 offset:256
v_lshlrev_b32 v250, 16, v73
v_mfma_f32_16x16x32_bf16 a[28:31], a[156:159], v[68:71], a[28:31]
s_waitcnt lgkmcnt(5)
ds_read_b64_tr_b16 a[154:155], v226 offset:8448
v_and_b32 v251, s86, v73
v_mfma_f32_16x16x32_bf16 a[32:35], a[128:131], v[64:67], a[32:35]
ds_read_b64_tr_b16 a[156:157], v227 offset:256
v_fma_f32 v250, v250, v244, v245
v_mfma_f32_16x16x32_bf16 a[36:39], a[128:131], v[68:71], a[36:39]
ds_read_b64_tr_b16 a[158:159], v227 offset:8448
buffer_load_dwordx4 v239, s[44:47], s71 offen offset:3072 lds
v_mfma_f32_16x16x32_bf16 a[40:43], a[132:135], v[64:67], a[40:43]
ds_read_b64_tr_b16 a[128:129], v228 offset:256
v_fma_f32 v251, v251, v244, v245
v_mfma_f32_16x16x32_bf16 a[44:47], a[132:135], v[68:71], a[44:47]
ds_read_b64_tr_b16 a[130:131], v228 offset:8448
v_cvt_pk_bf16_f32 v73, v250, v251
v_mfma_f32_16x16x32_bf16 a[48:51], a[136:139], v[64:67], a[48:51]
ds_read_b64_tr_b16 a[132:133], v229 offset:256
v_lshlrev_b32 v248, 16, v74
v_mfma_f32_16x16x32_bf16 a[52:55], a[136:139], v[68:71], a[52:55]
ds_read_b64_tr_b16 a[134:135], v229 offset:8448
v_and_b32 v249, s86, v74
v_mfma_f32_16x16x32_bf16 a[56:59], a[140:143], v[64:67], a[56:59]
s_add_u32 m0, s78, 69632
ds_read_b64_tr_b16 a[136:137], v230 offset:256
v_fma_f32 v248, v248, v244, v245
v_mfma_f32_16x16x32_bf16 a[60:63], a[140:143], v[68:71], a[60:63]
s_waitcnt lgkmcnt(5)
ds_read_b64_tr_b16 a[138:139], v230 offset:8448
buffer_load_dwordx4 v240, s[44:47], s71 offen offset:0 lds
v_mfma_f32_16x16x32_bf16 a[64:67], a[144:147], v[64:67], a[64:67]
ds_read_b64_tr_b16 a[140:141], v231 offset:256
v_fma_f32 v249, v249, v244, v245
v_mfma_f32_16x16x32_bf16 a[68:71], a[144:147], v[68:71], a[68:71]
ds_read_b64_tr_b16 a[142:143], v231 offset:8448
v_cvt_pk_bf16_f32 v74, v248, v249
v_mfma_f32_16x16x32_bf16 a[72:75], a[148:151], v[64:67], a[72:75]
ds_read_b64_tr_b16 a[144:145], v224 offset:16384
v_lshlrev_b32 v250, 16, v75
v_mfma_f32_16x16x32_bf16 a[76:79], a[148:151], v[68:71], a[76:79]
ds_read_b64_tr_b16 a[146:147], v224 offset:24576
v_and_b32 v251, s86, v75
v_mfma_f32_16x16x32_bf16 a[80:83], a[152:155], v[64:67], a[80:83]
ds_read_b64_tr_b16 a[148:149], v225 offset:16384
v_fma_f32 v250, v250, v244, v245
v_mfma_f32_16x16x32_bf16 a[84:87], a[152:155], v[68:71], a[84:87]
ds_read_b64_tr_b16 a[150:151], v225 offset:24576
buffer_load_dwordx4 v241, s[44:47], s71 offen offset:1024 lds
v_mfma_f32_16x16x32_bf16 a[88:91], a[156:159], v[64:67], a[88:91]
ds_read_b64_tr_b16 a[152:153], v226 offset:16384
v_fma_f32 v251, v251, v244, v245
v_mfma_f32_16x16x32_bf16 a[92:95], a[156:159], v[68:71], a[92:95]
s_waitcnt lgkmcnt(5)
ds_read_b64_tr_b16 a[154:155], v226 offset:24576
v_cvt_pk_bf16_f32 v75, v250, v251
v_mfma_f32_16x16x32_bf16 a[96:99], a[128:131], v[64:67], a[96:99]
ds_read_b64_tr_b16 a[156:157], v227 offset:16384
v_lshlrev_b32 v248, 16, v76
v_mfma_f32_16x16x32_bf16 a[100:103], a[128:131], v[68:71], a[100:103]
ds_read_b64_tr_b16 a[158:159], v227 offset:24576
v_and_b32 v249, s86, v76
v_mfma_f32_16x16x32_bf16 a[104:107], a[132:135], v[64:67], a[104:107]
ds_read_b64_tr_b16 a[128:129], v228 offset:16384
v_fma_f32 v248, v248, v246, v247
v_mfma_f32_16x16x32_bf16 a[108:111], a[132:135], v[68:71], a[108:111]
ds_read_b64_tr_b16 a[130:131], v228 offset:24576
buffer_load_dwordx4 v242, s[44:47], s71 offen offset:2048 lds
v_mfma_f32_16x16x32_bf16 a[112:115], a[136:139], v[64:67], a[112:115]
ds_read_b64_tr_b16 a[132:133], v229 offset:16384
v_fma_f32 v249, v249, v246, v247
v_mfma_f32_16x16x32_bf16 a[116:119], a[136:139], v[68:71], a[116:119]
ds_read_b64_tr_b16 a[134:135], v229 offset:24576
v_cvt_pk_bf16_f32 v76, v248, v249
v_mfma_f32_16x16x32_bf16 a[120:123], a[140:143], v[64:67], a[120:123]
ds_read_b64_tr_b16 a[136:137], v230 offset:16384
v_lshlrev_b32 v250, 16, v77
v_and_b32 v251, s86, v77
v_fma_f32 v250, v250, v246, v247
v_fma_f32 v251, v251, v246, v247
v_cvt_pk_bf16_f32 v77, v250, v251
v_lshlrev_b32 v248, 16, v78
v_and_b32 v249, s86, v78
v_fma_f32 v248, v248, v246, v247
v_fma_f32 v249, v249, v246, v247
v_cvt_pk_bf16_f32 v78, v248, v249
v_lshlrev_b32 v250, 16, v79
v_and_b32 v251, s86, v79
v_fma_f32 v250, v250, v246, v247
v_fma_f32 v251, v251, v246, v247
v_cvt_pk_bf16_f32 v79, v250, v251
v_mfma_f32_16x16x32_bf16 a[124:127], a[140:143], v[68:71], a[124:127]
s_waitcnt lgkmcnt(5)
ds_read_b64_tr_b16 a[138:139], v230 offset:24576
v_lshlrev_b32 v248, 16, v80
v_mfma_f32_16x16x32_bf16 a[0:3], a[144:147], v[72:75], a[0:3]
ds_read_b64_tr_b16 a[140:141], v231 offset:16384
v_and_b32 v249, s86, v80
v_mfma_f32_16x16x32_bf16 a[4:7], a[144:147], v[76:79], a[4:7]
ds_read_b64_tr_b16 a[142:143], v231 offset:24576
v_fma_f32 v248, v248, v244, v245
v_mfma_f32_16x16x32_bf16 a[8:11], a[148:151], v[72:75], a[8:11]
ds_read_b64_tr_b16 a[144:145], v224 offset:16640
v_fma_f32 v249, v249, v244, v245
v_mfma_f32_16x16x32_bf16 a[12:15], a[148:151], v[76:79], a[12:15]
ds_read_b64_tr_b16 a[146:147], v224 offset:24832
v_add_u32 v224, 0xfffe0000, v224
v_mfma_f32_16x16x32_bf16 a[16:19], a[152:155], v[72:75], a[16:19]
ds_read_b64_tr_b16 a[148:149], v225 offset:16640
v_cvt_pk_bf16_f32 v80, v248, v249
v_mfma_f32_16x16x32_bf16 a[20:23], a[152:155], v[76:79], a[20:23]
ds_read_b64_tr_b16 a[150:151], v225 offset:24832
v_add_u32 v225, 0xfffe0000, v225
v_mfma_f32_16x16x32_bf16 a[24:27], a[156:159], v[72:75], a[24:27]
ds_read_b64_tr_b16 a[152:153], v226 offset:16640
v_lshlrev_b32 v250, 16, v81
v_mfma_f32_16x16x32_bf16 a[28:31], a[156:159], v[76:79], a[28:31]
s_waitcnt lgkmcnt(5)
ds_read_b64_tr_b16 a[154:155], v226 offset:24832
v_add_u32 v226, 0xfffe0000, v226
v_mfma_f32_16x16x32_bf16 a[32:35], a[128:131], v[72:75], a[32:35]
ds_read_b64_tr_b16 a[156:157], v227 offset:16640
v_and_b32 v251, s86, v81
v_mfma_f32_16x16x32_bf16 a[36:39], a[128:131], v[76:79], a[36:39]
ds_read_b64_tr_b16 a[158:159], v227 offset:24832
v_add_u32 v227, 0xfffe0000, v227
v_mfma_f32_16x16x32_bf16 a[40:43], a[132:135], v[72:75], a[40:43]
ds_read_b64_tr_b16 a[128:129], v228 offset:16640
v_fma_f32 v250, v250, v244, v245
v_mfma_f32_16x16x32_bf16 a[44:47], a[132:135], v[76:79], a[44:47]
ds_read_b64_tr_b16 a[130:131], v228 offset:24832
v_add_u32 v228, 0xfffe0000, v228
v_mfma_f32_16x16x32_bf16 a[48:51], a[136:139], v[72:75], a[48:51]
ds_read_b64_tr_b16 a[132:133], v229 offset:16640
v_fma_f32 v251, v251, v244, v245
v_mfma_f32_16x16x32_bf16 a[52:55], a[136:139], v[76:79], a[52:55]
ds_read_b64_tr_b16 a[134:135], v229 offset:24832
v_add_u32 v229, 0xfffe0000, v229
v_mfma_f32_16x16x32_bf16 a[56:59], a[140:143], v[72:75], a[56:59]
ds_read_b64_tr_b16 a[136:137], v230 offset:16640
v_cvt_pk_bf16_f32 v81, v250, v251
v_mfma_f32_16x16x32_bf16 a[60:63], a[140:143], v[76:79], a[60:63]
s_waitcnt lgkmcnt(5)
ds_read_b64_tr_b16 a[138:139], v230 offset:24832
v_add_u32 v230, 0xfffe0000, v230
v_mfma_f32_16x16x32_bf16 a[64:67], a[144:147], v[72:75], a[64:67]
s_waitcnt vmcnt(17)
ds_read_b64_tr_b16 a[140:141], v231 offset:16640
v_accvgpr_read_b32 v254, a201
v_mfma_f32_16x16x32_bf16 a[68:71], a[144:147], v[76:79], a[68:71]
s_mov_b64 exec, 0xffff
buffer_load_dword a174, v254, s[56:59], 0 offen offset:0
s_mov_b64 exec, -1
ds_read_b64_tr_b16 a[142:143], v231 offset:24832
v_mfma_f32_16x16x32_bf16 a[72:75], a[148:151], v[72:75], a[72:75]
s_barrier
ds_read_b64_tr_b16 a[144:145], v224 offset:0
v_lshlrev_b32 v248, 16, v82
v_mfma_f32_16x16x32_bf16 a[76:79], a[148:151], v[76:79], a[76:79]
ds_read_b64_tr_b16 a[146:147], v224 offset:8192
v_and_b32 v249, s86, v82
v_mfma_f32_16x16x32_bf16 a[80:83], a[152:155], v[72:75], a[80:83]
ds_read_b64_tr_b16 a[148:149], v225 offset:0
v_fma_f32 v248, v248, v244, v245
v_mfma_f32_16x16x32_bf16 a[84:87], a[152:155], v[76:79], a[84:87]
s_add_u32 s71, s73, 458752
ds_read_b64_tr_b16 a[150:151], v225 offset:8192
v_fma_f32 v249, v249, v244, v245
v_mfma_f32_16x16x32_bf16 a[88:91], a[156:159], v[72:75], a[88:91]
s_add_u32 m0, s78, 98304
ds_read_b64_tr_b16 a[152:153], v226 offset:0
v_cvt_pk_bf16_f32 v82, v248, v249
v_mfma_f32_16x16x32_bf16 a[92:95], a[156:159], v[76:79], a[92:95]
s_waitcnt lgkmcnt(5)
ds_read_b64_tr_b16 a[154:155], v226 offset:8192
buffer_load_dwordx4 v236, s[44:47], s71 offen offset:0 lds
v_mfma_f32_16x16x32_bf16 a[96:99], a[128:131], v[72:75], a[96:99]
ds_read_b64_tr_b16 a[156:157], v227 offset:0
v_lshlrev_b32 v250, 16, v83
v_mfma_f32_16x16x32_bf16 a[100:103], a[128:131], v[76:79], a[100:103]
ds_read_b64_tr_b16 a[158:159], v227 offset:8192
v_and_b32 v251, s86, v83
v_mfma_f32_16x16x32_bf16 a[104:107], a[132:135], v[72:75], a[104:107]
ds_read_b64_tr_b16 a[128:129], v228 offset:0
v_fma_f32 v250, v250, v244, v245
v_mfma_f32_16x16x32_bf16 a[108:111], a[132:135], v[76:79], a[108:111]
ds_read_b64_tr_b16 a[130:131], v228 offset:8192
v_fma_f32 v251, v251, v244, v245
v_mfma_f32_16x16x32_bf16 a[112:115], a[136:139], v[72:75], a[112:115]
ds_read_b64_tr_b16 a[132:133], v229 offset:0
v_cvt_pk_bf16_f32 v83, v250, v251
v_mfma_f32_16x16x32_bf16 a[116:119], a[136:139], v[76:79], a[116:119]
ds_read_b64_tr_b16 a[134:135], v229 offset:8192
buffer_load_dwordx4 v237, s[44:47], s71 offen offset:1024 lds
v_mfma_f32_16x16x32_bf16 a[120:123], a[140:143], v[72:75], a[120:123]
ds_read_b64_tr_b16 a[136:137], v230 offset:0
v_lshlrev_b32 v248, 16, v84
v_and_b32 v249, s86, v84
v_fma_f32 v248, v248, v246, v247
v_fma_f32 v249, v249, v246, v247
v_cvt_pk_bf16_f32 v84, v248, v249
v_lshlrev_b32 v250, 16, v85
v_and_b32 v251, s86, v85
v_fma_f32 v250, v250, v246, v247
v_fma_f32 v251, v251, v246, v247
v_cvt_pk_bf16_f32 v85, v250, v251
v_lshlrev_b32 v248, 16, v86
v_and_b32 v249, s86, v86
v_fma_f32 v248, v248, v246, v247
v_fma_f32 v249, v249, v246, v247
v_cvt_pk_bf16_f32 v86, v248, v249
v_lshlrev_b32 v250, 16, v87
v_and_b32 v251, s86, v87
v_fma_f32 v250, v250, v246, v247
v_fma_f32 v251, v251, v246, v247
v_cvt_pk_bf16_f32 v87, v250, v251
v_mfma_f32_16x16x32_bf16 a[124:127], a[140:143], v[76:79], a[124:127]
s_waitcnt lgkmcnt(5)
ds_read_b64_tr_b16 a[138:139], v230 offset:8192
v_add_u32 v231, 0xfffe0000, v231
v_mfma_f32_16x16x32_bf16 a[0:3], a[144:147], v[80:83], a[0:3]
ds_read_b64_tr_b16 a[140:141], v231 offset:0
v_lshlrev_b32 v248, 16, v88
v_mfma_f32_16x16x32_bf16 a[4:7], a[144:147], v[84:87], a[4:7]
ds_read_b64_tr_b16 a[142:143], v231 offset:8192
v_and_b32 v249, s86, v88
v_mfma_f32_16x16x32_bf16 a[8:11], a[148:151], v[80:83], a[8:11]
ds_read_b64_tr_b16 a[144:145], v224 offset:256
v_fma_f32 v248, v248, v244, v245
v_mfma_f32_16x16x32_bf16 a[12:15], a[148:151], v[84:87], a[12:15]
ds_read_b64_tr_b16 a[146:147], v224 offset:8448
buffer_load_dwordx4 v238, s[44:47], s71 offen offset:2048 lds
v_mfma_f32_16x16x32_bf16 a[16:19], a[152:155], v[80:83], a[16:19]
ds_read_b64_tr_b16 a[148:149], v225 offset:256
v_fma_f32 v249, v249, v244, v245
v_mfma_f32_16x16x32_bf16 a[20:23], a[152:155], v[84:87], a[20:23]
ds_read_b64_tr_b16 a[150:151], v225 offset:8448
v_cvt_pk_bf16_f32 v88, v248, v249
v_mfma_f32_16x16x32_bf16 a[24:27], a[156:159], v[80:83], a[24:27]
ds_read_b64_tr_b16 a[152:153], v226 offset:256
v_lshlrev_b32 v250, 16, v89
v_mfma_f32_16x16x32_bf16 a[28:31], a[156:159], v[84:87], a[28:31]
s_waitcnt lgkmcnt(5)
ds_read_b64_tr_b16 a[154:155], v226 offset:8448
v_and_b32 v251, s86, v89
v_mfma_f32_16x16x32_bf16 a[32:35], a[128:131], v[80:83], a[32:35]
ds_read_b64_tr_b16 a[156:157], v227 offset:256
v_fma_f32 v250, v250, v244, v245
v_mfma_f32_16x16x32_bf16 a[36:39], a[128:131], v[84:87], a[36:39]
ds_read_b64_tr_b16 a[158:159], v227 offset:8448
buffer_load_dwordx4 v239, s[44:47], s71 offen offset:3072 lds
v_mfma_f32_16x16x32_bf16 a[40:43], a[132:135], v[80:83], a[40:43]
ds_read_b64_tr_b16 a[128:129], v228 offset:256
v_fma_f32 v251, v251, v244, v245
v_mfma_f32_16x16x32_bf16 a[44:47], a[132:135], v[84:87], a[44:47]
ds_read_b64_tr_b16 a[130:131], v228 offset:8448
v_cvt_pk_bf16_f32 v89, v250, v251
v_mfma_f32_16x16x32_bf16 a[48:51], a[136:139], v[80:83], a[48:51]
ds_read_b64_tr_b16 a[132:133], v229 offset:256
v_lshlrev_b32 v248, 16, v90
v_mfma_f32_16x16x32_bf16 a[52:55], a[136:139], v[84:87], a[52:55]
ds_read_b64_tr_b16 a[134:135], v229 offset:8448
v_and_b32 v249, s86, v90
v_mfma_f32_16x16x32_bf16 a[56:59], a[140:143], v[80:83], a[56:59]
s_add_u32 m0, s78, 102400
ds_read_b64_tr_b16 a[136:137], v230 offset:256
v_fma_f32 v248, v248, v244, v245
v_mfma_f32_16x16x32_bf16 a[60:63], a[140:143], v[84:87], a[60:63]
s_waitcnt lgkmcnt(5)
ds_read_b64_tr_b16 a[138:139], v230 offset:8448
buffer_load_dwordx4 v240, s[44:47], s71 offen offset:0 lds
v_mfma_f32_16x16x32_bf16 a[64:67], a[144:147], v[80:83], a[64:67]
ds_read_b64_tr_b16 a[140:141], v231 offset:256
v_fma_f32 v249, v249, v244, v245
v_mfma_f32_16x16x32_bf16 a[68:71], a[144:147], v[84:87], a[68:71]
ds_read_b64_tr_b16 a[142:143], v231 offset:8448
v_cvt_pk_bf16_f32 v90, v248, v249
v_mfma_f32_16x16x32_bf16 a[72:75], a[148:151], v[80:83], a[72:75]
ds_read_b64_tr_b16 a[144:145], v224 offset:16384
v_lshlrev_b32 v250, 16, v91
v_mfma_f32_16x16x32_bf16 a[76:79], a[148:151], v[84:87], a[76:79]
ds_read_b64_tr_b16 a[146:147], v224 offset:24576
v_and_b32 v251, s86, v91
v_mfma_f32_16x16x32_bf16 a[80:83], a[152:155], v[80:83], a[80:83]
ds_read_b64_tr_b16 a[148:149], v225 offset:16384
v_fma_f32 v250, v250, v244, v245
v_mfma_f32_16x16x32_bf16 a[84:87], a[152:155], v[84:87], a[84:87]
ds_read_b64_tr_b16 a[150:151], v225 offset:24576
buffer_load_dwordx4 v241, s[44:47], s71 offen offset:1024 lds
v_mfma_f32_16x16x32_bf16 a[88:91], a[156:159], v[80:83], a[88:91]
ds_read_b64_tr_b16 a[152:153], v226 offset:16384
v_fma_f32 v251, v251, v244, v245
v_mfma_f32_16x16x32_bf16 a[92:95], a[156:159], v[84:87], a[92:95]
s_waitcnt lgkmcnt(5)
ds_read_b64_tr_b16 a[154:155], v226 offset:24576
v_cvt_pk_bf16_f32 v91, v250, v251
v_mfma_f32_16x16x32_bf16 a[96:99], a[128:131], v[80:83], a[96:99]
ds_read_b64_tr_b16 a[156:157], v227 offset:16384
v_lshlrev_b32 v248, 16, v92
v_mfma_f32_16x16x32_bf16 a[100:103], a[128:131], v[84:87], a[100:103]
ds_read_b64_tr_b16 a[158:159], v227 offset:24576
v_and_b32 v249, s86, v92
v_mfma_f32_16x16x32_bf16 a[104:107], a[132:135], v[80:83], a[104:107]
ds_read_b64_tr_b16 a[128:129], v228 offset:16384
v_fma_f32 v248, v248, v246, v247
v_mfma_f32_16x16x32_bf16 a[108:111], a[132:135], v[84:87], a[108:111]
ds_read_b64_tr_b16 a[130:131], v228 offset:24576
buffer_load_dwordx4 v242, s[44:47], s71 offen offset:2048 lds
v_mfma_f32_16x16x32_bf16 a[112:115], a[136:139], v[80:83], a[112:115]
ds_read_b64_tr_b16 a[132:133], v229 offset:16384
v_fma_f32 v249, v249, v246, v247
v_mfma_f32_16x16x32_bf16 a[116:119], a[136:139], v[84:87], a[116:119]
ds_read_b64_tr_b16 a[134:135], v229 offset:24576
v_cvt_pk_bf16_f32 v92, v248, v249
v_mfma_f32_16x16x32_bf16 a[120:123], a[140:143], v[80:83], a[120:123]
ds_read_b64_tr_b16 a[136:137], v230 offset:16384
v_lshlrev_b32 v250, 16, v93
v_and_b32 v251, s86, v93
v_fma_f32 v250, v250, v246, v247
v_fma_f32 v251, v251, v246, v247
v_cvt_pk_bf16_f32 v93, v250, v251
v_lshlrev_b32 v248, 16, v94
v_and_b32 v249, s86, v94
v_fma_f32 v248, v248, v246, v247
v_fma_f32 v249, v249, v246, v247
v_cvt_pk_bf16_f32 v94, v248, v249
v_lshlrev_b32 v250, 16, v95
v_and_b32 v251, s86, v95
v_fma_f32 v250, v250, v246, v247
v_fma_f32 v251, v251, v246, v247
v_cvt_pk_bf16_f32 v95, v250, v251
v_mfma_f32_16x16x32_bf16 a[124:127], a[140:143], v[84:87], a[124:127]
s_waitcnt lgkmcnt(5)
ds_read_b64_tr_b16 a[138:139], v230 offset:24576
v_lshlrev_b32 v248, 16, v96
v_mfma_f32_16x16x32_bf16 a[0:3], a[144:147], v[88:91], a[0:3]
ds_read_b64_tr_b16 a[140:141], v231 offset:16384
v_and_b32 v249, s86, v96
v_mfma_f32_16x16x32_bf16 a[4:7], a[144:147], v[92:95], a[4:7]
ds_read_b64_tr_b16 a[142:143], v231 offset:24576
v_fma_f32 v248, v248, v244, v245
v_mfma_f32_16x16x32_bf16 a[8:11], a[148:151], v[88:91], a[8:11]
ds_read_b64_tr_b16 a[144:145], v224 offset:16640
v_fma_f32 v249, v249, v244, v245
v_mfma_f32_16x16x32_bf16 a[12:15], a[148:151], v[92:95], a[12:15]
ds_read_b64_tr_b16 a[146:147], v224 offset:24832
v_cvt_pk_bf16_f32 v96, v248, v249
v_mfma_f32_16x16x32_bf16 a[16:19], a[152:155], v[88:91], a[16:19]
ds_read_b64_tr_b16 a[148:149], v225 offset:16640
v_lshlrev_b32 v250, 16, v97
v_mfma_f32_16x16x32_bf16 a[20:23], a[152:155], v[92:95], a[20:23]
ds_read_b64_tr_b16 a[150:151], v225 offset:24832
v_and_b32 v251, s86, v97
v_mfma_f32_16x16x32_bf16 a[24:27], a[156:159], v[88:91], a[24:27]
ds_read_b64_tr_b16 a[152:153], v226 offset:16640
v_fma_f32 v250, v250, v244, v245
v_mfma_f32_16x16x32_bf16 a[28:31], a[156:159], v[92:95], a[28:31]
s_waitcnt lgkmcnt(5)
ds_read_b64_tr_b16 a[154:155], v226 offset:24832
v_fma_f32 v251, v251, v244, v245
v_mfma_f32_16x16x32_bf16 a[32:35], a[128:131], v[88:91], a[32:35]
ds_read_b64_tr_b16 a[156:157], v227 offset:16640
v_cvt_pk_bf16_f32 v97, v250, v251
v_mfma_f32_16x16x32_bf16 a[36:39], a[128:131], v[92:95], a[36:39]
ds_read_b64_tr_b16 a[158:159], v227 offset:24832
v_lshlrev_b32 v248, 16, v98
v_mfma_f32_16x16x32_bf16 a[40:43], a[132:135], v[88:91], a[40:43]
ds_read_b64_tr_b16 a[128:129], v228 offset:16640
v_and_b32 v249, s86, v98
v_mfma_f32_16x16x32_bf16 a[44:47], a[132:135], v[92:95], a[44:47]
ds_read_b64_tr_b16 a[130:131], v228 offset:24832
v_fma_f32 v248, v248, v244, v245
v_mfma_f32_16x16x32_bf16 a[48:51], a[136:139], v[88:91], a[48:51]
ds_read_b64_tr_b16 a[132:133], v229 offset:16640
v_fma_f32 v249, v249, v244, v245
v_mfma_f32_16x16x32_bf16 a[52:55], a[136:139], v[92:95], a[52:55]
ds_read_b64_tr_b16 a[134:135], v229 offset:24832
v_cvt_pk_bf16_f32 v98, v248, v249
v_mfma_f32_16x16x32_bf16 a[56:59], a[140:143], v[88:91], a[56:59]
ds_read_b64_tr_b16 a[136:137], v230 offset:16640
v_lshlrev_b32 v250, 16, v99
v_mfma_f32_16x16x32_bf16 a[60:63], a[140:143], v[92:95], a[60:63]
s_waitcnt lgkmcnt(5)
ds_read_b64_tr_b16 a[138:139], v230 offset:24832
v_and_b32 v251, s86, v99
v_mfma_f32_16x16x32_bf16 a[64:67], a[144:147], v[88:91], a[64:67]
ds_read_b64_tr_b16 a[140:141], v231 offset:16640
v_fma_f32 v250, v250, v244, v245
v_mfma_f32_16x16x32_bf16 a[68:71], a[144:147], v[92:95], a[68:71]
s_waitcnt vmcnt(16)
ds_read_b64_tr_b16 a[142:143], v231 offset:24832
v_fma_f32 v251, v251, v244, v245
v_mfma_f32_16x16x32_bf16 a[72:75], a[148:151], v[88:91], a[72:75]
s_barrier
ds_read_b64_tr_b16 a[144:145], v224 offset:32768
v_cvt_pk_bf16_f32 v99, v250, v251
v_mfma_f32_16x16x32_bf16 a[76:79], a[148:151], v[92:95], a[76:79]
ds_read_b64_tr_b16 a[146:147], v224 offset:40960
v_lshlrev_b32 v248, 16, v100
v_mfma_f32_16x16x32_bf16 a[80:83], a[152:155], v[88:91], a[80:83]
ds_read_b64_tr_b16 a[148:149], v225 offset:32768
v_and_b32 v249, s86, v100
v_mfma_f32_16x16x32_bf16 a[84:87], a[152:155], v[92:95], a[84:87]
s_add_u32 s71, s73, 688128
ds_read_b64_tr_b16 a[150:151], v225 offset:40960
v_fma_f32 v248, v248, v246, v247
v_mfma_f32_16x16x32_bf16 a[88:91], a[156:159], v[88:91], a[88:91]
s_add_u32 m0, s78, 131072
ds_read_b64_tr_b16 a[152:153], v226 offset:32768
v_fma_f32 v249, v249, v246, v247
v_mfma_f32_16x16x32_bf16 a[92:95], a[156:159], v[92:95], a[92:95]
s_waitcnt lgkmcnt(5)
ds_read_b64_tr_b16 a[154:155], v226 offset:40960
buffer_load_dwordx4 v236, s[44:47], s71 offen offset:0 lds
v_mfma_f32_16x16x32_bf16 a[96:99], a[128:131], v[88:91], a[96:99]
ds_read_b64_tr_b16 a[156:157], v227 offset:32768
v_cvt_pk_bf16_f32 v100, v248, v249
v_mfma_f32_16x16x32_bf16 a[100:103], a[128:131], v[92:95], a[100:103]
ds_read_b64_tr_b16 a[158:159], v227 offset:40960
v_lshlrev_b32 v250, 16, v101
v_mfma_f32_16x16x32_bf16 a[104:107], a[132:135], v[88:91], a[104:107]
ds_read_b64_tr_b16 a[128:129], v228 offset:32768
v_and_b32 v251, s86, v101
v_mfma_f32_16x16x32_bf16 a[108:111], a[132:135], v[92:95], a[108:111]
ds_read_b64_tr_b16 a[130:131], v228 offset:40960
v_fma_f32 v250, v250, v246, v247
v_mfma_f32_16x16x32_bf16 a[112:115], a[136:139], v[88:91], a[112:115]
ds_read_b64_tr_b16 a[132:133], v229 offset:32768
v_fma_f32 v251, v251, v246, v247
v_mfma_f32_16x16x32_bf16 a[116:119], a[136:139], v[92:95], a[116:119]
ds_read_b64_tr_b16 a[134:135], v229 offset:40960
buffer_load_dwordx4 v237, s[44:47], s71 offen offset:1024 lds
v_mfma_f32_16x16x32_bf16 a[120:123], a[140:143], v[88:91], a[120:123]
ds_read_b64_tr_b16 a[136:137], v230 offset:32768
v_cvt_pk_bf16_f32 v101, v250, v251
v_lshlrev_b32 v248, 16, v102
v_and_b32 v249, s86, v102
v_fma_f32 v248, v248, v246, v247
v_fma_f32 v249, v249, v246, v247
v_cvt_pk_bf16_f32 v102, v248, v249
v_lshlrev_b32 v250, 16, v103
v_and_b32 v251, s86, v103
v_fma_f32 v250, v250, v246, v247
v_fma_f32 v251, v251, v246, v247
v_cvt_pk_bf16_f32 v103, v250, v251
v_mfma_f32_16x16x32_bf16 a[124:127], a[140:143], v[92:95], a[124:127]
s_waitcnt lgkmcnt(5)
ds_read_b64_tr_b16 a[138:139], v230 offset:40960
v_lshlrev_b32 v248, 16, v104
v_mfma_f32_16x16x32_bf16 a[0:3], a[144:147], v[96:99], a[0:3]
ds_read_b64_tr_b16 a[140:141], v231 offset:32768
v_and_b32 v249, s86, v104
v_mfma_f32_16x16x32_bf16 a[4:7], a[144:147], v[100:103], a[4:7]
ds_read_b64_tr_b16 a[142:143], v231 offset:40960
v_fma_f32 v248, v248, v244, v245
v_mfma_f32_16x16x32_bf16 a[8:11], a[148:151], v[96:99], a[8:11]
ds_read_b64_tr_b16 a[144:145], v224 offset:33024
v_fma_f32 v249, v249, v244, v245
v_mfma_f32_16x16x32_bf16 a[12:15], a[148:151], v[100:103], a[12:15]
ds_read_b64_tr_b16 a[146:147], v224 offset:41216
buffer_load_dwordx4 v238, s[44:47], s71 offen offset:2048 lds
v_mfma_f32_16x16x32_bf16 a[16:19], a[152:155], v[96:99], a[16:19]
ds_read_b64_tr_b16 a[148:149], v225 offset:33024
v_cvt_pk_bf16_f32 v104, v248, v249
v_mfma_f32_16x16x32_bf16 a[20:23], a[152:155], v[100:103], a[20:23]
ds_read_b64_tr_b16 a[150:151], v225 offset:41216
v_lshlrev_b32 v250, 16, v105
v_mfma_f32_16x16x32_bf16 a[24:27], a[156:159], v[96:99], a[24:27]
ds_read_b64_tr_b16 a[152:153], v226 offset:33024
v_and_b32 v251, s86, v105
v_mfma_f32_16x16x32_bf16 a[28:31], a[156:159], v[100:103], a[28:31]
s_waitcnt lgkmcnt(5)
ds_read_b64_tr_b16 a[154:155], v226 offset:41216
v_fma_f32 v250, v250, v244, v245
v_mfma_f32_16x16x32_bf16 a[32:35], a[128:131], v[96:99], a[32:35]
ds_read_b64_tr_b16 a[156:157], v227 offset:33024
v_fma_f32 v251, v251, v244, v245
v_mfma_f32_16x16x32_bf16 a[36:39], a[128:131], v[100:103], a[36:39]
ds_read_b64_tr_b16 a[158:159], v227 offset:41216
buffer_load_dwordx4 v239, s[44:47], s71 offen offset:3072 lds
v_mfma_f32_16x16x32_bf16 a[40:43], a[132:135], v[96:99], a[40:43]
ds_read_b64_tr_b16 a[128:129], v228 offset:33024
v_cvt_pk_bf16_f32 v105, v250, v251
v_mfma_f32_16x16x32_bf16 a[44:47], a[132:135], v[100:103], a[44:47]
ds_read_b64_tr_b16 a[130:131], v228 offset:41216
v_lshlrev_b32 v248, 16, v106
v_mfma_f32_16x16x32_bf16 a[48:51], a[136:139], v[96:99], a[48:51]
ds_read_b64_tr_b16 a[132:133], v229 offset:33024
v_and_b32 v249, s86, v106
v_mfma_f32_16x16x32_bf16 a[52:55], a[136:139], v[100:103], a[52:55]
ds_read_b64_tr_b16 a[134:135], v229 offset:41216
v_fma_f32 v248, v248, v244, v245
v_mfma_f32_16x16x32_bf16 a[56:59], a[140:143], v[96:99], a[56:59]
s_add_u32 m0, s78, 135168
ds_read_b64_tr_b16 a[136:137], v230 offset:33024
v_fma_f32 v249, v249, v244, v245
v_mfma_f32_16x16x32_bf16 a[60:63], a[140:143], v[100:103], a[60:63]
s_waitcnt lgkmcnt(5)
ds_read_b64_tr_b16 a[138:139], v230 offset:41216
buffer_load_dwordx4 v240, s[44:47], s71 offen offset:0 lds
v_mfma_f32_16x16x32_bf16 a[64:67], a[144:147], v[96:99], a[64:67]
ds_read_b64_tr_b16 a[140:141], v231 offset:33024
v_cvt_pk_bf16_f32 v106, v248, v249
v_mfma_f32_16x16x32_bf16 a[68:71], a[144:147], v[100:103], a[68:71]
ds_read_b64_tr_b16 a[142:143], v231 offset:41216
v_lshlrev_b32 v250, 16, v107
v_mfma_f32_16x16x32_bf16 a[72:75], a[148:151], v[96:99], a[72:75]
ds_read_b64_tr_b16 a[144:145], v224 offset:49152
v_and_b32 v251, s86, v107
v_mfma_f32_16x16x32_bf16 a[76:79], a[148:151], v[100:103], a[76:79]
ds_read_b64_tr_b16 a[146:147], v224 offset:57344
v_fma_f32 v250, v250, v244, v245
v_mfma_f32_16x16x32_bf16 a[80:83], a[152:155], v[96:99], a[80:83]
ds_read_b64_tr_b16 a[148:149], v225 offset:49152
v_fma_f32 v251, v251, v244, v245
v_mfma_f32_16x16x32_bf16 a[84:87], a[152:155], v[100:103], a[84:87]
ds_read_b64_tr_b16 a[150:151], v225 offset:57344
buffer_load_dwordx4 v241, s[44:47], s71 offen offset:1024 lds
v_mfma_f32_16x16x32_bf16 a[88:91], a[156:159], v[96:99], a[88:91]
ds_read_b64_tr_b16 a[152:153], v226 offset:49152
v_cvt_pk_bf16_f32 v107, v250, v251
v_mfma_f32_16x16x32_bf16 a[92:95], a[156:159], v[100:103], a[92:95]
s_waitcnt lgkmcnt(5)
ds_read_b64_tr_b16 a[154:155], v226 offset:57344
v_lshlrev_b32 v248, 16, v108
v_mfma_f32_16x16x32_bf16 a[96:99], a[128:131], v[96:99], a[96:99]
ds_read_b64_tr_b16 a[156:157], v227 offset:49152
v_and_b32 v249, s86, v108
v_mfma_f32_16x16x32_bf16 a[100:103], a[128:131], v[100:103], a[100:103]
ds_read_b64_tr_b16 a[158:159], v227 offset:57344
v_fma_f32 v248, v248, v246, v247
v_mfma_f32_16x16x32_bf16 a[104:107], a[132:135], v[96:99], a[104:107]
ds_read_b64_tr_b16 a[128:129], v228 offset:49152
v_fma_f32 v249, v249, v246, v247
v_mfma_f32_16x16x32_bf16 a[108:111], a[132:135], v[100:103], a[108:111]
ds_read_b64_tr_b16 a[130:131], v228 offset:57344
buffer_load_dwordx4 v242, s[44:47], s71 offen offset:2048 lds
v_mfma_f32_16x16x32_bf16 a[112:115], a[136:139], v[96:99], a[112:115]
ds_read_b64_tr_b16 a[132:133], v229 offset:49152
v_cvt_pk_bf16_f32 v108, v248, v249
v_mfma_f32_16x16x32_bf16 a[116:119], a[136:139], v[100:103], a[116:119]
ds_read_b64_tr_b16 a[134:135], v229 offset:57344
v_lshlrev_b32 v250, 16, v109
v_mfma_f32_16x16x32_bf16 a[120:123], a[140:143], v[96:99], a[120:123]
ds_read_b64_tr_b16 a[136:137], v230 offset:49152
v_and_b32 v251, s86, v109
v_fma_f32 v250, v250, v246, v247
v_fma_f32 v251, v251, v246, v247
v_cvt_pk_bf16_f32 v109, v250, v251
v_lshlrev_b32 v248, 16, v110
v_and_b32 v249, s86, v110
v_fma_f32 v248, v248, v246, v247
v_fma_f32 v249, v249, v246, v247
v_cvt_pk_bf16_f32 v110, v248, v249
v_lshlrev_b32 v250, 16, v111
v_and_b32 v251, s86, v111
v_fma_f32 v250, v250, v246, v247
v_fma_f32 v251, v251, v246, v247
v_cvt_pk_bf16_f32 v111, v250, v251
v_mfma_f32_16x16x32_bf16 a[124:127], a[140:143], v[100:103], a[124:127]
s_waitcnt lgkmcnt(5)
ds_read_b64_tr_b16 a[138:139], v230 offset:57344
v_add_u32 v243, 0xffff0000, v243
v_mfma_f32_16x16x32_bf16 a[0:3], a[144:147], v[104:107], a[0:3]
ds_read_b64_tr_b16 a[140:141], v231 offset:49152
v_accvgpr_read_b32 v252, a190
v_mfma_f32_16x16x32_bf16 a[4:7], a[144:147], v[108:111], a[4:7]
ds_read_b64_tr_b16 a[142:143], v231 offset:57344
v_add_u32 v253, 32768, v252
v_mfma_f32_16x16x32_bf16 a[8:11], a[148:151], v[104:107], a[8:11]
ds_read_b64_tr_b16 a[144:145], v224 offset:49408
v_accvgpr_read_b32 v232, a180
v_mfma_f32_16x16x32_bf16 a[12:15], a[148:151], v[108:111], a[12:15]
ds_read_b64_tr_b16 a[146:147], v224 offset:57600
v_accvgpr_read_b32 v234, a181
v_mfma_f32_16x16x32_bf16 a[16:19], a[152:155], v[104:107], a[16:19]
ds_read_b64_tr_b16 a[148:149], v225 offset:49408
v_accvgpr_read_b32 v235, a182
v_mfma_f32_16x16x32_bf16 a[20:23], a[152:155], v[108:111], a[20:23]
ds_read_b64_tr_b16 a[150:151], v225 offset:57600
v_mfma_f32_16x16x32_bf16 a[24:27], a[156:159], v[104:107], a[24:27]
ds_read_b64_tr_b16 a[152:153], v226 offset:49408
v_mfma_f32_16x16x32_bf16 a[28:31], a[156:159], v[108:111], a[28:31]
s_waitcnt lgkmcnt(5)
ds_read_b64_tr_b16 a[154:155], v226 offset:57600
v_accvgpr_read_b32 v248, a0
v_mfma_f32_16x16x32_bf16 a[32:35], a[128:131], v[104:107], a[32:35]
ds_read_b64_tr_b16 a[156:157], v227 offset:49408
v_accvgpr_read_b32 v254, a202
v_mfma_f32_16x16x32_bf16 a[36:39], a[128:131], v[108:111], a[36:39]
s_mov_b64 exec, 0x1fff
buffer_load_dword a174, v254, s[48:51], 0 offen offset:0
s_mov_b64 exec, -1
ds_read_b64_tr_b16 a[158:159], v227 offset:57600
v_mfma_f32_16x16x32_bf16 a[40:43], a[132:135], v[104:107], a[40:43]
ds_read_b64_tr_b16 a[128:129], v228 offset:49408
v_accvgpr_read_b32 v249, a1
v_mfma_f32_16x16x32_bf16 a[44:47], a[132:135], v[108:111], a[44:47]
ds_read_b64_tr_b16 a[130:131], v228 offset:57600
v_accvgpr_read_b32 v250, a8
v_mfma_f32_16x16x32_bf16 a[48:51], a[136:139], v[104:107], a[48:51]
ds_read_b64_tr_b16 a[132:133], v229 offset:49408
v_accvgpr_read_b32 v251, a9
v_mfma_f32_16x16x32_bf16 a[52:55], a[136:139], v[108:111], a[52:55]
ds_read_b64_tr_b16 a[134:135], v229 offset:57600
v_cvt_pk_bf16_f32 v248, v248, v249
v_mfma_f32_16x16x32_bf16 a[56:59], a[140:143], v[104:107], a[56:59]
ds_read_b64_tr_b16 a[136:137], v230 offset:49408
v_cvt_pk_bf16_f32 v250, v250, v251
v_mfma_f32_16x16x32_bf16 a[60:63], a[140:143], v[108:111], a[60:63]
s_waitcnt lgkmcnt(5)
ds_read_b64_tr_b16 a[138:139], v230 offset:57600
v_accvgpr_read_b32 v249, a2
v_mfma_f32_16x16x32_bf16 a[64:67], a[144:147], v[104:107], a[64:67]
ds_read_b64_tr_b16 a[140:141], v231 offset:49408
v_accvgpr_read_b32 v251, a3
v_mfma_f32_16x16x32_bf16 a[68:71], a[144:147], v[108:111], a[68:71]
s_waitcnt vmcnt(16)
ds_read_b64_tr_b16 a[142:143], v231 offset:57600
v_permlane16_swap_b32 v248, v250
v_mfma_f32_16x16x32_bf16 a[72:75], a[148:151], v[104:107], a[72:75]
s_barrier
ds_read_b64_tr_b16 a[144:145], v243 offset:0
v_accvgpr_read_b32 v224, a176
v_mfma_f32_16x16x32_bf16 a[76:79], a[148:151], v[108:111], a[76:79]
ds_read_b64_tr_b16 a[146:147], v243 offset:3584
v_xor_b32 v225, 32, v224
v_mfma_f32_16x16x32_bf16 a[80:83], a[152:155], v[104:107], a[80:83]
ds_read_b64_tr_b16 a[148:149], v243 offset:14464
v_xor_b32 v226, 64, v224
v_mfma_f32_16x16x32_bf16 a[84:87], a[152:155], v[108:111], a[84:87]
s_add_u32 s71, s73, 917504
ds_read_b64_tr_b16 a[150:151], v243 offset:18048
v_xor_b32 v227, 96, v224
v_mfma_f32_16x16x32_bf16 a[88:91], a[156:159], v[104:107], a[88:91]
s_add_u32 m0, s78, 0
ds_read_b64_tr_b16 a[152:153], v243 offset:32
v_xor_b32 v228, 128, v224
v_mfma_f32_16x16x32_bf16 a[92:95], a[156:159], v[108:111], a[92:95]
s_waitcnt lgkmcnt(5)
ds_read_b64_tr_b16 a[154:155], v243 offset:3616
buffer_load_dwordx4 v236, s[44:47], s71 offen offset:0 lds
v_mfma_f32_16x16x32_bf16 a[96:99], a[128:131], v[104:107], a[96:99]
ds_read_b64_tr_b16 a[156:157], v243 offset:14496
v_xor_b32 v229, 160, v224
v_mfma_f32_16x16x32_bf16 a[100:103], a[128:131], v[108:111], a[100:103]
ds_read_b64_tr_b16 a[158:159], v243 offset:18080
v_xor_b32 v230, 192, v224
v_mfma_f32_16x16x32_bf16 a[104:107], a[132:135], v[104:107], a[104:107]
ds_read_b64_tr_b16 a[128:129], v243 offset:64
v_xor_b32 v231, 224, v224
v_mfma_f32_16x16x32_bf16 a[108:111], a[132:135], v[108:111], a[108:111]
ds_read_b64_tr_b16 a[130:131], v243 offset:3648
v_cvt_pk_bf16_f32 v249, v249, v251
v_mfma_f32_16x16x32_bf16 a[112:115], a[136:139], v[104:107], a[112:115]
ds_read_b64_tr_b16 a[132:133], v243 offset:14528
v_accvgpr_write_b32 a0, v248
v_mfma_f32_16x16x32_bf16 a[116:119], a[136:139], v[108:111], a[116:119]
ds_read_b64_tr_b16 a[134:135], v243 offset:18112
buffer_load_dwordx4 v237, s[44:47], s71 offen offset:1024 lds
v_mfma_f32_16x16x32_bf16 a[120:123], a[140:143], v[104:107], a[120:123]
ds_read_b64_tr_b16 a[136:137], v243 offset:96
v_accvgpr_write_b32 a2, v250
v_mfma_f32_16x16x32_bf16 a[124:127], a[140:143], v[108:111], a[124:127]
s_waitcnt lgkmcnt(5)
ds_read_b64_tr_b16 a[138:139], v243 offset:3680
v_accvgpr_read_b32 v248, a10
v_mfma_f32_16x16x32_bf16 v[112:115], a[144:147], v[16:19], v[112:115]
ds_read_b64_tr_b16 a[140:141], v243 offset:14560
v_accvgpr_read_b32 v244, a177
v_mfma_f32_16x16x32_bf16 v[116:119], a[144:147], v[20:23], v[116:119]
ds_read_b64_tr_b16 a[142:143], v243 offset:18144
v_xor_b32 v245, 64, v244
v_mfma_f32_16x16x32_bf16 v[120:123], a[148:151], v[16:19], v[120:123]
ds_read_b64_tr_b16 a[144:145], v243 offset:128
v_xor_b32 v246, 128, v244
v_mfma_f32_16x16x32_bf16 v[124:127], a[148:151], v[20:23], v[124:127]
ds_read_b64_tr_b16 a[146:147], v243 offset:3712
buffer_load_dwordx4 v238, s[44:47], s71 offen offset:2048 lds
v_mfma_f32_16x16x32_bf16 v[128:131], a[152:155], v[16:19], v[128:131]
ds_read_b64_tr_b16 a[148:149], v243 offset:14592
v_xor_b32 v247, 192, v244
v_mfma_f32_16x16x32_bf16 v[132:135], a[152:155], v[20:23], v[132:135]
ds_read_b64_tr_b16 a[150:151], v243 offset:18176
v_accvgpr_read_b32 v250, a11
v_mfma_f32_16x16x32_bf16 v[136:139], a[156:159], v[16:19], v[136:139]
ds_read_b64_tr_b16 a[152:153], v243 offset:160
v_cvt_pk_bf16_f32 v251, v248, v250
v_mfma_f32_16x16x32_bf16 v[140:143], a[156:159], v[20:23], v[140:143]
s_waitcnt lgkmcnt(5)
ds_read_b64_tr_b16 a[154:155], v243 offset:3744
v_permlane16_swap_b32 v249, v251
v_mfma_f32_16x16x32_bf16 v[144:147], a[128:131], v[16:19], v[144:147]
ds_read_b64_tr_b16 a[156:157], v243 offset:14624
v_accvgpr_write_b32 a1, v249
v_mfma_f32_16x16x32_bf16 v[148:151], a[128:131], v[20:23], v[148:151]
ds_read_b64_tr_b16 a[158:159], v243 offset:18208
buffer_load_dwordx4 v239, s[44:47], s71 offen offset:3072 lds
v_mfma_f32_16x16x32_bf16 v[152:155], a[132:135], v[16:19], v[152:155]
ds_read_b64_tr_b16 a[128:129], v243 offset:192
v_accvgpr_write_b32 a3, v251
v_mfma_f32_16x16x32_bf16 v[156:159], a[132:135], v[20:23], v[156:159]
ds_read_b64_tr_b16 a[130:131], v243 offset:3776
v_accvgpr_read_b32 v248, a4
v_mfma_f32_16x16x32_bf16 v[160:163], a[136:139], v[16:19], v[160:163]
ds_read_b64_tr_b16 a[132:133], v243 offset:14656
v_accvgpr_read_b32 v249, a5
v_mfma_f32_16x16x32_bf16 v[164:167], a[136:139], v[20:23], v[164:167]
ds_read_b64_tr_b16 a[134:135], v243 offset:18240
v_accvgpr_read_b32 v250, a12
v_mfma_f32_16x16x32_bf16 v[168:171], a[140:143], v[16:19], v[168:171]
s_add_u32 m0, s78, 4096
ds_read_b64_tr_b16 a[136:137], v243 offset:7168
v_accvgpr_read_b32 v251, a13
v_mfma_f32_16x16x32_bf16 v[172:175], a[140:143], v[20:23], v[172:175]
s_waitcnt lgkmcnt(5)
ds_read_b64_tr_b16 a[138:139], v243 offset:10752
buffer_load_dwordx4 v240, s[44:47], s71 offen offset:0 lds
v_mfma_f32_16x16x32_bf16 v[176:179], a[144:147], v[16:19], v[176:179]
ds_read_b64_tr_b16 a[140:141], v243 offset:21632
v_cvt_pk_bf16_f32 v248, v248, v249
v_mfma_f32_16x16x32_bf16 v[180:183], a[144:147], v[20:23], v[180:183]
ds_read_b64_tr_b16 a[142:143], v243 offset:25216
v_cvt_pk_bf16_f32 v250, v250, v251
v_mfma_f32_16x16x32_bf16 v[184:187], a[148:151], v[16:19], v[184:187]
ds_read_b64_tr_b16 a[144:145], v243 offset:7200
v_accvgpr_read_b32 v249, a6
v_mfma_f32_16x16x32_bf16 v[188:191], a[148:151], v[20:23], v[188:191]
ds_read_b64_tr_b16 a[146:147], v243 offset:10784
v_accvgpr_read_b32 v251, a7
v_mfma_f32_16x16x32_bf16 v[192:195], a[152:155], v[16:19], v[192:195]
ds_read_b64_tr_b16 a[148:149], v243 offset:21664
v_permlane16_swap_b32 v248, v250
v_mfma_f32_16x16x32_bf16 v[196:199], a[152:155], v[20:23], v[196:199]
ds_read_b64_tr_b16 a[150:151], v243 offset:25248
buffer_load_dwordx4 v241, s[44:47], s71 offen offset:1024 lds
v_mfma_f32_16x16x32_bf16 v[200:203], a[156:159], v[16:19], v[200:203]
ds_read_b64_tr_b16 a[152:153], v243 offset:7232
v_cvt_pk_bf16_f32 v249, v249, v251
v_mfma_f32_16x16x32_bf16 v[204:207], a[156:159], v[20:23], v[204:207]
s_waitcnt lgkmcnt(5)
ds_read_b64_tr_b16 a[154:155], v243 offset:10816
v_accvgpr_write_b32 a4, v248
v_mfma_f32_16x16x32_bf16 v[208:211], a[128:131], v[16:19], v[208:211]
ds_read_b64_tr_b16 a[156:157], v243 offset:21696
v_accvgpr_write_b32 a6, v250
v_mfma_f32_16x16x32_bf16 v[212:215], a[128:131], v[20:23], v[212:215]
ds_read_b64_tr_b16 a[158:159], v243 offset:25280
v_accvgpr_read_b32 v248, a14
v_mfma_f32_16x16x32_bf16 v[216:219], a[132:135], v[16:19], v[216:219]
ds_read_b64_tr_b16 a[128:129], v243 offset:7264
v_accvgpr_read_b32 v250, a15
v_mfma_f32_16x16x32_bf16 v[220:223], a[132:135], v[20:23], v[220:223]
ds_read_b64_tr_b16 a[130:131], v243 offset:10848
buffer_load_dwordx4 v242, s[44:47], s71 offen offset:2048 lds
v_mfma_f32_16x16x32_bf16 v[112:115], a[136:139], v[24:27], v[112:115]
ds_read_b64_tr_b16 a[132:133], v243 offset:21728
v_cvt_pk_bf16_f32 v251, v248, v250
v_mfma_f32_16x16x32_bf16 v[116:119], a[136:139], v[28:31], v[116:119]
ds_read_b64_tr_b16 a[134:135], v243 offset:25312
v_permlane16_swap_b32 v249, v251
v_mfma_f32_16x16x32_bf16 v[120:123], a[140:143], v[24:27], v[120:123]
ds_read_b64_tr_b16 a[136:137], v243 offset:7296
v_accvgpr_write_b32 a5, v249
v_mfma_f32_16x16x32_bf16 v[124:127], a[140:143], v[28:31], v[124:127]
s_waitcnt lgkmcnt(5)
ds_read_b64_tr_b16 a[138:139], v243 offset:10880
v_accvgpr_write_b32 a7, v251
v_mfma_f32_16x16x32_bf16 v[128:131], a[144:147], v[24:27], v[128:131]
ds_read_b64_tr_b16 a[140:141], v243 offset:21760
v_accvgpr_read_b32 v248, a16
v_mfma_f32_16x16x32_bf16 v[132:135], a[144:147], v[28:31], v[132:135]
ds_read_b64_tr_b16 a[142:143], v243 offset:25344
v_accvgpr_read_b32 v249, a17
v_mfma_f32_16x16x32_bf16 v[136:139], a[148:151], v[24:27], v[136:139]
ds_read_b64_tr_b16 a[144:145], v243 offset:7328
v_accvgpr_read_b32 v250, a24
v_mfma_f32_16x16x32_bf16 v[140:143], a[148:151], v[28:31], v[140:143]
ds_read_b64_tr_b16 a[146:147], v243 offset:10912
v_accvgpr_read_b32 v251, a25
v_mfma_f32_16x16x32_bf16 v[144:147], a[152:155], v[24:27], v[144:147]
ds_read_b64_tr_b16 a[148:149], v243 offset:21792
v_cvt_pk_bf16_f32 v248, v248, v249
v_mfma_f32_16x16x32_bf16 v[148:151], a[152:155], v[28:31], v[148:151]
ds_read_b64_tr_b16 a[150:151], v243 offset:25376
v_cvt_pk_bf16_f32 v250, v250, v251
v_mfma_f32_16x16x32_bf16 v[152:155], a[156:159], v[24:27], v[152:155]
ds_read_b64_tr_b16 a[152:153], v243 offset:7360
v_accvgpr_read_b32 v249, a18
v_mfma_f32_16x16x32_bf16 v[156:159], a[156:159], v[28:31], v[156:159]
s_waitcnt lgkmcnt(5)
ds_read_b64_tr_b16 a[154:155], v243 offset:10944
v_accvgpr_read_b32 v251, a19
v_mfma_f32_16x16x32_bf16 v[160:163], a[128:131], v[24:27], v[160:163]
ds_read_b64_tr_b16 a[156:157], v243 offset:21824
v_permlane16_swap_b32 v248, v250
v_mfma_f32_16x16x32_bf16 v[164:167], a[128:131], v[28:31], v[164:167]
s_waitcnt vmcnt(15)
ds_read_b64_tr_b16 a[158:159], v243 offset:25408
v_cvt_pk_bf16_f32 v249, v249, v251
v_mfma_f32_16x16x32_bf16 v[168:171], a[132:135], v[24:27], v[168:171]
s_barrier
ds_read_b64_tr_b16 a[128:129], v243 offset:32768
v_accvgpr_write_b32 a16, v248
v_mfma_f32_16x16x32_bf16 v[172:175], a[132:135], v[28:31], v[172:175]
ds_read_b64_tr_b16 a[130:131], v243 offset:36352
v_accvgpr_write_b32 a18, v250
v_mfma_f32_16x16x32_bf16 v[176:179], a[136:139], v[24:27], v[176:179]
ds_read_b64_tr_b16 a[132:133], v243 offset:47232
v_accvgpr_read_b32 v248, a26
v_mfma_f32_16x16x32_bf16 v[180:183], a[136:139], v[28:31], v[180:183]
s_add_u32 s71, s73, 1146880
ds_read_b64_tr_b16 a[134:135], v243 offset:50816
v_accvgpr_read_b32 v250, a27
v_mfma_f32_16x16x32_bf16 v[184:187], a[140:143], v[24:27], v[184:187]
s_add_u32 m0, s78, 32768
ds_read_b64_tr_b16 a[136:137], v243 offset:32800
v_cvt_pk_bf16_f32 v251, v248, v250
v_mfma_f32_16x16x32_bf16 v[188:191], a[140:143], v[28:31], v[188:191]
s_waitcnt lgkmcnt(5)
ds_read_b64_tr_b16 a[138:139], v243 offset:36384
buffer_load_dwordx4 v236, s[44:47], s71 offen offset:0 lds
v_mfma_f32_16x16x32_bf16 v[192:195], a[144:147], v[24:27], v[192:195]
ds_read_b64_tr_b16 a[140:141], v243 offset:47264
v_permlane16_swap_b32 v249, v251
v_mfma_f32_16x16x32_bf16 v[196:199], a[144:147], v[28:31], v[196:199]
ds_read_b64_tr_b16 a[142:143], v243 offset:50848
v_accvgpr_write_b32 a17, v249
v_mfma_f32_16x16x32_bf16 v[200:203], a[148:151], v[24:27], v[200:203]
ds_read_b64_tr_b16 a[144:145], v243 offset:32832
v_accvgpr_write_b32 a19, v251
v_mfma_f32_16x16x32_bf16 v[204:207], a[148:151], v[28:31], v[204:207]
ds_read_b64_tr_b16 a[146:147], v243 offset:36416
v_accvgpr_read_b32 v248, a20
v_mfma_f32_16x16x32_bf16 v[208:211], a[152:155], v[24:27], v[208:211]
ds_read_b64_tr_b16 a[148:149], v243 offset:47296
v_accvgpr_read_b32 v249, a21
v_mfma_f32_16x16x32_bf16 v[212:215], a[152:155], v[28:31], v[212:215]
ds_read_b64_tr_b16 a[150:151], v243 offset:50880
buffer_load_dwordx4 v237, s[44:47], s71 offen offset:1024 lds
v_mfma_f32_16x16x32_bf16 v[216:219], a[156:159], v[24:27], v[216:219]
ds_read_b64_tr_b16 a[152:153], v243 offset:32864
v_accvgpr_read_b32 v250, a28
v_mfma_f32_16x16x32_bf16 v[220:223], a[156:159], v[28:31], v[220:223]
s_waitcnt lgkmcnt(5)
ds_read_b64_tr_b16 a[154:155], v243 offset:36448
v_accvgpr_read_b32 v251, a29
v_mfma_f32_16x16x32_bf16 v[112:115], a[128:131], v[32:35], v[112:115]
ds_read_b64_tr_b16 a[156:157], v243 offset:47328
v_cvt_pk_bf16_f32 v248, v248, v249
v_mfma_f32_16x16x32_bf16 v[116:119], a[128:131], v[36:39], v[116:119]
ds_read_b64_tr_b16 a[158:159], v243 offset:50912
v_cvt_pk_bf16_f32 v250, v250, v251
v_mfma_f32_16x16x32_bf16 v[120:123], a[132:135], v[32:35], v[120:123]
ds_read_b64_tr_b16 a[128:129], v243 offset:32896
v_accvgpr_read_b32 v249, a22
v_mfma_f32_16x16x32_bf16 v[124:127], a[132:135], v[36:39], v[124:127]
ds_read_b64_tr_b16 a[130:131], v243 offset:36480
buffer_load_dwordx4 v238, s[44:47], s71 offen offset:2048 lds
v_mfma_f32_16x16x32_bf16 v[128:131], a[136:139], v[32:35], v[128:131]
ds_read_b64_tr_b16 a[132:133], v243 offset:47360
v_accvgpr_read_b32 v251, a23
v_mfma_f32_16x16x32_bf16 v[132:135], a[136:139], v[36:39], v[132:135]
ds_read_b64_tr_b16 a[134:135], v243 offset:50944
v_permlane16_swap_b32 v248, v250
v_mfma_f32_16x16x32_bf16 v[136:139], a[140:143], v[32:35], v[136:139]
ds_read_b64_tr_b16 a[136:137], v243 offset:32928
v_cvt_pk_bf16_f32 v249, v249, v251
v_mfma_f32_16x16x32_bf16 v[140:143], a[140:143], v[36:39], v[140:143]
s_waitcnt lgkmcnt(5)
ds_read_b64_tr_b16 a[138:139], v243 offset:36512
v_accvgpr_write_b32 a20, v248
v_mfma_f32_16x16x32_bf16 v[144:147], a[144:147], v[32:35], v[144:147]
ds_read_b64_tr_b16 a[140:141], v243 offset:47392
v_accvgpr_write_b32 a22, v250
v_mfma_f32_16x16x32_bf16 v[148:151], a[144:147], v[36:39], v[148:151]
ds_read_b64_tr_b16 a[142:143], v243 offset:50976
buffer_load_dwordx4 v239, s[44:47], s71 offen offset:3072 lds
v_mfma_f32_16x16x32_bf16 v[152:155], a[148:151], v[32:35], v[152:155]
ds_read_b64_tr_b16 a[144:145], v243 offset:32960
v_accvgpr_read_b32 v248, a30
v_mfma_f32_16x16x32_bf16 v[156:159], a[148:151], v[36:39], v[156:159]
ds_read_b64_tr_b16 a[146:147], v243 offset:36544
v_accvgpr_read_b32 v250, a31
v_mfma_f32_16x16x32_bf16 v[160:163], a[152:155], v[32:35], v[160:163]
ds_read_b64_tr_b16 a[148:149], v243 offset:47424
v_cvt_pk_bf16_f32 v251, v248, v250
v_mfma_f32_16x16x32_bf16 v[164:167], a[152:155], v[36:39], v[164:167]
ds_read_b64_tr_b16 a[150:151], v243 offset:51008
v_permlane16_swap_b32 v249, v251
v_mfma_f32_16x16x32_bf16 v[168:171], a[156:159], v[32:35], v[168:171]
s_add_u32 m0, s78, 36864
ds_read_b64_tr_b16 a[152:153], v243 offset:39936
v_accvgpr_write_b32 a21, v249
v_mfma_f32_16x16x32_bf16 v[172:175], a[156:159], v[36:39], v[172:175]
s_waitcnt lgkmcnt(5)
ds_read_b64_tr_b16 a[154:155], v243 offset:43520
buffer_load_dwordx4 v240, s[44:47], s71 offen offset:0 lds
v_mfma_f32_16x16x32_bf16 v[176:179], a[128:131], v[32:35], v[176:179]
ds_read_b64_tr_b16 a[156:157], v243 offset:54400
v_accvgpr_write_b32 a23, v251
v_mfma_f32_16x16x32_bf16 v[180:183], a[128:131], v[36:39], v[180:183]
ds_read_b64_tr_b16 a[158:159], v243 offset:57984
v_accvgpr_read_b32 v248, a32
v_mfma_f32_16x16x32_bf16 v[184:187], a[132:135], v[32:35], v[184:187]
ds_read_b64_tr_b16 a[128:129], v243 offset:39968
v_accvgpr_read_b32 v249, a33
v_mfma_f32_16x16x32_bf16 v[188:191], a[132:135], v[36:39], v[188:191]
ds_read_b64_tr_b16 a[130:131], v243 offset:43552
v_accvgpr_read_b32 v250, a40
v_mfma_f32_16x16x32_bf16 v[192:195], a[136:139], v[32:35], v[192:195]
ds_read_b64_tr_b16 a[132:133], v243 offset:54432
v_accvgpr_read_b32 v251, a41
v_mfma_f32_16x16x32_bf16 v[196:199], a[136:139], v[36:39], v[196:199]
ds_read_b64_tr_b16 a[134:135], v243 offset:58016
buffer_load_dwordx4 v241, s[44:47], s71 offen offset:1024 lds
v_mfma_f32_16x16x32_bf16 v[200:203], a[140:143], v[32:35], v[200:203]
ds_read_b64_tr_b16 a[136:137], v243 offset:40000
v_cvt_pk_bf16_f32 v248, v248, v249
v_mfma_f32_16x16x32_bf16 v[204:207], a[140:143], v[36:39], v[204:207]
s_waitcnt lgkmcnt(5)
ds_read_b64_tr_b16 a[138:139], v243 offset:43584
v_cvt_pk_bf16_f32 v250, v250, v251
v_mfma_f32_16x16x32_bf16 v[208:211], a[144:147], v[32:35], v[208:211]
ds_read_b64_tr_b16 a[140:141], v243 offset:54464
v_accvgpr_read_b32 v249, a34
v_mfma_f32_16x16x32_bf16 v[212:215], a[144:147], v[36:39], v[212:215]
ds_read_b64_tr_b16 a[142:143], v243 offset:58048
v_accvgpr_read_b32 v251, a35
v_mfma_f32_16x16x32_bf16 v[216:219], a[148:151], v[32:35], v[216:219]
ds_read_b64_tr_b16 a[144:145], v243 offset:40032
v_permlane16_swap_b32 v248, v250
v_mfma_f32_16x16x32_bf16 v[220:223], a[148:151], v[36:39], v[220:223]
ds_read_b64_tr_b16 a[146:147], v243 offset:43616
buffer_load_dwordx4 v242, s[44:47], s71 offen offset:2048 lds
v_mfma_f32_16x16x32_bf16 v[112:115], a[152:155], v[40:43], v[112:115]
ds_read_b64_tr_b16 a[148:149], v243 offset:54496
v_cvt_pk_bf16_f32 v249, v249, v251
v_mfma_f32_16x16x32_bf16 v[116:119], a[152:155], v[44:47], v[116:119]
ds_read_b64_tr_b16 a[150:151], v243 offset:58080
v_accvgpr_write_b32 a32, v248
v_mfma_f32_16x16x32_bf16 v[120:123], a[156:159], v[40:43], v[120:123]
ds_read_b64_tr_b16 a[152:153], v243 offset:40064
v_accvgpr_write_b32 a34, v250
v_mfma_f32_16x16x32_bf16 v[124:127], a[156:159], v[44:47], v[124:127]
s_waitcnt lgkmcnt(5)
ds_read_b64_tr_b16 a[154:155], v243 offset:43648
v_accvgpr_read_b32 v248, a42
v_mfma_f32_16x16x32_bf16 v[128:131], a[128:131], v[40:43], v[128:131]
ds_read_b64_tr_b16 a[156:157], v243 offset:54528
v_accvgpr_read_b32 v250, a43
v_mfma_f32_16x16x32_bf16 v[132:135], a[128:131], v[44:47], v[132:135]
ds_read_b64_tr_b16 a[158:159], v243 offset:58112
v_cvt_pk_bf16_f32 v251, v248, v250
v_mfma_f32_16x16x32_bf16 v[136:139], a[132:135], v[40:43], v[136:139]
ds_read_b64_tr_b16 a[128:129], v243 offset:40096
v_permlane16_swap_b32 v249, v251
v_mfma_f32_16x16x32_bf16 v[140:143], a[132:135], v[44:47], v[140:143]
ds_read_b64_tr_b16 a[130:131], v243 offset:43680
v_accvgpr_write_b32 a33, v249
v_mfma_f32_16x16x32_bf16 v[144:147], a[136:139], v[40:43], v[144:147]
ds_read_b64_tr_b16 a[132:133], v243 offset:54560
v_accvgpr_write_b32 a35, v251
v_mfma_f32_16x16x32_bf16 v[148:151], a[136:139], v[44:47], v[148:151]
ds_read_b64_tr_b16 a[134:135], v243 offset:58144
v_accvgpr_read_b32 v248, a36
v_mfma_f32_16x16x32_bf16 v[152:155], a[140:143], v[40:43], v[152:155]
ds_read_b64_tr_b16 a[136:137], v243 offset:40128
v_accvgpr_read_b32 v249, a37
v_mfma_f32_16x16x32_bf16 v[156:159], a[140:143], v[44:47], v[156:159]
s_waitcnt lgkmcnt(5)
ds_read_b64_tr_b16 a[138:139], v243 offset:43712
v_accvgpr_read_b32 v250, a44
v_mfma_f32_16x16x32_bf16 v[160:163], a[144:147], v[40:43], v[160:163]
s_waitcnt vmcnt(15)
ds_read_b64_tr_b16 a[140:141], v243 offset:54592
v_accvgpr_read_b32 v254, a203
v_mfma_f32_16x16x32_bf16 v[164:167], a[144:147], v[44:47], v[164:167]
s_mov_b64 exec, 0xfffffff
buffer_load_dword a174, v254, s[60:63], 0 offen offset:0
s_mov_b64 exec, -1
ds_read_b64_tr_b16 a[142:143], v243 offset:58176
v_add_u32 v243, 0x10000, v243
v_mfma_f32_16x16x32_bf16 v[168:171], a[148:151], v[40:43], v[168:171]
s_barrier
ds_read_b64_tr_b16 a[144:145], v243 offset:0
v_accvgpr_read_b32 v251, a45
v_mfma_f32_16x16x32_bf16 v[172:175], a[148:151], v[44:47], v[172:175]
ds_read_b64_tr_b16 a[146:147], v243 offset:3584
v_cvt_pk_bf16_f32 v248, v248, v249
v_mfma_f32_16x16x32_bf16 v[176:179], a[152:155], v[40:43], v[176:179]
ds_read_b64_tr_b16 a[148:149], v243 offset:14464
v_cvt_pk_bf16_f32 v250, v250, v251
v_mfma_f32_16x16x32_bf16 v[180:183], a[152:155], v[44:47], v[180:183]
s_add_u32 s71, s73, 1376256
ds_read_b64_tr_b16 a[150:151], v243 offset:18048
v_accvgpr_read_b32 v249, a38
v_mfma_f32_16x16x32_bf16 v[184:187], a[156:159], v[40:43], v[184:187]
s_add_u32 m0, s78, 65536
ds_read_b64_tr_b16 a[152:153], v243 offset:32
v_accvgpr_read_b32 v251, a39
v_mfma_f32_16x16x32_bf16 v[188:191], a[156:159], v[44:47], v[188:191]
s_waitcnt lgkmcnt(5)
ds_read_b64_tr_b16 a[154:155], v243 offset:3616
buffer_load_dwordx4 v236, s[44:47], s71 offen offset:0 lds
v_mfma_f32_16x16x32_bf16 v[192:195], a[128:131], v[40:43], v[192:195]
ds_read_b64_tr_b16 a[156:157], v243 offset:14496
v_permlane16_swap_b32 v248, v250
v_mfma_f32_16x16x32_bf16 v[196:199], a[128:131], v[44:47], v[196:199]
ds_read_b64_tr_b16 a[158:159], v243 offset:18080
v_cvt_pk_bf16_f32 v249, v249, v251
v_mfma_f32_16x16x32_bf16 v[200:203], a[132:135], v[40:43], v[200:203]
ds_read_b64_tr_b16 a[128:129], v243 offset:64
v_accvgpr_write_b32 a36, v248
v_mfma_f32_16x16x32_bf16 v[204:207], a[132:135], v[44:47], v[204:207]
ds_read_b64_tr_b16 a[130:131], v243 offset:3648
v_accvgpr_write_b32 a38, v250
v_mfma_f32_16x16x32_bf16 v[208:211], a[136:139], v[40:43], v[208:211]
ds_read_b64_tr_b16 a[132:133], v243 offset:14528
v_accvgpr_read_b32 v248, a46
v_mfma_f32_16x16x32_bf16 v[212:215], a[136:139], v[44:47], v[212:215]
ds_read_b64_tr_b16 a[134:135], v243 offset:18112
buffer_load_dwordx4 v237, s[44:47], s71 offen offset:1024 lds
v_mfma_f32_16x16x32_bf16 v[216:219], a[140:143], v[40:43], v[216:219]
ds_read_b64_tr_b16 a[136:137], v243 offset:96
v_accvgpr_read_b32 v250, a47
v_mfma_f32_16x16x32_bf16 v[220:223], a[140:143], v[44:47], v[220:223]
s_waitcnt lgkmcnt(5)
ds_read_b64_tr_b16 a[138:139], v243 offset:3680
v_cvt_pk_bf16_f32 v251, v248, v250
v_mfma_f32_16x16x32_bf16 v[112:115], a[144:147], v[48:51], v[112:115]
ds_read_b64_tr_b16 a[140:141], v243 offset:14560
v_permlane16_swap_b32 v249, v251
v_mfma_f32_16x16x32_bf16 v[116:119], a[144:147], v[52:55], v[116:119]
ds_read_b64_tr_b16 a[142:143], v243 offset:18144
v_accvgpr_write_b32 a37, v249
v_mfma_f32_16x16x32_bf16 v[120:123], a[148:151], v[48:51], v[120:123]
ds_read_b64_tr_b16 a[144:145], v243 offset:128
v_accvgpr_write_b32 a39, v251
v_mfma_f32_16x16x32_bf16 v[124:127], a[148:151], v[52:55], v[124:127]
ds_read_b64_tr_b16 a[146:147], v243 offset:3712
buffer_load_dwordx4 v238, s[44:47], s71 offen offset:2048 lds
v_mfma_f32_16x16x32_bf16 v[128:131], a[152:155], v[48:51], v[128:131]
ds_read_b64_tr_b16 a[148:149], v243 offset:14592
v_accvgpr_read_b32 v248, a48
v_mfma_f32_16x16x32_bf16 v[132:135], a[152:155], v[52:55], v[132:135]
ds_read_b64_tr_b16 a[150:151], v243 offset:18176
v_accvgpr_read_b32 v249, a49
v_mfma_f32_16x16x32_bf16 v[136:139], a[156:159], v[48:51], v[136:139]
ds_read_b64_tr_b16 a[152:153], v243 offset:160
v_accvgpr_read_b32 v250, a56
v_mfma_f32_16x16x32_bf16 v[140:143], a[156:159], v[52:55], v[140:143]
s_waitcnt lgkmcnt(5)
ds_read_b64_tr_b16 a[154:155], v243 offset:3744
v_accvgpr_read_b32 v251, a57
v_mfma_f32_16x16x32_bf16 v[144:147], a[128:131], v[48:51], v[144:147]
ds_read_b64_tr_b16 a[156:157], v243 offset:14624
v_cvt_pk_bf16_f32 v248, v248, v249
v_mfma_f32_16x16x32_bf16 v[148:151], a[128:131], v[52:55], v[148:151]
ds_read_b64_tr_b16 a[158:159], v243 offset:18208
buffer_load_dwordx4 v239, s[44:47], s71 offen offset:3072 lds
v_mfma_f32_16x16x32_bf16 v[152:155], a[132:135], v[48:51], v[152:155]
ds_read_b64_tr_b16 a[128:129], v243 offset:192
v_cvt_pk_bf16_f32 v250, v250, v251
v_mfma_f32_16x16x32_bf16 v[156:159], a[132:135], v[52:55], v[156:159]
ds_read_b64_tr_b16 a[130:131], v243 offset:3776
v_accvgpr_read_b32 v249, a50
v_mfma_f32_16x16x32_bf16 v[160:163], a[136:139], v[48:51], v[160:163]
ds_read_b64_tr_b16 a[132:133], v243 offset:14656
v_accvgpr_read_b32 v251, a51
v_mfma_f32_16x16x32_bf16 v[164:167], a[136:139], v[52:55], v[164:167]
ds_read_b64_tr_b16 a[134:135], v243 offset:18240
v_permlane16_swap_b32 v248, v250
v_mfma_f32_16x16x32_bf16 v[168:171], a[140:143], v[48:51], v[168:171]
s_add_u32 m0, s78, 69632
ds_read_b64_tr_b16 a[136:137], v243 offset:7168
v_cvt_pk_bf16_f32 v249, v249, v251
v_mfma_f32_16x16x32_bf16 v[172:175], a[140:143], v[52:55], v[172:175]
s_waitcnt lgkmcnt(5)
ds_read_b64_tr_b16 a[138:139], v243 offset:10752
buffer_load_dwordx4 v240, s[44:47], s71 offen offset:0 lds
v_mfma_f32_16x16x32_bf16 v[176:179], a[144:147], v[48:51], v[176:179]
ds_read_b64_tr_b16 a[140:141], v243 offset:21632
v_accvgpr_write_b32 a48, v248
v_mfma_f32_16x16x32_bf16 v[180:183], a[144:147], v[52:55], v[180:183]
ds_read_b64_tr_b16 a[142:143], v243 offset:25216
v_accvgpr_write_b32 a50, v250
v_mfma_f32_16x16x32_bf16 v[184:187], a[148:151], v[48:51], v[184:187]
ds_read_b64_tr_b16 a[144:145], v243 offset:7200
v_accvgpr_read_b32 v248, a58
v_mfma_f32_16x16x32_bf16 v[188:191], a[148:151], v[52:55], v[188:191]
ds_read_b64_tr_b16 a[146:147], v243 offset:10784
v_accvgpr_read_b32 v250, a59
v_mfma_f32_16x16x32_bf16 v[192:195], a[152:155], v[48:51], v[192:195]
ds_read_b64_tr_b16 a[148:149], v243 offset:21664
v_cvt_pk_bf16_f32 v251, v248, v250
v_mfma_f32_16x16x32_bf16 v[196:199], a[152:155], v[52:55], v[196:199]
ds_read_b64_tr_b16 a[150:151], v243 offset:25248
buffer_load_dwordx4 v241, s[44:47], s71 offen offset:1024 lds
v_mfma_f32_16x16x32_bf16 v[200:203], a[156:159], v[48:51], v[200:203]
ds_read_b64_tr_b16 a[152:153], v243 offset:7232
v_permlane16_swap_b32 v249, v251
v_mfma_f32_16x16x32_bf16 v[204:207], a[156:159], v[52:55], v[204:207]
s_waitcnt lgkmcnt(5)
ds_read_b64_tr_b16 a[154:155], v243 offset:10816
v_accvgpr_write_b32 a49, v249
v_mfma_f32_16x16x32_bf16 v[208:211], a[128:131], v[48:51], v[208:211]
ds_read_b64_tr_b16 a[156:157], v243 offset:21696
v_accvgpr_write_b32 a51, v251
v_mfma_f32_16x16x32_bf16 v[212:215], a[128:131], v[52:55], v[212:215]
ds_read_b64_tr_b16 a[158:159], v243 offset:25280
v_accvgpr_read_b32 v248, a52
v_mfma_f32_16x16x32_bf16 v[216:219], a[132:135], v[48:51], v[216:219]
ds_read_b64_tr_b16 a[128:129], v243 offset:7264
v_accvgpr_read_b32 v249, a53
v_mfma_f32_16x16x32_bf16 v[220:223], a[132:135], v[52:55], v[220:223]
ds_read_b64_tr_b16 a[130:131], v243 offset:10848
buffer_load_dwordx4 v242, s[44:47], s71 offen offset:2048 lds
v_mfma_f32_16x16x32_bf16 v[112:115], a[136:139], v[56:59], v[112:115]
ds_read_b64_tr_b16 a[132:133], v243 offset:21728
v_accvgpr_read_b32 v250, a60
v_mfma_f32_16x16x32_bf16 v[116:119], a[136:139], v[60:63], v[116:119]
ds_read_b64_tr_b16 a[134:135], v243 offset:25312
v_accvgpr_read_b32 v251, a61
v_mfma_f32_16x16x32_bf16 v[120:123], a[140:143], v[56:59], v[120:123]
ds_read_b64_tr_b16 a[136:137], v243 offset:7296
v_cvt_pk_bf16_f32 v248, v248, v249
v_mfma_f32_16x16x32_bf16 v[124:127], a[140:143], v[60:63], v[124:127]
s_waitcnt lgkmcnt(5)
ds_read_b64_tr_b16 a[138:139], v243 offset:10880
v_cvt_pk_bf16_f32 v250, v250, v251
v_mfma_f32_16x16x32_bf16 v[128:131], a[144:147], v[56:59], v[128:131]
ds_read_b64_tr_b16 a[140:141], v243 offset:21760
v_accvgpr_read_b32 v249, a54
v_mfma_f32_16x16x32_bf16 v[132:135], a[144:147], v[60:63], v[132:135]
ds_read_b64_tr_b16 a[142:143], v243 offset:25344
v_accvgpr_read_b32 v251, a55
v_mfma_f32_16x16x32_bf16 v[136:139], a[148:151], v[56:59], v[136:139]
ds_read_b64_tr_b16 a[144:145], v243 offset:7328
v_permlane16_swap_b32 v248, v250
v_mfma_f32_16x16x32_bf16 v[140:143], a[148:151], v[60:63], v[140:143]
ds_read_b64_tr_b16 a[146:147], v243 offset:10912
v_cvt_pk_bf16_f32 v249, v249, v251
v_mfma_f32_16x16x32_bf16 v[144:147], a[152:155], v[56:59], v[144:147]
ds_read_b64_tr_b16 a[148:149], v243 offset:21792
v_accvgpr_write_b32 a52, v248
v_mfma_f32_16x16x32_bf16 v[148:151], a[152:155], v[60:63], v[148:151]
ds_read_b64_tr_b16 a[150:151], v243 offset:25376
v_accvgpr_write_b32 a54, v250
v_mfma_f32_16x16x32_bf16 v[152:155], a[156:159], v[56:59], v[152:155]
ds_read_b64_tr_b16 a[152:153], v243 offset:7360
v_accvgpr_read_b32 v248, a62
v_mfma_f32_16x16x32_bf16 v[156:159], a[156:159], v[60:63], v[156:159]
s_waitcnt lgkmcnt(5)
ds_read_b64_tr_b16 a[154:155], v243 offset:10944
v_accvgpr_read_b32 v250, a63
v_mfma_f32_16x16x32_bf16 v[160:163], a[128:131], v[56:59], v[160:163]
ds_read_b64_tr_b16 a[156:157], v243 offset:21824
v_cvt_pk_bf16_f32 v251, v248, v250
v_mfma_f32_16x16x32_bf16 v[164:167], a[128:131], v[60:63], v[164:167]
s_waitcnt vmcnt(15)
ds_read_b64_tr_b16 a[158:159], v243 offset:25408
v_add_u32 v243, 0xfffe0000, v243
v_mfma_f32_16x16x32_bf16 v[168:171], a[132:135], v[56:59], v[168:171]
s_barrier
ds_read_b64_tr_b16 a[128:129], v243 offset:0
v_permlane16_swap_b32 v249, v251
v_mfma_f32_16x16x32_bf16 v[172:175], a[132:135], v[60:63], v[172:175]
ds_read_b64_tr_b16 a[130:131], v243 offset:3584
v_accvgpr_write_b32 a53, v249
v_mfma_f32_16x16x32_bf16 v[176:179], a[136:139], v[56:59], v[176:179]
ds_read_b64_tr_b16 a[132:133], v243 offset:14464
v_accvgpr_write_b32 a55, v251
v_mfma_f32_16x16x32_bf16 v[180:183], a[136:139], v[60:63], v[180:183]
s_add_u32 s71, s75, 0
ds_read_b64_tr_b16 a[134:135], v243 offset:18048
v_accvgpr_read_b32 v248, a64
v_mfma_f32_16x16x32_bf16 v[184:187], a[140:143], v[56:59], v[184:187]
s_add_u32 m0, s77, 98304
ds_read_b64_tr_b16 a[136:137], v243 offset:32
v_accvgpr_read_b32 v249, a65
v_mfma_f32_16x16x32_bf16 v[188:191], a[140:143], v[60:63], v[188:191]
s_waitcnt lgkmcnt(5)
ds_read_b64_tr_b16 a[138:139], v243 offset:3616
buffer_load_dwordx4 v224, s[52:55], s71 offen offset:0 lds
v_mfma_f32_16x16x32_bf16 v[192:195], a[144:147], v[56:59], v[192:195]
ds_read_b64_tr_b16 a[140:141], v243 offset:14496
v_accvgpr_read_b32 v250, a72
v_mfma_f32_16x16x32_bf16 v[196:199], a[144:147], v[60:63], v[196:199]
ds_read_b64_tr_b16 a[142:143], v243 offset:18080
v_accvgpr_read_b32 v251, a73
v_mfma_f32_16x16x32_bf16 v[200:203], a[148:151], v[56:59], v[200:203]
ds_read_b64_tr_b16 a[144:145], v243 offset:64
v_cvt_pk_bf16_f32 v248, v248, v249
v_mfma_f32_16x16x32_bf16 v[204:207], a[148:151], v[60:63], v[204:207]
ds_read_b64_tr_b16 a[146:147], v243 offset:3648
v_cvt_pk_bf16_f32 v250, v250, v251
v_mfma_f32_16x16x32_bf16 v[208:211], a[152:155], v[56:59], v[208:211]
ds_read_b64_tr_b16 a[148:149], v243 offset:14528
v_accvgpr_read_b32 v249, a66
v_mfma_f32_16x16x32_bf16 v[212:215], a[152:155], v[60:63], v[212:215]
ds_read_b64_tr_b16 a[150:151], v243 offset:18112
buffer_load_dwordx4 v225, s[52:55], s71 offen offset:1024 lds
v_mfma_f32_16x16x32_bf16 v[216:219], a[156:159], v[56:59], v[216:219]
ds_read_b64_tr_b16 a[152:153], v243 offset:96
v_accvgpr_read_b32 v251, a67
v_mfma_f32_16x16x32_bf16 v[220:223], a[156:159], v[60:63], v[220:223]
s_waitcnt lgkmcnt(5)
ds_read_b64_tr_b16 a[154:155], v243 offset:3680
v_permlane16_swap_b32 v248, v250
v_mfma_f32_16x16x32_bf16 v[112:115], a[128:131], v[64:67], v[112:115]
ds_read_b64_tr_b16 a[156:157], v243 offset:14560
v_cvt_pk_bf16_f32 v249, v249, v251
v_mfma_f32_16x16x32_bf16 v[116:119], a[128:131], v[68:71], v[116:119]
ds_read_b64_tr_b16 a[158:159], v243 offset:18144
v_accvgpr_write_b32 a64, v248
v_mfma_f32_16x16x32_bf16 v[120:123], a[132:135], v[64:67], v[120:123]
ds_read_b64_tr_b16 a[128:129], v243 offset:128
v_accvgpr_write_b32 a66, v250
v_mfma_f32_16x16x32_bf16 v[124:127], a[132:135], v[68:71], v[124:127]
ds_read_b64_tr_b16 a[130:131], v243 offset:3712
buffer_load_dwordx4 v226, s[52:55], s71 offen offset:2048 lds
v_mfma_f32_16x16x32_bf16 v[128:131], a[136:139], v[64:67], v[128:131]
ds_read_b64_tr_b16 a[132:133], v243 offset:14592
v_accvgpr_read_b32 v248, a74
v_mfma_f32_16x16x32_bf16 v[132:135], a[136:139], v[68:71], v[132:135]
ds_read_b64_tr_b16 a[134:135], v243 offset:18176
v_accvgpr_read_b32 v250, a75
v_mfma_f32_16x16x32_bf16 v[136:139], a[140:143], v[64:67], v[136:139]
ds_read_b64_tr_b16 a[136:137], v243 offset:160
v_cvt_pk_bf16_f32 v251, v248, v250
v_mfma_f32_16x16x32_bf16 v[140:143], a[140:143], v[68:71], v[140:143]
s_waitcnt lgkmcnt(5)
ds_read_b64_tr_b16 a[138:139], v243 offset:3744
v_permlane16_swap_b32 v249, v251
v_mfma_f32_16x16x32_bf16 v[144:147], a[144:147], v[64:67], v[144:147]
ds_read_b64_tr_b16 a[140:141], v243 offset:14624
v_accvgpr_write_b32 a65, v249
v_mfma_f32_16x16x32_bf16 v[148:151], a[144:147], v[68:71], v[148:151]
ds_read_b64_tr_b16 a[142:143], v243 offset:18208
buffer_load_dwordx4 v227, s[52:55], s71 offen offset:3072 lds
v_mfma_f32_16x16x32_bf16 v[152:155], a[148:151], v[64:67], v[152:155]
ds_read_b64_tr_b16 a[144:145], v243 offset:192
v_accvgpr_write_b32 a67, v251
v_mfma_f32_16x16x32_bf16 v[156:159], a[148:151], v[68:71], v[156:159]
ds_read_b64_tr_b16 a[146:147], v243 offset:3776
v_accvgpr_read_b32 v248, a68
v_mfma_f32_16x16x32_bf16 v[160:163], a[152:155], v[64:67], v[160:163]
ds_read_b64_tr_b16 a[148:149], v243 offset:14656
v_accvgpr_read_b32 v249, a69
v_mfma_f32_16x16x32_bf16 v[164:167], a[152:155], v[68:71], v[164:167]
s_add_u32 s71, s75, 4096
ds_read_b64_tr_b16 a[150:151], v243 offset:18240
v_accvgpr_read_b32 v250, a76
v_mfma_f32_16x16x32_bf16 v[168:171], a[156:159], v[64:67], v[168:171]
s_add_u32 m0, s77, 102400
ds_read_b64_tr_b16 a[152:153], v243 offset:7168
v_accvgpr_read_b32 v251, a77
v_mfma_f32_16x16x32_bf16 v[172:175], a[156:159], v[68:71], v[172:175]
s_waitcnt lgkmcnt(5)
ds_read_b64_tr_b16 a[154:155], v243 offset:10752
buffer_load_dwordx4 v228, s[52:55], s71 offen offset:0 lds
v_mfma_f32_16x16x32_bf16 v[176:179], a[128:131], v[64:67], v[176:179]
ds_read_b64_tr_b16 a[156:157], v243 offset:21632
v_cvt_pk_bf16_f32 v248, v248, v249
v_mfma_f32_16x16x32_bf16 v[180:183], a[128:131], v[68:71], v[180:183]
ds_read_b64_tr_b16 a[158:159], v243 offset:25216
v_cvt_pk_bf16_f32 v250, v250, v251
v_mfma_f32_16x16x32_bf16 v[184:187], a[132:135], v[64:67], v[184:187]
ds_read_b64_tr_b16 a[128:129], v243 offset:7200
v_accvgpr_read_b32 v249, a70
v_mfma_f32_16x16x32_bf16 v[188:191], a[132:135], v[68:71], v[188:191]
ds_read_b64_tr_b16 a[130:131], v243 offset:10784
v_accvgpr_read_b32 v251, a71
v_mfma_f32_16x16x32_bf16 v[192:195], a[136:139], v[64:67], v[192:195]
ds_read_b64_tr_b16 a[132:133], v243 offset:21664
v_permlane16_swap_b32 v248, v250
v_mfma_f32_16x16x32_bf16 v[196:199], a[136:139], v[68:71], v[196:199]
ds_read_b64_tr_b16 a[134:135], v243 offset:25248
buffer_load_dwordx4 v229, s[52:55], s71 offen offset:1024 lds
v_mfma_f32_16x16x32_bf16 v[200:203], a[140:143], v[64:67], v[200:203]
ds_read_b64_tr_b16 a[136:137], v243 offset:7232
v_cvt_pk_bf16_f32 v249, v249, v251
v_mfma_f32_16x16x32_bf16 v[204:207], a[140:143], v[68:71], v[204:207]
s_waitcnt lgkmcnt(5)
ds_read_b64_tr_b16 a[138:139], v243 offset:10816
v_accvgpr_write_b32 a68, v248
v_mfma_f32_16x16x32_bf16 v[208:211], a[144:147], v[64:67], v[208:211]
ds_read_b64_tr_b16 a[140:141], v243 offset:21696
v_accvgpr_write_b32 a70, v250
v_mfma_f32_16x16x32_bf16 v[212:215], a[144:147], v[68:71], v[212:215]
ds_read_b64_tr_b16 a[142:143], v243 offset:25280
v_accvgpr_read_b32 v248, a78
v_mfma_f32_16x16x32_bf16 v[216:219], a[148:151], v[64:67], v[216:219]
ds_read_b64_tr_b16 a[144:145], v243 offset:7264
v_accvgpr_read_b32 v250, a79
v_mfma_f32_16x16x32_bf16 v[220:223], a[148:151], v[68:71], v[220:223]
ds_read_b64_tr_b16 a[146:147], v243 offset:10848
buffer_load_dwordx4 v230, s[52:55], s71 offen offset:2048 lds
v_mfma_f32_16x16x32_bf16 v[112:115], a[152:155], v[72:75], v[112:115]
ds_read_b64_tr_b16 a[148:149], v243 offset:21728
v_mov_b32 v64, 0
v_mfma_f32_16x16x32_bf16 v[116:119], a[152:155], v[76:79], v[116:119]
ds_read_b64_tr_b16 a[150:151], v243 offset:25312
v_mov_b32 v65, 0
v_mfma_f32_16x16x32_bf16 v[120:123], a[156:159], v[72:75], v[120:123]
ds_read_b64_tr_b16 a[152:153], v243 offset:7296
v_mov_b32 v66, 0
v_mfma_f32_16x16x32_bf16 v[124:127], a[156:159], v[76:79], v[124:127]
s_waitcnt lgkmcnt(5)
ds_read_b64_tr_b16 a[154:155], v243 offset:10880
v_mov_b32 v67, 0
v_mfma_f32_16x16x32_bf16 v[128:131], a[128:131], v[72:75], v[128:131]
ds_read_b64_tr_b16 a[156:157], v243 offset:21760
v_mov_b32 v68, 0
v_mfma_f32_16x16x32_bf16 v[132:135], a[128:131], v[76:79], v[132:135]
ds_read_b64_tr_b16 a[158:159], v243 offset:25344
buffer_load_dwordx4 v231, s[52:55], s71 offen offset:3072 lds
v_mfma_f32_16x16x32_bf16 v[136:139], a[132:135], v[72:75], v[136:139]
ds_read_b64_tr_b16 a[128:129], v243 offset:7328
v_mov_b32 v69, 0
v_mfma_f32_16x16x32_bf16 v[140:143], a[132:135], v[76:79], v[140:143]
ds_read_b64_tr_b16 a[130:131], v243 offset:10912
v_mov_b32 v70, 0
v_mfma_f32_16x16x32_bf16 v[144:147], a[136:139], v[72:75], v[144:147]
ds_read_b64_tr_b16 a[132:133], v243 offset:21792
v_mov_b32 v71, 0
v_mfma_f32_16x16x32_bf16 v[148:151], a[136:139], v[76:79], v[148:151]
ds_read_b64_tr_b16 a[134:135], v243 offset:25376
v_cvt_pk_bf16_f32 v251, v248, v250
v_mfma_f32_16x16x32_bf16 v[152:155], a[140:143], v[72:75], v[152:155]
ds_read_b64_tr_b16 a[136:137], v243 offset:7360
v_permlane16_swap_b32 v249, v251
v_mfma_f32_16x16x32_bf16 v[156:159], a[140:143], v[76:79], v[156:159]
s_waitcnt lgkmcnt(5)
ds_read_b64_tr_b16 a[138:139], v243 offset:10944
v_accvgpr_write_b32 a69, v249
v_mfma_f32_16x16x32_bf16 v[160:163], a[144:147], v[72:75], v[160:163]
ds_read_b64_tr_b16 a[140:141], v243 offset:21824
v_accvgpr_write_b32 a71, v251
v_mfma_f32_16x16x32_bf16 v[164:167], a[144:147], v[76:79], v[164:167]
s_waitcnt vmcnt(16)
ds_read_b64_tr_b16 a[142:143], v243 offset:25408
v_accvgpr_read_b32 v248, a80
v_mfma_f32_16x16x32_bf16 v[168:171], a[148:151], v[72:75], v[168:171]
s_barrier
ds_read_b64_tr_b16 a[144:145], v243 offset:32768
v_accvgpr_read_b32 v249, a81
v_mfma_f32_16x16x32_bf16 v[172:175], a[148:151], v[76:79], v[172:175]
ds_read_b64_tr_b16 a[146:147], v243 offset:36352
v_accvgpr_read_b32 v250, a88
v_mfma_f32_16x16x32_bf16 v[176:179], a[152:155], v[72:75], v[176:179]
ds_read_b64_tr_b16 a[148:149], v243 offset:47232
v_accvgpr_read_b32 v251, a89
v_mfma_f32_16x16x32_bf16 v[180:183], a[152:155], v[76:79], v[180:183]
s_add_u32 s71, s75, 32768
ds_read_b64_tr_b16 a[150:151], v243 offset:50816
v_cvt_pk_bf16_f32 v248, v248, v249
v_mfma_f32_16x16x32_bf16 v[184:187], a[156:159], v[72:75], v[184:187]
s_add_u32 m0, s77, 131072
ds_read_b64_tr_b16 a[152:153], v243 offset:32800
v_cvt_pk_bf16_f32 v250, v250, v251
v_mfma_f32_16x16x32_bf16 v[188:191], a[156:159], v[76:79], v[188:191]
s_waitcnt lgkmcnt(5)
ds_read_b64_tr_b16 a[154:155], v243 offset:36384
buffer_load_dwordx4 v224, s[52:55], s71 offen offset:0 lds
v_mfma_f32_16x16x32_bf16 v[192:195], a[128:131], v[72:75], v[192:195]
ds_read_b64_tr_b16 a[156:157], v243 offset:47264
v_accvgpr_read_b32 v249, a82
v_mfma_f32_16x16x32_bf16 v[196:199], a[128:131], v[76:79], v[196:199]
ds_read_b64_tr_b16 a[158:159], v243 offset:50848
v_accvgpr_read_b32 v251, a83
v_mfma_f32_16x16x32_bf16 v[200:203], a[132:135], v[72:75], v[200:203]
ds_read_b64_tr_b16 a[128:129], v243 offset:32832
v_permlane16_swap_b32 v248, v250
v_mfma_f32_16x16x32_bf16 v[204:207], a[132:135], v[76:79], v[204:207]
ds_read_b64_tr_b16 a[130:131], v243 offset:36416
v_cvt_pk_bf16_f32 v249, v249, v251
v_mfma_f32_16x16x32_bf16 v[208:211], a[136:139], v[72:75], v[208:211]
ds_read_b64_tr_b16 a[132:133], v243 offset:47296
v_accvgpr_write_b32 a80, v248
v_mfma_f32_16x16x32_bf16 v[212:215], a[136:139], v[76:79], v[212:215]
ds_read_b64_tr_b16 a[134:135], v243 offset:50880
buffer_load_dwordx4 v225, s[52:55], s71 offen offset:1024 lds
v_mfma_f32_16x16x32_bf16 v[216:219], a[140:143], v[72:75], v[216:219]
ds_read_b64_tr_b16 a[136:137], v243 offset:32864
v_accvgpr_write_b32 a82, v250
v_mfma_f32_16x16x32_bf16 v[220:223], a[140:143], v[76:79], v[220:223]
s_waitcnt lgkmcnt(5)
ds_read_b64_tr_b16 a[138:139], v243 offset:36448
v_accvgpr_read_b32 v248, a90
v_mfma_f32_16x16x32_bf16 v[112:115], a[144:147], v[80:83], v[112:115]
ds_read_b64_tr_b16 a[140:141], v243 offset:47328
v_accvgpr_read_b32 v72, a178
v_mfma_f32_16x16x32_bf16 v[116:119], a[144:147], v[84:87], v[116:119]
ds_read_b64_tr_b16 a[142:143], v243 offset:50912
v_xor_b32 v73, 64, v72
v_mfma_f32_16x16x32_bf16 v[120:123], a[148:151], v[80:83], v[120:123]
ds_read_b64_tr_b16 a[144:145], v243 offset:32896
v_xor_b32 v74, 128, v72
v_mfma_f32_16x16x32_bf16 v[124:127], a[148:151], v[84:87], v[124:127]
ds_read_b64_tr_b16 a[146:147], v243 offset:36480
buffer_load_dwordx4 v226, s[52:55], s71 offen offset:2048 lds
v_mfma_f32_16x16x32_bf16 v[128:131], a[152:155], v[80:83], v[128:131]
ds_read_b64_tr_b16 a[148:149], v243 offset:47360
v_xor_b32 v75, 192, v72
v_mfma_f32_16x16x32_bf16 v[132:135], a[152:155], v[84:87], v[132:135]
ds_read_b64_tr_b16 a[150:151], v243 offset:50944
v_accvgpr_read_b32 v250, a91
v_mfma_f32_16x16x32_bf16 v[136:139], a[156:159], v[80:83], v[136:139]
ds_read_b64_tr_b16 a[152:153], v243 offset:32928
v_cvt_pk_bf16_f32 v251, v248, v250
v_mfma_f32_16x16x32_bf16 v[140:143], a[156:159], v[84:87], v[140:143]
s_waitcnt lgkmcnt(5)
ds_read_b64_tr_b16 a[154:155], v243 offset:36512
v_permlane16_swap_b32 v249, v251
v_mfma_f32_16x16x32_bf16 v[144:147], a[128:131], v[80:83], v[144:147]
ds_read_b64_tr_b16 a[156:157], v243 offset:47392
v_accvgpr_write_b32 a81, v249
v_mfma_f32_16x16x32_bf16 v[148:151], a[128:131], v[84:87], v[148:151]
ds_read_b64_tr_b16 a[158:159], v243 offset:50976
buffer_load_dwordx4 v227, s[52:55], s71 offen offset:3072 lds
v_mfma_f32_16x16x32_bf16 v[152:155], a[132:135], v[80:83], v[152:155]
ds_read_b64_tr_b16 a[128:129], v243 offset:32960
v_accvgpr_write_b32 a83, v251
v_mfma_f32_16x16x32_bf16 v[156:159], a[132:135], v[84:87], v[156:159]
ds_read_b64_tr_b16 a[130:131], v243 offset:36544
v_accvgpr_read_b32 v248, a84
v_mfma_f32_16x16x32_bf16 v[160:163], a[136:139], v[80:83], v[160:163]
ds_read_b64_tr_b16 a[132:133], v243 offset:47424
v_accvgpr_read_b32 v249, a85
v_mfma_f32_16x16x32_bf16 v[164:167], a[136:139], v[84:87], v[164:167]
s_add_u32 s71, s75, 36864
ds_read_b64_tr_b16 a[134:135], v243 offset:51008
v_accvgpr_read_b32 v250, a92
v_mfma_f32_16x16x32_bf16 v[168:171], a[140:143], v[80:83], v[168:171]
s_add_u32 m0, s77, 135168
ds_read_b64_tr_b16 a[136:137], v243 offset:39936
v_accvgpr_read_b32 v251, a93
v_mfma_f32_16x16x32_bf16 v[172:175], a[140:143], v[84:87], v[172:175]
s_waitcnt lgkmcnt(5)
ds_read_b64_tr_b16 a[138:139], v243 offset:43520
buffer_load_dwordx4 v228, s[52:55], s71 offen offset:0 lds
v_mfma_f32_16x16x32_bf16 v[176:179], a[144:147], v[80:83], v[176:179]
ds_read_b64_tr_b16 a[140:141], v243 offset:54400
v_cvt_pk_bf16_f32 v248, v248, v249
v_mfma_f32_16x16x32_bf16 v[180:183], a[144:147], v[84:87], v[180:183]
ds_read_b64_tr_b16 a[142:143], v243 offset:57984
v_cvt_pk_bf16_f32 v250, v250, v251
v_mfma_f32_16x16x32_bf16 v[184:187], a[148:151], v[80:83], v[184:187]
ds_read_b64_tr_b16 a[144:145], v243 offset:39968
v_accvgpr_read_b32 v249, a86
v_mfma_f32_16x16x32_bf16 v[188:191], a[148:151], v[84:87], v[188:191]
ds_read_b64_tr_b16 a[146:147], v243 offset:43552
v_accvgpr_read_b32 v251, a87
v_mfma_f32_16x16x32_bf16 v[192:195], a[152:155], v[80:83], v[192:195]
ds_read_b64_tr_b16 a[148:149], v243 offset:54432
v_permlane16_swap_b32 v248, v250
v_mfma_f32_16x16x32_bf16 v[196:199], a[152:155], v[84:87], v[196:199]
ds_read_b64_tr_b16 a[150:151], v243 offset:58016
buffer_load_dwordx4 v229, s[52:55], s71 offen offset:1024 lds
v_mfma_f32_16x16x32_bf16 v[200:203], a[156:159], v[80:83], v[200:203]
ds_read_b64_tr_b16 a[152:153], v243 offset:40000
v_cvt_pk_bf16_f32 v249, v249, v251
v_mfma_f32_16x16x32_bf16 v[204:207], a[156:159], v[84:87], v[204:207]
s_waitcnt lgkmcnt(5)
ds_read_b64_tr_b16 a[154:155], v243 offset:43584
v_accvgpr_write_b32 a84, v248
v_mfma_f32_16x16x32_bf16 v[208:211], a[128:131], v[80:83], v[208:211]
ds_read_b64_tr_b16 a[156:157], v243 offset:54464
v_accvgpr_write_b32 a86, v250
v_mfma_f32_16x16x32_bf16 v[212:215], a[128:131], v[84:87], v[212:215]
ds_read_b64_tr_b16 a[158:159], v243 offset:58048
v_accvgpr_read_b32 v248, a94
v_mfma_f32_16x16x32_bf16 v[216:219], a[132:135], v[80:83], v[216:219]
ds_read_b64_tr_b16 a[128:129], v243 offset:40032
v_accvgpr_read_b32 v250, a95
v_mfma_f32_16x16x32_bf16 v[220:223], a[132:135], v[84:87], v[220:223]
ds_read_b64_tr_b16 a[130:131], v243 offset:43616
buffer_load_dwordx4 v230, s[52:55], s71 offen offset:2048 lds
v_mfma_f32_16x16x32_bf16 v[112:115], a[136:139], v[88:91], v[112:115]
ds_read_b64_tr_b16 a[132:133], v243 offset:54496
v_cvt_pk_bf16_f32 v251, v248, v250
v_mfma_f32_16x16x32_bf16 v[116:119], a[136:139], v[92:95], v[116:119]
ds_read_b64_tr_b16 a[134:135], v243 offset:58080
v_permlane16_swap_b32 v249, v251
v_mfma_f32_16x16x32_bf16 v[120:123], a[140:143], v[88:91], v[120:123]
ds_read_b64_tr_b16 a[136:137], v243 offset:40064
v_accvgpr_write_b32 a85, v249
v_mfma_f32_16x16x32_bf16 v[124:127], a[140:143], v[92:95], v[124:127]
s_waitcnt lgkmcnt(5)
ds_read_b64_tr_b16 a[138:139], v243 offset:43648
v_accvgpr_write_b32 a87, v251
v_mfma_f32_16x16x32_bf16 v[128:131], a[144:147], v[88:91], v[128:131]
ds_read_b64_tr_b16 a[140:141], v243 offset:54528
v_accvgpr_read_b32 v248, a96
v_mfma_f32_16x16x32_bf16 v[132:135], a[144:147], v[92:95], v[132:135]
ds_read_b64_tr_b16 a[142:143], v243 offset:58112
buffer_load_dwordx4 v231, s[52:55], s71 offen offset:3072 lds
v_mfma_f32_16x16x32_bf16 v[136:139], a[148:151], v[88:91], v[136:139]
ds_read_b64_tr_b16 a[144:145], v243 offset:40096
v_accvgpr_read_b32 v249, a97
v_mfma_f32_16x16x32_bf16 v[140:143], a[148:151], v[92:95], v[140:143]
ds_read_b64_tr_b16 a[146:147], v243 offset:43680
v_accvgpr_read_b32 v250, a104
v_mfma_f32_16x16x32_bf16 v[144:147], a[152:155], v[88:91], v[144:147]
ds_read_b64_tr_b16 a[148:149], v243 offset:54560
v_accvgpr_read_b32 v251, a105
v_mfma_f32_16x16x32_bf16 v[148:151], a[152:155], v[92:95], v[148:151]
ds_read_b64_tr_b16 a[150:151], v243 offset:58144
v_cvt_pk_bf16_f32 v248, v248, v249
v_mfma_f32_16x16x32_bf16 v[152:155], a[156:159], v[88:91], v[152:155]
ds_read_b64_tr_b16 a[152:153], v243 offset:40128
v_cvt_pk_bf16_f32 v250, v250, v251
v_mfma_f32_16x16x32_bf16 v[156:159], a[156:159], v[92:95], v[156:159]
s_waitcnt lgkmcnt(5)
ds_read_b64_tr_b16 a[154:155], v243 offset:43712
v_accvgpr_read_b32 v249, a98
v_mfma_f32_16x16x32_bf16 v[160:163], a[128:131], v[88:91], v[160:163]
ds_read_b64_tr_b16 a[156:157], v243 offset:54592
v_accvgpr_read_b32 v251, a99
v_mfma_f32_16x16x32_bf16 v[164:167], a[128:131], v[92:95], v[164:167]
s_waitcnt vmcnt(16)
ds_read_b64_tr_b16 a[158:159], v243 offset:58176
v_add_u32 v243, 0x10000, v243
v_mfma_f32_16x16x32_bf16 v[168:171], a[132:135], v[88:91], v[168:171]
s_barrier
ds_read_b64_tr_b16 a[128:129], v243 offset:0
v_permlane16_swap_b32 v248, v250
v_mfma_f32_16x16x32_bf16 v[172:175], a[132:135], v[92:95], v[172:175]
ds_read_b64_tr_b16 a[130:131], v243 offset:3584
v_cvt_pk_bf16_f32 v249, v249, v251
v_mfma_f32_16x16x32_bf16 v[176:179], a[136:139], v[88:91], v[176:179]
ds_read_b64_tr_b16 a[132:133], v243 offset:7168
v_accvgpr_write_b32 a96, v248
v_mfma_f32_16x16x32_bf16 v[180:183], a[136:139], v[92:95], v[180:183]
s_add_u32 s71, s75, 0
ds_read_b64_tr_b16 a[134:135], v243 offset:10752
v_accvgpr_write_b32 a98, v250
v_mfma_f32_16x16x32_bf16 v[184:187], a[140:143], v[88:91], v[184:187]
s_add_u32 m0, s77, 0
ds_read_b64_tr_b16 a[136:137], v243 offset:14464
v_accvgpr_read_b32 v248, a106
v_mfma_f32_16x16x32_bf16 v[188:191], a[140:143], v[92:95], v[188:191]
s_waitcnt lgkmcnt(5)
ds_read_b64_tr_b16 a[138:139], v243 offset:18048
buffer_load_dwordx4 v244, s[56:59], s71 offen offset:0 lds
v_mfma_f32_16x16x32_bf16 v[192:195], a[144:147], v[88:91], v[192:195]
ds_read_b64_tr_b16 a[140:141], v243 offset:21632
v_accvgpr_read_b32 v250, a107
v_mfma_f32_16x16x32_bf16 v[196:199], a[144:147], v[92:95], v[196:199]
ds_read_b64_tr_b16 a[142:143], v243 offset:25216
v_cvt_pk_bf16_f32 v251, v248, v250
v_mfma_f32_16x16x32_bf16 v[200:203], a[148:151], v[88:91], v[200:203]
ds_read_b64_tr_b16 a[144:145], v243 offset:32
v_permlane16_swap_b32 v249, v251
v_mfma_f32_16x16x32_bf16 v[204:207], a[148:151], v[92:95], v[204:207]
ds_read_b64_tr_b16 a[146:147], v243 offset:3616
v_accvgpr_write_b32 a97, v249
v_mfma_f32_16x16x32_bf16 v[208:211], a[152:155], v[88:91], v[208:211]
ds_read_b64_tr_b16 a[148:149], v243 offset:7200
v_accvgpr_write_b32 a99, v251
v_mfma_f32_16x16x32_bf16 v[212:215], a[152:155], v[92:95], v[212:215]
ds_read_b64_tr_b16 a[150:151], v243 offset:10784
buffer_load_dwordx4 v245, s[56:59], s71 offen offset:1024 lds
v_mfma_f32_16x16x32_bf16 v[216:219], a[156:159], v[88:91], v[216:219]
ds_read_b64_tr_b16 a[152:153], v243 offset:14496
v_accvgpr_read_b32 v248, a100
v_mfma_f32_16x16x32_bf16 v[220:223], a[156:159], v[92:95], v[220:223]
s_waitcnt lgkmcnt(5)
ds_read_b64_tr_b16 a[154:155], v243 offset:18080
v_accvgpr_read_b32 v249, a101
v_mfma_f32_16x16x32_bf16 v[112:115], a[128:131], v[96:99], v[112:115]
ds_read_b64_tr_b16 a[156:157], v243 offset:21664
v_accvgpr_read_b32 v250, a108
v_mfma_f32_16x16x32_bf16 v[116:119], a[128:131], v[100:103], v[116:119]
ds_read_b64_tr_b16 a[158:159], v243 offset:25248
v_accvgpr_read_b32 v251, a109
v_mfma_f32_16x16x32_bf16 v[112:115], a[132:135], v[104:107], v[112:115]
ds_read_b64_tr_b16 a[128:129], v243 offset:64
v_cvt_pk_bf16_f32 v248, v248, v249
v_mfma_f32_16x16x32_bf16 v[116:119], a[132:135], v[108:111], v[116:119]
ds_read_b64_tr_b16 a[130:131], v243 offset:3648
buffer_load_dwordx4 v246, s[56:59], s71 offen offset:2048 lds
v_mfma_f32_16x16x32_bf16 v[120:123], a[136:139], v[96:99], v[120:123]
ds_read_b64_tr_b16 a[132:133], v243 offset:7232
v_cvt_pk_bf16_f32 v250, v250, v251
v_mfma_f32_16x16x32_bf16 v[124:127], a[136:139], v[100:103], v[124:127]
ds_read_b64_tr_b16 a[134:135], v243 offset:10816
v_accvgpr_read_b32 v249, a102
v_mfma_f32_16x16x32_bf16 v[120:123], a[140:143], v[104:107], v[120:123]
ds_read_b64_tr_b16 a[136:137], v243 offset:14528
v_accvgpr_read_b32 v251, a103
v_mfma_f32_16x16x32_bf16 v[124:127], a[140:143], v[108:111], v[124:127]
s_waitcnt lgkmcnt(5)
ds_read_b64_tr_b16 a[138:139], v243 offset:18112
v_permlane16_swap_b32 v248, v250
v_mfma_f32_16x16x32_bf16 v[128:131], a[144:147], v[96:99], v[128:131]
ds_read_b64_tr_b16 a[140:141], v243 offset:21696
v_cvt_pk_bf16_f32 v249, v249, v251
v_mfma_f32_16x16x32_bf16 v[132:135], a[144:147], v[100:103], v[132:135]
ds_read_b64_tr_b16 a[142:143], v243 offset:25280
buffer_load_dwordx4 v247, s[56:59], s71 offen offset:3072 lds
v_mfma_f32_16x16x32_bf16 v[128:131], a[148:151], v[104:107], v[128:131]
ds_read_b64_tr_b16 a[144:145], v243 offset:96
v_accvgpr_write_b32 a100, v248
v_mfma_f32_16x16x32_bf16 v[132:135], a[148:151], v[108:111], v[132:135]
ds_read_b64_tr_b16 a[146:147], v243 offset:3680
v_accvgpr_write_b32 a102, v250
v_mfma_f32_16x16x32_bf16 v[136:139], a[152:155], v[96:99], v[136:139]
ds_read_b64_tr_b16 a[148:149], v243 offset:7264
v_accvgpr_read_b32 v248, a110
v_mfma_f32_16x16x32_bf16 v[140:143], a[152:155], v[100:103], v[140:143]
s_add_u32 s71, s75, 4096
ds_read_b64_tr_b16 a[150:151], v243 offset:10848
v_accvgpr_read_b32 v250, a111
v_mfma_f32_16x16x32_bf16 v[136:139], a[156:159], v[104:107], v[136:139]
s_add_u32 m0, s77, 4096
ds_read_b64_tr_b16 a[152:153], v243 offset:14560
v_cvt_pk_bf16_f32 v251, v248, v250
v_mfma_f32_16x16x32_bf16 v[140:143], a[156:159], v[108:111], v[140:143]
s_waitcnt lgkmcnt(5)
ds_read_b64_tr_b16 a[154:155], v243 offset:18144
buffer_load_dwordx4 v244, s[56:59], s71 offen offset:0 lds
v_mfma_f32_16x16x32_bf16 v[144:147], a[128:131], v[96:99], v[144:147]
ds_read_b64_tr_b16 a[156:157], v243 offset:21728
v_permlane16_swap_b32 v249, v251
v_mfma_f32_16x16x32_bf16 v[148:151], a[128:131], v[100:103], v[148:151]
ds_read_b64_tr_b16 a[158:159], v243 offset:25312
v_accvgpr_write_b32 a101, v249
v_mfma_f32_16x16x32_bf16 v[144:147], a[132:135], v[104:107], v[144:147]
ds_read_b64_tr_b16 a[128:129], v243 offset:128
v_accvgpr_write_b32 a103, v251
v_mfma_f32_16x16x32_bf16 v[148:151], a[132:135], v[108:111], v[148:151]
ds_read_b64_tr_b16 a[130:131], v243 offset:3712
v_accvgpr_read_b32 v248, a112
v_mfma_f32_16x16x32_bf16 v[152:155], a[136:139], v[96:99], v[152:155]
ds_read_b64_tr_b16 a[132:133], v243 offset:7296
v_accvgpr_read_b32 v249, a113
v_mfma_f32_16x16x32_bf16 v[156:159], a[136:139], v[100:103], v[156:159]
ds_read_b64_tr_b16 a[134:135], v243 offset:10880
buffer_load_dwordx4 v245, s[56:59], s71 offen offset:1024 lds
v_mfma_f32_16x16x32_bf16 v[152:155], a[140:143], v[104:107], v[152:155]
ds_read_b64_tr_b16 a[136:137], v243 offset:14592
v_accvgpr_read_b32 v250, a120
v_mfma_f32_16x16x32_bf16 v[156:159], a[140:143], v[108:111], v[156:159]
s_waitcnt lgkmcnt(5)
ds_read_b64_tr_b16 a[138:139], v243 offset:18176
v_accvgpr_read_b32 v251, a121
v_mfma_f32_16x16x32_bf16 v[160:163], a[144:147], v[96:99], v[160:163]
ds_read_b64_tr_b16 a[140:141], v243 offset:21760
v_cvt_pk_bf16_f32 v248, v248, v249
v_mfma_f32_16x16x32_bf16 v[164:167], a[144:147], v[100:103], v[164:167]
ds_read_b64_tr_b16 a[142:143], v243 offset:25344
v_cvt_pk_bf16_f32 v250, v250, v251
v_mfma_f32_16x16x32_bf16 v[160:163], a[148:151], v[104:107], v[160:163]
ds_read_b64_tr_b16 a[144:145], v243 offset:160
v_accvgpr_read_b32 v249, a114
v_mfma_f32_16x16x32_bf16 v[164:167], a[148:151], v[108:111], v[164:167]
ds_read_b64_tr_b16 a[146:147], v243 offset:3744
buffer_load_dwordx4 v246, s[56:59], s71 offen offset:2048 lds
v_mfma_f32_16x16x32_bf16 v[168:171], a[152:155], v[96:99], v[168:171]
ds_read_b64_tr_b16 a[148:149], v243 offset:7328
v_accvgpr_read_b32 v251, a115
v_mfma_f32_16x16x32_bf16 v[172:175], a[152:155], v[100:103], v[172:175]
ds_read_b64_tr_b16 a[150:151], v243 offset:10912
v_permlane16_swap_b32 v248, v250
v_mfma_f32_16x16x32_bf16 v[168:171], a[156:159], v[104:107], v[168:171]
ds_read_b64_tr_b16 a[152:153], v243 offset:14624
v_cvt_pk_bf16_f32 v249, v249, v251
v_mfma_f32_16x16x32_bf16 v[172:175], a[156:159], v[108:111], v[172:175]
s_waitcnt lgkmcnt(5)
ds_read_b64_tr_b16 a[154:155], v243 offset:18208
v_accvgpr_write_b32 a112, v248
v_mfma_f32_16x16x32_bf16 v[176:179], a[128:131], v[96:99], v[176:179]
ds_read_b64_tr_b16 a[156:157], v243 offset:21792
v_accvgpr_write_b32 a114, v250
v_mfma_f32_16x16x32_bf16 v[180:183], a[128:131], v[100:103], v[180:183]
ds_read_b64_tr_b16 a[158:159], v243 offset:25376
buffer_load_dwordx4 v247, s[56:59], s71 offen offset:3072 lds
v_mfma_f32_16x16x32_bf16 v[176:179], a[132:135], v[104:107], v[176:179]
ds_read_b64_tr_b16 a[128:129], v243 offset:192
v_accvgpr_read_b32 v248, a122
v_mfma_f32_16x16x32_bf16 v[180:183], a[132:135], v[108:111], v[180:183]
ds_read_b64_tr_b16 a[130:131], v243 offset:3776
v_accvgpr_read_b32 v250, a123
v_mfma_f32_16x16x32_bf16 v[184:187], a[136:139], v[96:99], v[184:187]
ds_read_b64_tr_b16 a[132:133], v243 offset:7360
v_cvt_pk_bf16_f32 v251, v248, v250
v_mfma_f32_16x16x32_bf16 v[188:191], a[136:139], v[100:103], v[188:191]
ds_read_b64_tr_b16 a[134:135], v243 offset:10944
v_permlane16_swap_b32 v249, v251
v_mfma_f32_16x16x32_bf16 v[184:187], a[140:143], v[104:107], v[184:187]
ds_read_b64_tr_b16 a[136:137], v243 offset:14656
v_accvgpr_write_b32 a113, v249
v_mfma_f32_16x16x32_bf16 v[188:191], a[140:143], v[108:111], v[188:191]
s_waitcnt lgkmcnt(5)
ds_read_b64_tr_b16 a[138:139], v243 offset:18240
v_accvgpr_write_b32 a115, v251
v_mfma_f32_16x16x32_bf16 v[192:195], a[144:147], v[96:99], v[192:195]
ds_read_b64_tr_b16 a[140:141], v243 offset:21824
v_accvgpr_read_b32 v248, a116
v_mfma_f32_16x16x32_bf16 v[196:199], a[144:147], v[100:103], v[196:199]
s_waitcnt vmcnt(16)
ds_read_b64_tr_b16 a[142:143], v243 offset:25408
v_accvgpr_read_b32 v249, a117
v_mfma_f32_16x16x32_bf16 v[192:195], a[148:151], v[104:107], v[192:195]
s_barrier
ds_read_b128 a[144:147], v72 offset:0
v_accvgpr_read_b32 v250, a124
v_mfma_f32_16x16x32_bf16 v[196:199], a[148:151], v[108:111], v[196:199]
v_accvgpr_read_b32 v251, a125
v_cvt_pk_bf16_f32 v248, v248, v249
v_mfma_f32_16x16x32_bf16 v[200:203], a[152:155], v[96:99], v[200:203]
ds_read_b128 a[148:151], v73 offset:0
v_cvt_pk_bf16_f32 v250, v250, v251
v_mfma_f32_16x16x32_bf16 v[204:207], a[152:155], v[100:103], v[204:207]
s_add_u32 s71, s75, 65536
v_accvgpr_read_b32 v249, a118
v_accvgpr_read_b32 v251, a119
v_mfma_f32_16x16x32_bf16 v[200:203], a[156:159], v[104:107], v[200:203]
s_add_u32 m0, s77, 32768
ds_read_b128 a[152:155], v74 offset:0
v_permlane16_swap_b32 v248, v250
v_mfma_f32_16x16x32_bf16 v[204:207], a[156:159], v[108:111], v[204:207]
s_waitcnt lgkmcnt(3)
buffer_load_dwordx4 v224, s[52:55], s71 offen offset:0 lds
v_mfma_f32_16x16x32_bf16 v[208:211], a[128:131], v[96:99], v[208:211]
ds_read_b128 a[156:159], v75 offset:0
v_cvt_pk_bf16_f32 v249, v249, v251
v_mfma_f32_16x16x32_bf16 v[212:215], a[128:131], v[100:103], v[212:215]
v_accvgpr_write_b32 a116, v248
v_accvgpr_write_b32 a118, v250
v_mfma_f32_16x16x32_bf16 v[208:211], a[132:135], v[104:107], v[208:211]
ds_read_b128 a[128:131], v72 offset:256
v_accvgpr_read_b32 v248, a126
v_mfma_f32_16x16x32_bf16 v[212:215], a[132:135], v[108:111], v[212:215]
v_accvgpr_read_b32 v250, a127
v_cvt_pk_bf16_f32 v251, v248, v250
v_mfma_f32_16x16x32_bf16 v[216:219], a[136:139], v[96:99], v[216:219]
ds_read_b128 a[132:135], v73 offset:256
v_permlane16_swap_b32 v249, v251
v_mfma_f32_16x16x32_bf16 v[220:223], a[136:139], v[100:103], v[220:223]
buffer_load_dwordx4 v225, s[52:55], s71 offen offset:1024 lds
v_mfma_f32_16x16x32_bf16 v[216:219], a[140:143], v[104:107], v[216:219]
ds_read_b128 a[136:139], v74 offset:256
v_accvgpr_write_b32 a117, v249
v_mfma_f32_16x16x32_bf16 v[220:223], a[140:143], v[108:111], v[220:223]
s_waitcnt lgkmcnt(3)
v_accvgpr_write_b32 a119, v251
v_mov_b32 v76, 0
v_mfma_f32_16x16x32_bf16 v[0:3], a[144:147], a[0:3], v[64:67]
ds_read_b128 a[140:143], v75 offset:256
v_mov_b32 v77, 0
v_mfma_f32_16x16x32_bf16 v[4:7], a[144:147], a[4:7], v[68:71]
v_mul_f32 v120, v112, v120
v_mul_f32 v121, v113, v121
v_mfma_f32_16x16x32_bf16 v[0:3], a[148:151], a[16:19], v[0:3]
ds_read_b128 a[144:147], v72 offset:8192
v_mul_f32 v122, v114, v122
v_mfma_f32_16x16x32_bf16 v[4:7], a[148:151], a[20:23], v[4:7]
buffer_load_dwordx4 v226, s[52:55], s71 offen offset:2048 lds
v_mfma_f32_16x16x32_bf16 v[0:3], a[152:155], a[32:35], v[0:3]
ds_read_b128 a[148:151], v73 offset:8192
v_mul_f32 v123, v115, v123
v_mfma_f32_16x16x32_bf16 v[4:7], a[152:155], a[36:39], v[4:7]
v_mul_f32 v124, v116, v124
v_mul_f32 v125, v117, v125
v_mfma_f32_16x16x32_bf16 v[0:3], a[156:159], a[48:51], v[0:3]
ds_read_b128 a[152:155], v74 offset:8192
v_mul_f32 v126, v118, v126
v_mfma_f32_16x16x32_bf16 v[4:7], a[156:159], a[52:55], v[4:7]
s_waitcnt lgkmcnt(3)
v_mul_f32 v127, v119, v127
v_cvt_pk_bf16_f32 v112, v120, v121
v_mfma_f32_16x16x32_bf16 v[0:3], a[128:131], a[64:67], v[0:3]
ds_read_b128 a[156:159], v75 offset:8192
v_cvt_pk_bf16_f32 v113, v122, v123
v_mfma_f32_16x16x32_bf16 v[4:7], a[128:131], a[68:71], v[4:7]
buffer_load_dwordx4 v227, s[52:55], s71 offen offset:3072 lds
v_mfma_f32_16x16x32_bf16 v[0:3], a[132:135], a[80:83], v[0:3]
ds_read_b128 a[128:131], v72 offset:8448
v_cvt_pk_bf16_f32 v116, v124, v125
v_mfma_f32_16x16x32_bf16 v[4:7], a[132:135], a[84:87], v[4:7]
v_cvt_pk_bf16_f32 v117, v126, v127
v_mul_f32 v136, v128, v136
v_mfma_f32_16x16x32_bf16 v[0:3], a[136:139], a[96:99], v[0:3]
ds_read_b128 a[132:135], v73 offset:8448
v_mul_f32 v137, v129, v137
v_mfma_f32_16x16x32_bf16 v[4:7], a[136:139], a[100:103], v[4:7]
s_add_u32 s71, s75, 69632
v_mul_f32 v138, v130, v138
v_mul_f32 v139, v131, v139
v_mfma_f32_16x16x32_bf16 v[0:3], a[140:143], a[112:115], v[0:3]
s_add_u32 m0, s77, 36864
ds_read_b128 a[136:139], v74 offset:8448
v_mul_f32 v140, v132, v140
v_mfma_f32_16x16x32_bf16 v[4:7], a[140:143], a[116:119], v[4:7]
s_waitcnt lgkmcnt(3)
buffer_load_dwordx4 v228, s[52:55], s71 offen offset:0 lds
v_mfma_f32_16x16x32_bf16 v[8:11], a[144:147], a[0:3], v[64:67]
ds_read_b128 a[140:143], v75 offset:8448
v_mul_f32 v141, v133, v141
v_mfma_f32_16x16x32_bf16 v[12:15], a[144:147], a[4:7], v[68:71]
v_mul_f32 v142, v134, v142
v_mul_f32 v143, v135, v143
v_mfma_f32_16x16x32_bf16 v[8:11], a[148:151], a[16:19], v[8:11]
ds_read_b128 a[144:147], v72 offset:16384
v_cvt_pk_bf16_f32 v114, v136, v137
v_mfma_f32_16x16x32_bf16 v[12:15], a[148:151], a[20:23], v[12:15]
v_max3_f32 v248, v0, v1, v2
v_max_f32 v248, v248, v3
v_mfma_f32_16x16x32_bf16 v[8:11], a[152:155], a[32:35], v[8:11]
ds_read_b128 a[148:151], v73 offset:16384
v_max3_f32 v249, v4, v5, v6
v_mfma_f32_16x16x32_bf16 v[12:15], a[152:155], a[36:39], v[12:15]
buffer_load_dwordx4 v229, s[52:55], s71 offen offset:1024 lds
v_mfma_f32_16x16x32_bf16 v[8:11], a[156:159], a[48:51], v[8:11]
ds_read_b128 a[152:155], v74 offset:16384
v_max_f32 v249, v249, v7
v_mfma_f32_16x16x32_bf16 v[12:15], a[156:159], a[52:55], v[12:15]
s_waitcnt lgkmcnt(3)
v_cvt_pk_bf16_f32 v115, v138, v139
v_accvgpr_read_b32 v82, a179
v_mfma_f32_16x16x32_bf16 v[8:11], a[128:131], a[64:67], v[8:11]
s_add_u32 s81, s94, 0
ds_read_b128 a[156:159], v75 offset:16384
buffer_store_dwordx4 v[112:115], v252, s[64:67], s81 offen offset:0 sc1
v_mfma_f32_16x16x32_bf16 v[12:15], a[128:131], a[68:71], v[12:15]
v_cvt_pk_bf16_f32 v118, v140, v141
v_cvt_pk_bf16_f32 v119, v142, v143
v_mfma_f32_16x16x32_bf16 v[8:11], a[132:135], a[80:83], v[8:11]
s_add_u32 s81, s94, 3072
ds_read_b128 a[128:131], v72 offset:16640
buffer_store_dwordx4 v[116:119], v252, s[64:67], s81 offen offset:0 sc1
v_mfma_f32_16x16x32_bf16 v[12:15], a[132:135], a[84:87], v[12:15]
buffer_load_dwordx4 v230, s[52:55], s71 offen offset:2048 lds
v_mfma_f32_16x16x32_bf16 v[8:11], a[136:139], a[96:99], v[8:11]
ds_read_b128 a[132:135], v73 offset:16640
v_mul_f32 v152, v144, v152
v_mfma_f32_16x16x32_bf16 v[12:15], a[136:139], a[100:103], v[12:15]
v_mul_f32 v153, v145, v153
v_mul_f32 v154, v146, v154
v_mfma_f32_16x16x32_bf16 v[8:11], a[140:143], a[112:115], v[8:11]
ds_read_b128 a[136:139], v74 offset:16640
v_mul_f32 v155, v147, v155
v_mfma_f32_16x16x32_bf16 v[12:15], a[140:143], a[116:119], v[12:15]
s_waitcnt lgkmcnt(3)
v_mul_f32 v156, v148, v156
v_mul_f32 v157, v149, v157
v_mfma_f32_16x16x32_bf16 v[16:19], a[144:147], a[0:3], v[64:67]
ds_read_b128 a[140:143], v75 offset:16640
v_mul_f32 v158, v150, v158
v_mfma_f32_16x16x32_bf16 v[20:23], a[144:147], a[4:7], v[68:71]
buffer_load_dwordx4 v231, s[52:55], s71 offen offset:3072 lds
v_mfma_f32_16x16x32_bf16 v[16:19], a[148:151], a[16:19], v[16:19]
ds_read_b128 a[144:147], v72 offset:24576
v_mul_f32 v159, v151, v159
v_mfma_f32_16x16x32_bf16 v[20:23], a[148:151], a[20:23], v[20:23]
v_max3_f32 v248, v248, v8, v9
v_max3_f32 v248, v248, v10, v11
v_mfma_f32_16x16x32_bf16 v[16:19], a[152:155], a[32:35], v[16:19]
ds_read_b128 a[148:151], v73 offset:24576
v_max3_f32 v249, v249, v12, v13
v_mfma_f32_16x16x32_bf16 v[20:23], a[152:155], a[36:39], v[20:23]
v_max3_f32 v249, v249, v14, v15
v_cvt_pk_bf16_f32 v120, v152, v153
v_mfma_f32_16x16x32_bf16 v[16:19], a[156:159], a[48:51], v[16:19]
ds_read_b128 a[152:155], v74 offset:24576
v_cvt_pk_bf16_f32 v121, v154, v155
v_mfma_f32_16x16x32_bf16 v[20:23], a[156:159], a[52:55], v[20:23]
s_waitcnt lgkmcnt(3)
v_cvt_pk_bf16_f32 v124, v156, v157
v_cvt_pk_bf16_f32 v125, v158, v159
v_mfma_f32_16x16x32_bf16 v[16:19], a[128:131], a[64:67], v[16:19]
ds_read_b128 a[156:159], v75 offset:24576
v_mul_f32 v168, v160, v168
v_mfma_f32_16x16x32_bf16 v[20:23], a[128:131], a[68:71], v[20:23]
v_mul_f32 v169, v161, v169
v_mul_f32 v170, v162, v170
v_mfma_f32_16x16x32_bf16 v[16:19], a[132:135], a[80:83], v[16:19]
ds_read_b128 a[128:131], v72 offset:24832
v_mul_f32 v171, v163, v171
v_mfma_f32_16x16x32_bf16 v[20:23], a[132:135], a[84:87], v[20:23]
v_mul_f32 v172, v164, v172
v_mul_f32 v173, v165, v173
v_mfma_f32_16x16x32_bf16 v[16:19], a[136:139], a[96:99], v[16:19]
ds_read_b128 a[132:135], v73 offset:24832
v_mul_f32 v174, v166, v174
v_mfma_f32_16x16x32_bf16 v[20:23], a[136:139], a[100:103], v[20:23]
v_mul_f32 v175, v167, v175
v_cvt_pk_bf16_f32 v122, v168, v169
v_mfma_f32_16x16x32_bf16 v[16:19], a[140:143], a[112:115], v[16:19]
ds_read_b128 a[136:139], v74 offset:24832
v_cvt_pk_bf16_f32 v123, v170, v171
v_mfma_f32_16x16x32_bf16 v[20:23], a[140:143], a[116:119], v[20:23]
s_waitcnt lgkmcnt(3)
v_xor_b32 v83, 32, v82
v_xor_b32 v84, 64, v82
v_mfma_f32_16x16x32_bf16 v[24:27], a[144:147], a[0:3], v[64:67]
s_add_u32 s81, s94, 0
ds_read_b128 a[140:143], v75 offset:24832
buffer_store_dwordx4 v[120:123], v252, s[64:67], s81 offen offset:1024 sc1
v_mfma_f32_16x16x32_bf16 v[28:31], a[144:147], a[4:7], v[68:71]
s_waitcnt vmcnt(19)
v_cvt_pk_bf16_f32 v126, v172, v173
v_cvt_pk_bf16_f32 v127, v174, v175
v_mfma_f32_16x16x32_bf16 v[24:27], a[148:151], a[16:19], v[24:27]
s_barrier
ds_read_b128 a[144:147], v72 offset:32768
v_xor_b32 v85, 96, v82
v_mfma_f32_16x16x32_bf16 v[28:31], a[148:151], a[20:23], v[28:31]
s_add_u32 s81, s94, 3072
buffer_store_dwordx4 v[124:127], v252, s[64:67], s81 offen offset:1024 sc1
v_mfma_f32_16x16x32_bf16 v[24:27], a[152:155], a[32:35], v[24:27]
ds_read_b128 a[148:151], v73 offset:32768
v_max3_f32 v248, v248, v16, v17
v_mfma_f32_16x16x32_bf16 v[28:31], a[152:155], a[36:39], v[28:31]
s_add_u32 s71, s75, 32768
v_max3_f32 v248, v248, v18, v19
v_max3_f32 v249, v249, v20, v21
v_mfma_f32_16x16x32_bf16 v[24:27], a[156:159], a[48:51], v[24:27]
s_add_u32 m0, s77, 65536
ds_read_b128 a[152:155], v74 offset:32768
v_max3_f32 v249, v249, v22, v23
v_mfma_f32_16x16x32_bf16 v[28:31], a[156:159], a[52:55], v[28:31]
s_waitcnt lgkmcnt(3)
buffer_load_dwordx4 v244, s[56:59], s71 offen offset:0 lds
v_mfma_f32_16x16x32_bf16 v[24:27], a[128:131], a[64:67], v[24:27]
ds_read_b128 a[156:159], v75 offset:32768
v_mul_f32 v184, v176, v184
v_mfma_f32_16x16x32_bf16 v[28:31], a[128:131], a[68:71], v[28:31]
v_mul_f32 v185, v177, v185
v_mul_f32 v186, v178, v186
v_mfma_f32_16x16x32_bf16 v[24:27], a[132:135], a[80:83], v[24:27]
ds_read_b128 a[128:131], v72 offset:33024
v_mul_f32 v187, v179, v187
v_mfma_f32_16x16x32_bf16 v[28:31], a[132:135], a[84:87], v[28:31]
v_mul_f32 v188, v180, v188
v_mul_f32 v189, v181, v189
v_mfma_f32_16x16x32_bf16 v[24:27], a[136:139], a[96:99], v[24:27]
ds_read_b128 a[132:135], v73 offset:33024
v_mul_f32 v190, v182, v190
v_mfma_f32_16x16x32_bf16 v[28:31], a[136:139], a[100:103], v[28:31]
buffer_load_dwordx4 v245, s[56:59], s71 offen offset:1024 lds
v_mfma_f32_16x16x32_bf16 v[24:27], a[140:143], a[112:115], v[24:27]
ds_read_b128 a[136:139], v74 offset:33024
v_mul_f32 v191, v183, v191
v_mfma_f32_16x16x32_bf16 v[28:31], a[140:143], a[116:119], v[28:31]
s_waitcnt lgkmcnt(3)
v_cvt_pk_bf16_f32 v128, v184, v185
v_cvt_pk_bf16_f32 v129, v186, v187
v_mfma_f32_16x16x32_bf16 v[32:35], a[144:147], a[0:3], v[64:67]
ds_read_b128 a[140:143], v75 offset:33024
v_cvt_pk_bf16_f32 v132, v188, v189
v_mfma_f32_16x16x32_bf16 v[36:39], a[144:147], a[4:7], v[68:71]
v_cvt_pk_bf16_f32 v133, v190, v191
v_mul_f32 v200, v192, v200
v_mfma_f32_16x16x32_bf16 v[32:35], a[148:151], a[16:19], v[32:35]
ds_read_b128 a[144:147], v72 offset:40960
v_mul_f32 v201, v193, v201
v_mfma_f32_16x16x32_bf16 v[36:39], a[148:151], a[20:23], v[36:39]
buffer_load_dwordx4 v246, s[56:59], s71 offen offset:2048 lds
v_mfma_f32_16x16x32_bf16 v[32:35], a[152:155], a[32:35], v[32:35]
ds_read_b128 a[148:151], v73 offset:40960
v_max3_f32 v248, v248, v24, v25
v_mfma_f32_16x16x32_bf16 v[36:39], a[152:155], a[36:39], v[36:39]
v_max3_f32 v248, v248, v26, v27
v_max3_f32 v249, v249, v28, v29
v_mfma_f32_16x16x32_bf16 v[32:35], a[156:159], a[48:51], v[32:35]
ds_read_b128 a[152:155], v74 offset:40960
v_max3_f32 v249, v249, v30, v31
v_mfma_f32_16x16x32_bf16 v[36:39], a[156:159], a[52:55], v[36:39]
s_waitcnt lgkmcnt(3)
v_max_f32_e64 v250, |v248|, |v249|
v_mul_f32 v202, v194, v202
v_mfma_f32_16x16x32_bf16 v[32:35], a[128:131], a[64:67], v[32:35]
ds_read_b128 a[156:159], v75 offset:40960
v_mul_f32 v203, v195, v203
v_mfma_f32_16x16x32_bf16 v[36:39], a[128:131], a[68:71], v[36:39]
buffer_load_dwordx4 v247, s[56:59], s71 offen offset:3072 lds
v_mfma_f32_16x16x32_bf16 v[32:35], a[132:135], a[80:83], v[32:35]
ds_read_b128 a[128:131], v72 offset:41216
v_cmp_lt_f32 vcc, s85, v250
v_mfma_f32_16x16x32_bf16 v[36:39], a[132:135], a[84:87], v[36:39]
v_mul_f32 v204, v196, v204
v_mul_f32 v205, v197, v205
v_mfma_f32_16x16x32_bf16 v[32:35], a[136:139], a[96:99], v[32:35]
s_cbranch_vccnz L_rareA0
L_backA0:
ds_read_b128 a[132:135], v73 offset:41216
v_mul_f32 v206, v198, v206
v_mfma_f32_16x16x32_bf16 v[36:39], a[136:139], a[100:103], v[36:39]
s_add_u32 s71, s75, 36864
v_mul_f32 v207, v199, v207
v_cvt_pk_bf16_f32 v130, v200, v201
v_mfma_f32_16x16x32_bf16 v[32:35], a[140:143], a[112:115], v[32:35]
s_add_u32 m0, s77, 69632
ds_read_b128 a[136:139], v74 offset:41216
v_cvt_pk_bf16_f32 v131, v202, v203
v_mfma_f32_16x16x32_bf16 v[36:39], a[140:143], a[116:119], v[36:39]
s_waitcnt lgkmcnt(3)
buffer_load_dwordx4 v244, s[56:59], s71 offen offset:0 lds
v_mfma_f32_16x16x32_bf16 v[40:43], a[144:147], a[0:3], v[64:67]
s_add_u32 s81, s94, 0
ds_read_b128 a[140:143], v75 offset:41216
buffer_store_dwordx4 v[128:131], v252, s[64:67], s81 offen offset:2048 sc1
v_mfma_f32_16x16x32_bf16 v[44:47], a[144:147], a[4:7], v[68:71]
v_cvt_pk_bf16_f32 v134, v204, v205
v_cvt_pk_bf16_f32 v135, v206, v207
v_mfma_f32_16x16x32_bf16 v[40:43], a[148:151], a[16:19], v[40:43]
s_add_u32 s81, s94, 3072
ds_read_b128 a[144:147], v72 offset:49152
buffer_store_dwordx4 v[132:135], v252, s[64:67], s81 offen offset:2048 sc1
v_mfma_f32_16x16x32_bf16 v[44:47], a[148:151], a[20:23], v[44:47]
s_cmp_lg_u32 s84, 0
v_mul_f32 v216, v208, v216
v_mul_f32 v217, v209, v217
v_mfma_f32_16x16x32_bf16 v[40:43], a[152:155], a[32:35], v[40:43]
s_cbranch_scc1 L_rareB0
L_backB0:
ds_read_b128 a[148:151], v73 offset:49152
v_mul_f32 v218, v210, v218
v_mfma_f32_16x16x32_bf16 v[44:47], a[152:155], a[36:39], v[44:47]
buffer_load_dwordx4 v245, s[56:59], s71 offen offset:1024 lds
v_mfma_f32_16x16x32_bf16 v[40:43], a[156:159], a[48:51], v[40:43]
ds_read_b128 a[152:155], v74 offset:49152
v_mul_f32 v219, v211, v219
v_mfma_f32_16x16x32_bf16 v[44:47], a[156:159], a[52:55], v[44:47]
s_waitcnt lgkmcnt(3)
v_mul_f32 v220, v212, v220
v_mul_f32 v221, v213, v221
v_mfma_f32_16x16x32_bf16 v[40:43], a[128:131], a[64:67], v[40:43]
ds_read_b128 a[156:159], v75 offset:49152
v_mul_f32 v222, v214, v222
v_mfma_f32_16x16x32_bf16 v[44:47], a[128:131], a[68:71], v[44:47]
v_mul_f32 v223, v215, v223
v_cvt_pk_bf16_f32 v136, v216, v217
v_mfma_f32_16x16x32_bf16 v[40:43], a[132:135], a[80:83], v[40:43]
ds_read_b128 a[128:131], v72 offset:49408
v_cvt_pk_bf16_f32 v137, v218, v219
v_mfma_f32_16x16x32_bf16 v[44:47], a[132:135], a[84:87], v[44:47]
buffer_load_dwordx4 v246, s[56:59], s71 offen offset:2048 lds
v_mfma_f32_16x16x32_bf16 v[40:43], a[136:139], a[96:99], v[40:43]
ds_read_b128 a[132:135], v73 offset:49408
v_cvt_pk_bf16_f32 v138, v220, v221
v_mfma_f32_16x16x32_bf16 v[44:47], a[136:139], a[100:103], v[44:47]
v_cvt_pk_bf16_f32 v139, v222, v223
v_xor_b32 v86, 128, v82
v_mfma_f32_16x16x32_bf16 v[40:43], a[140:143], a[112:115], v[40:43]
s_add_u32 s81, s94, 6144
ds_read_b128 a[136:139], v74 offset:49408
buffer_store_dwordx4 v[136:139], v252, s[64:67], s81 offen offset:0 sc1
v_mfma_f32_16x16x32_bf16 v[44:47], a[140:143], a[116:119], v[44:47]
s_waitcnt lgkmcnt(3)
v_xor_b32 v87, 160, v82
v_xor_b32 v88, 192, v82
v_mfma_f32_16x16x32_bf16 v[48:51], a[144:147], a[0:3], v[64:67]
ds_read_b128 a[140:143], v75 offset:49408
v_xor_b32 v89, 224, v82
v_mfma_f32_16x16x32_bf16 v[52:55], a[144:147], a[4:7], v[68:71]
buffer_load_dwordx4 v247, s[56:59], s71 offen offset:3072 lds
v_mfma_f32_16x16x32_bf16 v[48:51], a[148:151], a[16:19], v[48:51]
ds_read_b128 a[144:147], v72 offset:57344
v_mov_b32 v78, 0x3f803f80
v_mfma_f32_16x16x32_bf16 v[52:55], a[148:151], a[20:23], v[52:55]
v_accvgpr_write_b32 a200, v78
v_accvgpr_write_b32 a201, v78
v_mfma_f32_16x16x32_bf16 v[48:51], a[152:155], a[32:35], v[48:51]
ds_read_b128 a[148:151], v73 offset:57344
v_accvgpr_write_b32 a202, v78
v_mfma_f32_16x16x32_bf16 v[52:55], a[152:155], a[36:39], v[52:55]
v_accvgpr_write_b32 a203, v78
v_exp_f32 v0, v0
v_mfma_f32_16x16x32_bf16 v[48:51], a[156:159], a[48:51], v[48:51]
ds_read_b128 a[152:155], v74 offset:57344
v_exp_f32 v1, v1
v_mfma_f32_16x16x32_bf16 v[52:55], a[156:159], a[52:55], v[52:55]
s_waitcnt lgkmcnt(3)
v_exp_f32 v4, v4
v_mfma_f32_16x16x32_bf16 v[48:51], a[128:131], a[64:67], v[48:51]
ds_read_b128 a[156:159], v75 offset:57344
v_exp_f32 v5, v5
v_mfma_f32_16x16x32_bf16 v[52:55], a[128:131], a[68:71], v[52:55]
v_exp_f32 v2, v2
v_mfma_f32_16x16x32_bf16 v[48:51], a[132:135], a[80:83], v[48:51]
ds_read_b128 a[128:131], v72 offset:57600
v_exp_f32 v3, v3
v_mfma_f32_16x16x32_bf16 v[52:55], a[132:135], a[84:87], v[52:55]
v_exp_f32 v6, v6
v_mfma_f32_16x16x32_bf16 v[48:51], a[136:139], a[96:99], v[48:51]
ds_read_b128 a[132:135], v73 offset:57600
v_exp_f32 v7, v7
v_mfma_f32_16x16x32_bf16 v[52:55], a[136:139], a[100:103], v[52:55]
v_exp_f32 v8, v8
v_mfma_f32_16x16x32_bf16 v[48:51], a[140:143], a[112:115], v[48:51]
ds_read_b128 a[136:139], v74 offset:57600
v_exp_f32 v9, v9
v_mfma_f32_16x16x32_bf16 v[52:55], a[140:143], a[116:119], v[52:55]
s_waitcnt lgkmcnt(3)
v_exp_f32 v12, v12
v_mfma_f32_16x16x32_bf16 v[56:59], a[144:147], a[0:3], v[64:67]
ds_read_b128 a[140:143], v75 offset:57600
v_exp_f32 v13, v13
v_mfma_f32_16x16x32_bf16 v[60:63], a[144:147], a[4:7], v[68:71]
s_waitcnt vmcnt(23)
v_exp_f32 v10, v10
v_mfma_f32_16x16x32_bf16 v[56:59], a[148:151], a[16:19], v[56:59]
s_barrier
v_exp_f32 v11, v11
v_mfma_f32_16x16x32_bf16 v[60:63], a[148:151], a[20:23], v[60:63]
v_exp_f32 v14, v14
v_mfma_f32_16x16x32_bf16 v[56:59], a[152:155], a[32:35], v[56:59]
ds_read_b64_tr_b16 a[148:149], v82 offset:0
v_exp_f32 v15, v15
v_mfma_f32_16x16x32_bf16 v[60:63], a[152:155], a[36:39], v[60:63]
s_add_u32 s71, s75, 98304
ds_read_b64_tr_b16 a[150:151], v82 offset:8192
v_cvt_pk_bf16_f32 v0, v0, v1
v_mfma_f32_16x16x32_bf16 v[56:59], a[156:159], a[48:51], v[56:59]
s_add_u32 m0, s77, 98304
ds_read_b64_tr_b16 a[152:153], v83 offset:0
ds_read_b64_tr_b16 a[154:155], v83 offset:8192
v_mfma_f32_16x16x32_bf16 v[60:63], a[156:159], a[52:55], v[60:63]
s_waitcnt lgkmcnt(4)
buffer_load_dwordx4 v224, s[52:55], s71 offen offset:0 lds
v_mfma_f32_16x16x32_bf16 v[56:59], a[128:131], a[64:67], v[56:59]
ds_read_b64_tr_b16 a[156:157], v84 offset:0
v_cvt_pk_bf16_f32 v4, v4, v5
v_mfma_f32_16x16x32_bf16 v[60:63], a[128:131], a[68:71], v[60:63]
ds_read_b64_tr_b16 a[158:159], v84 offset:8192
v_cvt_pk_bf16_f32 v1, v2, v3
v_mfma_f32_16x16x32_bf16 v[56:59], a[132:135], a[80:83], v[56:59]
ds_read_b64_tr_b16 a[128:129], v85 offset:0
v_cvt_pk_bf16_f32 v5, v6, v7
v_mfma_f32_16x16x32_bf16 v[60:63], a[132:135], a[84:87], v[60:63]
ds_read_b64_tr_b16 a[130:131], v85 offset:8192
v_cvt_pk_bf16_f32 v2, v8, v9
v_mfma_f32_16x16x32_bf16 v[56:59], a[136:139], a[96:99], v[56:59]
ds_read_b64_tr_b16 a[132:133], v86 offset:0
ds_read_b64_tr_b16 a[134:135], v86 offset:8192
v_mfma_f32_16x16x32_bf16 v[60:63], a[136:139], a[100:103], v[60:63]
buffer_load_dwordx4 v225, s[52:55], s71 offen offset:1024 lds
v_cvt_pk_bf16_f32 v6, v12, v13
v_cvt_pk_bf16_f32 v3, v10, v11
v_mfma_f32_16x16x32_bf16 v[56:59], a[140:143], a[112:115], v[56:59]
ds_read_b64_tr_b16 a[136:137], v87 offset:0
v_cvt_pk_bf16_f32 v7, v14, v15
v_mfma_f32_16x16x32_bf16 v[60:63], a[140:143], a[116:119], v[60:63]
s_waitcnt lgkmcnt(5)
ds_read_b64_tr_b16 a[138:139], v87 offset:8192
v_max3_f32 v248, v32, v33, v34
v_mfma_f32_16x16x32_bf16 a[192:195], a[200:203], v[0:3], 0
ds_read_b64_tr_b16 a[140:141], v88 offset:0
v_max_f32 v248, v248, v35
v_mfma_f32_16x16x32_bf16 a[196:199], a[200:203], v[4:7], 0
ds_read_b64_tr_b16 a[142:143], v88 offset:8192
v_max3_f32 v249, v36, v37, v38
v_mfma_f32_16x16x32_bf16 v[96:99], a[148:151], v[0:3], 0
ds_read_b64_tr_b16 a[144:145], v89 offset:0
ds_read_b64_tr_b16 a[146:147], v89 offset:8192
v_mfma_f32_16x16x32_bf16 v[100:103], a[148:151], v[4:7], 0
buffer_load_dwordx4 v226, s[52:55], s71 offen offset:2048 lds
v_mfma_f32_16x16x32_bf16 v[104:107], a[152:155], v[0:3], 0
ds_read_b64_tr_b16 a[148:149], v82 offset:256
v_max_f32 v249, v249, v39
v_mfma_f32_16x16x32_bf16 v[108:111], a[152:155], v[4:7], 0
ds_read_b64_tr_b16 a[150:151], v82 offset:8448
v_max3_f32 v248, v248, v40, v41
v_mfma_f32_16x16x32_bf16 v[112:115], a[156:159], v[0:3], 0
ds_read_b64_tr_b16 a[152:153], v83 offset:256
v_max3_f32 v248, v248, v42, v43
v_mfma_f32_16x16x32_bf16 v[116:119], a[156:159], v[4:7], 0
s_waitcnt lgkmcnt(5)
ds_read_b64_tr_b16 a[154:155], v83 offset:8448
v_max3_f32 v249, v249, v44, v45
v_mfma_f32_16x16x32_bf16 v[120:123], a[128:131], v[0:3], 0
ds_read_b64_tr_b16 a[156:157], v84 offset:256
ds_read_b64_tr_b16 a[158:159], v84 offset:8448
v_mfma_f32_16x16x32_bf16 v[124:127], a[128:131], v[4:7], 0
buffer_load_dwordx4 v227, s[52:55], s71 offen offset:3072 lds
v_mfma_f32_16x16x32_bf16 v[128:131], a[132:135], v[0:3], 0
ds_read_b64_tr_b16 a[128:129], v85 offset:256
v_max3_f32 v249, v249, v46, v47
v_mfma_f32_16x16x32_bf16 v[132:135], a[132:135], v[4:7], 0
ds_read_b64_tr_b16 a[130:131], v85 offset:8448
v_max3_f32 v248, v248, v48, v49
v_max3_f32 v248, v248, v50, v51
v_max3_f32 v249, v249, v52, v53
v_max3_f32 v249, v249, v54, v55
v_max3_f32 v248, v248, v56, v57
v_max3_f32 v248, v248, v58, v59
v_max3_f32 v249, v249, v60, v61
v_max3_f32 v249, v249, v62, v63
v_max_f32 v250, v248, v249
v_mfma_f32_16x16x32_bf16 v[136:139], a[136:139], v[0:3], 0
s_add_u32 s71, s75, 102400
ds_read_b64_tr_b16 a[132:133], v86 offset:256
v_cmp_lt_f32 vcc, s85, v250
v_mfma_f32_16x16x32_bf16 v[140:143], a[136:139], v[4:7], 0
s_add_u32 m0, s77, 102400
ds_read_b64_tr_b16 a[134:135], v86 offset:8448
v_exp_f32 v16, v16
v_mfma_f32_16x16x32_bf16 v[144:147], a[140:143], v[0:3], 0
s_cbranch_vccnz L_rareA1
L_backA1:
ds_read_b64_tr_b16 a[136:137], v87 offset:256
ds_read_b64_tr_b16 a[138:139], v87 offset:8448
v_mfma_f32_16x16x32_bf16 v[148:151], a[140:143], v[4:7], 0
s_waitcnt lgkmcnt(6)
buffer_load_dwordx4 v228, s[52:55], s71 offen offset:0 lds
v_mfma_f32_16x16x32_bf16 v[152:155], a[144:147], v[0:3], 0
ds_read_b64_tr_b16 a[140:141], v88 offset:256
v_exp_f32 v17, v17
v_mfma_f32_16x16x32_bf16 v[156:159], a[144:147], v[4:7], 0
ds_read_b64_tr_b16 a[142:143], v88 offset:8448
v_exp_f32 v20, v20
v_mfma_f32_16x16x32_bf16 v[160:163], a[148:151], v[0:3], 0
ds_read_b64_tr_b16 a[144:145], v89 offset:256
v_exp_f32 v21, v21
v_mfma_f32_16x16x32_bf16 v[164:167], a[148:151], v[4:7], 0
ds_read_b64_tr_b16 a[146:147], v89 offset:8448
v_exp_f32 v18, v18
v_mfma_f32_16x16x32_bf16 v[168:171], a[152:155], v[0:3], 0
v_exp_f32 v19, v19
v_mfma_f32_16x16x32_bf16 v[172:175], a[152:155], v[4:7], 0
buffer_load_dwordx4 v229, s[52:55], s71 offen offset:1024 lds
v_mfma_f32_16x16x32_bf16 v[176:179], a[156:159], v[0:3], 0
ds_read_b64_tr_b16 a[152:153], v82 offset:16384
v_exp_f32 v22, v22
v_mfma_f32_16x16x32_bf16 v[180:183], a[156:159], v[4:7], 0
s_waitcnt lgkmcnt(3)
ds_read_b64_tr_b16 a[154:155], v82 offset:24576
v_exp_f32 v23, v23
v_mfma_f32_16x16x32_bf16 v[184:187], a[128:131], v[0:3], 0
ds_read_b64_tr_b16 a[156:157], v83 offset:16384
v_exp_f32 v24, v24
v_mfma_f32_16x16x32_bf16 v[188:191], a[128:131], v[4:7], 0
ds_read_b64_tr_b16 a[158:159], v83 offset:24576
v_exp_f32 v25, v25
v_mfma_f32_16x16x32_bf16 v[192:195], a[132:135], v[0:3], 0
ds_read_b64_tr_b16 a[128:129], v84 offset:16384
ds_read_b64_tr_b16 a[130:131], v84 offset:24576
v_mfma_f32_16x16x32_bf16 v[196:199], a[132:135], v[4:7], 0
buffer_load_dwordx4 v230, s[52:55], s71 offen offset:2048 lds
v_mfma_f32_16x16x32_bf16 v[200:203], a[136:139], v[0:3], 0
ds_read_b64_tr_b16 a[132:133], v85 offset:16384
v_exp_f32 v28, v28
v_mfma_f32_16x16x32_bf16 v[204:207], a[136:139], v[4:7], 0
ds_read_b64_tr_b16 a[134:135], v85 offset:24576
v_exp_f32 v29, v29
v_mfma_f32_16x16x32_bf16 v[208:211], a[140:143], v[0:3], 0
ds_read_b64_tr_b16 a[136:137], v86 offset:16384
v_exp_f32 v26, v26
v_exp_f32 v27, v27
v_mfma_f32_16x16x32_bf16 v[212:215], a[140:143], v[4:7], 0
s_waitcnt lgkmcnt(5)
ds_read_b64_tr_b16 a[138:139], v86 offset:24576
v_exp_f32 v30, v30
v_exp_f32 v31, v31
v_cvt_pk_bf16_f32 v16, v16, v17
v_cvt_pk_bf16_f32 v20, v20, v21
v_cvt_pk_bf16_f32 v17, v18, v19
v_cvt_pk_bf16_f32 v21, v22, v23
v_cvt_pk_bf16_f32 v18, v24, v25
v_cvt_pk_bf16_f32 v22, v28, v29
v_cvt_pk_bf16_f32 v19, v26, v27
v_mfma_f32_16x16x32_bf16 v[216:219], a[144:147], v[0:3], 0
ds_read_b64_tr_b16 a[140:141], v87 offset:16384
ds_read_b64_tr_b16 a[142:143], v87 offset:24576
v_cvt_pk_bf16_f32 v23, v30, v31
v_mfma_f32_16x16x32_bf16 v[220:223], a[144:147], v[4:7], 0
buffer_load_dwordx4 v231, s[52:55], s71 offen offset:3072 lds
v_mfma_f32_16x16x32_bf16 a[192:195], a[200:203], v[16:19], a[192:195]
ds_read_b64_tr_b16 a[144:145], v88 offset:16384
v_add_u32 v72, 0xffff0000, v72
v_mfma_f32_16x16x32_bf16 a[196:199], a[200:203], v[20:23], a[196:199]
ds_read_b64_tr_b16 a[146:147], v88 offset:24576
v_add_u32 v73, 0xffff0000, v73
v_mfma_f32_16x16x32_bf16 v[96:99], a[152:155], v[16:19], v[96:99]
ds_read_b64_tr_b16 a[148:149], v89 offset:16384
v_add_u32 v74, 0xffff0000, v74
v_mfma_f32_16x16x32_bf16 v[100:103], a[152:155], v[20:23], v[100:103]
ds_read_b64_tr_b16 a[150:151], v89 offset:24576
v_add_u32 v75, 0xffff0000, v75
v_mfma_f32_16x16x32_bf16 v[104:107], a[156:159], v[16:19], v[104:107]
ds_read_b64_tr_b16 a[152:153], v82 offset:16640
v_accvgpr_read_b32 v238, a183
v_mfma_f32_16x16x32_bf16 v[108:111], a[156:159], v[20:23], v[108:111]
s_waitcnt lgkmcnt(5)
ds_read_b64_tr_b16 a[154:155], v82 offset:24832
v_accvgpr_read_b32 v239, a186
v_mfma_f32_16x16x32_bf16 v[112:115], a[128:131], v[16:19], v[112:115]
ds_read_b64_tr_b16 a[156:157], v83 offset:16640
v_add_u32 v82, 0x10000, v82
v_mfma_f32_16x16x32_bf16 v[116:119], a[128:131], v[20:23], v[116:119]
ds_read_b64_tr_b16 a[158:159], v83 offset:24832
v_add_u32 v83, 0x10000, v83
v_mfma_f32_16x16x32_bf16 v[120:123], a[132:135], v[16:19], v[120:123]
ds_read_b64_tr_b16 a[128:129], v84 offset:16640
v_exp_f32 v32, v32
v_mfma_f32_16x16x32_bf16 v[124:127], a[132:135], v[20:23], v[124:127]
ds_read_b64_tr_b16 a[130:131], v84 offset:24832
v_add_u32 v84, 0x10000, v84
v_mfma_f32_16x16x32_bf16 v[128:131], a[136:139], v[16:19], v[128:131]
ds_read_b64_tr_b16 a[132:133], v85 offset:16640
v_exp_f32 v33, v33
v_mfma_f32_16x16x32_bf16 v[132:135], a[136:139], v[20:23], v[132:135]
ds_read_b64_tr_b16 a[134:135], v85 offset:24832
v_add_u32 v85, 0x10000, v85
v_mfma_f32_16x16x32_bf16 v[136:139], a[140:143], v[16:19], v[136:139]
ds_read_b64_tr_b16 a[136:137], v86 offset:16640
v_exp_f32 v36, v36
v_mfma_f32_16x16x32_bf16 v[140:143], a[140:143], v[20:23], v[140:143]
s_waitcnt lgkmcnt(5)
ds_read_b64_tr_b16 a[138:139], v86 offset:24832
v_add_u32 v86, 0x10000, v86
v_mfma_f32_16x16x32_bf16 v[144:147], a[144:147], v[16:19], v[144:147]
ds_read_b64_tr_b16 a[140:141], v87 offset:16640
v_exp_f32 v37, v37
v_mfma_f32_16x16x32_bf16 v[148:151], a[144:147], v[20:23], v[148:151]
ds_read_b64_tr_b16 a[142:143], v87 offset:24832
buffer_load_dword v233, v235, s[64:67], s96 offen offset:0 sc1
v_mfma_f32_16x16x32_bf16 v[152:155], a[148:151], v[16:19], v[152:155]
ds_read_b64_tr_b16 a[144:145], v88 offset:16640
v_exp_f32 v34, v34
v_mfma_f32_16x16x32_bf16 v[156:159], a[148:151], v[20:23], v[156:159]
ds_read_b64_tr_b16 a[146:147], v88 offset:24832
v_exp_f32 v35, v35
v_mfma_f32_16x16x32_bf16 v[160:163], a[152:155], v[16:19], v[160:163]
ds_read_b64_tr_b16 a[148:149], v89 offset:16640
v_exp_f32 v38, v38
v_mfma_f32_16x16x32_bf16 v[164:167], a[152:155], v[20:23], v[164:167]
s_waitcnt vmcnt(22)
ds_read_b64_tr_b16 a[150:151], v89 offset:24832
v_exp_f32 v39, v39
v_mfma_f32_16x16x32_bf16 v[168:171], a[156:159], v[16:19], v[168:171]
s_barrier
ds_read_b128 a[152:155], v72 offset:0
v_add_u32 v87, 0x10000, v87
v_mfma_f32_16x16x32_bf16 v[172:175], a[156:159], v[20:23], v[172:175]
s_waitcnt lgkmcnt(5)
v_exp_f32 v40, v40
v_mfma_f32_16x16x32_bf16 v[176:179], a[128:131], v[16:19], v[176:179]
ds_read_b128 a[156:159], v73 offset:0
v_cvt_pk_bf16_f32 v32, v32, v33
v_mfma_f32_16x16x32_bf16 v[180:183], a[128:131], v[20:23], v[180:183]
s_add_u32 s71, s75, 65536
v_exp_f32 v41, v41
v_mfma_f32_16x16x32_bf16 v[184:187], a[132:135], v[16:19], v[184:187]
s_add_u32 m0, s77, 131072
ds_read_b128 a[128:131], v74 offset:0
v_cvt_pk_bf16_f32 v36, v36, v37
v_mfma_f32_16x16x32_bf16 v[188:191], a[132:135], v[20:23], v[188:191]
buffer_load_dwordx4 v244, s[56:59], s71 offen offset:0 lds
v_mfma_f32_16x16x32_bf16 v[192:195], a[136:139], v[16:19], v[192:195]
ds_read_b128 a[132:135], v75 offset:0
v_cvt_pk_bf16_f32 v33, v34, v35
v_mfma_f32_16x16x32_bf16 v[196:199], a[136:139], v[20:23], v[196:199]
v_exp_f32 v44, v44
v_mfma_f32_16x16x32_bf16 v[200:203], a[140:143], v[16:19], v[200:203]
ds_read_b128 a[136:139], v72 offset:256
v_cvt_pk_bf16_f32 v37, v38, v39
v_mfma_f32_16x16x32_bf16 v[204:207], a[140:143], v[20:23], v[204:207]
s_waitcnt lgkmcnt(3)
v_exp_f32 v45, v45
v_mfma_f32_16x16x32_bf16 v[208:211], a[144:147], v[16:19], v[208:211]
ds_read_b128 a[140:143], v73 offset:256
v_cvt_pk_bf16_f32 v34, v40, v41
v_mfma_f32_16x16x32_bf16 v[212:215], a[144:147], v[20:23], v[212:215]
buffer_load_dwordx4 v245, s[56:59], s71 offen offset:1024 lds
v_mfma_f32_16x16x32_bf16 v[216:219], a[148:151], v[16:19], v[216:219]
s_cmp_lg_u32 s84, 0
ds_read_b128 a[144:147], v74 offset:256
v_cvt_pk_bf16_f32 v38, v44, v45
v_mfma_f32_16x16x32_bf16 v[220:223], a[148:151], v[20:23], v[220:223]
s_cbranch_scc1 L_rareB1
L_backB1:
v_exp_f32 v42, v42
v_mfma_f32_16x16x32_bf16 v[0:3], a[152:155], a[0:3], v[64:67]
ds_read_b128 a[148:151], v75 offset:256
v_add_u32 v88, 0x10000, v88
v_mfma_f32_16x16x32_bf16 v[4:7], a[152:155], a[4:7], v[68:71]
v_exp_f32 v43, v43
v_mfma_f32_16x16x32_bf16 v[0:3], a[156:159], a[16:19], v[0:3]
ds_read_b128 a[152:155], v72 offset:8192
v_cvt_pk_bf16_f32 v35, v42, v43
v_mfma_f32_16x16x32_bf16 v[4:7], a[156:159], a[20:23], v[4:7]
s_waitcnt lgkmcnt(3)
buffer_load_dwordx4 v246, s[56:59], s71 offen offset:2048 lds
v_mfma_f32_16x16x32_bf16 v[0:3], a[128:131], a[32:35], v[0:3]
ds_read_b128 a[156:159], v73 offset:8192
v_add_u32 v89, 0x10000, v89
v_mfma_f32_16x16x32_bf16 v[4:7], a[128:131], a[36:39], v[4:7]
v_exp_f32 v46, v46
v_mfma_f32_16x16x32_bf16 v[0:3], a[132:135], a[48:51], v[0:3]
ds_read_b128 a[128:131], v74 offset:8192
v_accvgpr_read_b32 v90, a184
v_mfma_f32_16x16x32_bf16 v[4:7], a[132:135], a[52:55], v[4:7]
v_exp_f32 v47, v47
v_mfma_f32_16x16x32_bf16 v[0:3], a[136:139], a[64:67], v[0:3]
ds_read_b128 a[132:135], v75 offset:8192
v_cvt_pk_bf16_f32 v39, v46, v47
v_mfma_f32_16x16x32_bf16 v[4:7], a[136:139], a[68:71], v[4:7]
buffer_load_dwordx4 v247, s[56:59], s71 offen offset:3072 lds
v_mfma_f32_16x16x32_bf16 v[0:3], a[140:143], a[80:83], v[0:3]
ds_read_b128 a[136:139], v72 offset:8448
v_add_u32 v91, 6144, v90
v_mfma_f32_16x16x32_bf16 v[4:7], a[140:143], a[84:87], v[4:7]
s_waitcnt lgkmcnt(3)
v_exp_f32 v48, v48
v_mfma_f32_16x16x32_bf16 v[0:3], a[144:147], a[96:99], v[0:3]
ds_read_b128 a[140:143], v73 offset:8448
v_add_u32 v92, 12288, v90
v_mfma_f32_16x16x32_bf16 v[4:7], a[144:147], a[100:103], v[4:7]
s_add_u32 s71, s75, 69632
v_exp_f32 v49, v49
v_mfma_f32_16x16x32_bf16 v[0:3], a[148:151], a[112:115], v[0:3]
s_add_u32 m0, s77, 135168
ds_read_b128 a[144:147], v74 offset:8448
v_cvt_pk_bf16_f32 v48, v48, v49
v_mfma_f32_16x16x32_bf16 v[4:7], a[148:151], a[116:119], v[4:7]
buffer_load_dwordx4 v244, s[56:59], s71 offen offset:0 lds
v_mfma_f32_16x16x32_bf16 v[8:11], a[152:155], a[0:3], v[64:67]
ds_read_b128 a[148:151], v75 offset:8448
v_add_u32 v93, 18432, v90
v_mfma_f32_16x16x32_bf16 v[12:15], a[152:155], a[4:7], v[68:71]
v_exp_f32 v52, v52
v_mfma_f32_16x16x32_bf16 v[8:11], a[156:159], a[16:19], v[8:11]
ds_read_b128 a[152:155], v72 offset:16384
v_mfma_f32_16x16x32_bf16 v[12:15], a[156:159], a[20:23], v[12:15]
s_waitcnt lgkmcnt(3)
v_max3_f32 v248, v0, v1, v2
v_max_f32 v248, v248, v3
v_mfma_f32_16x16x32_bf16 v[8:11], a[128:131], a[32:35], v[8:11]
ds_read_b128 a[156:159], v73 offset:16384
v_max3_f32 v249, v4, v5, v6
v_mfma_f32_16x16x32_bf16 v[12:15], a[128:131], a[36:39], v[12:15]
buffer_load_dwordx4 v245, s[56:59], s71 offen offset:1024 lds
v_mfma_f32_16x16x32_bf16 v[8:11], a[132:135], a[48:51], v[8:11]
ds_read_b128 a[128:131], v74 offset:16384
v_max_f32 v249, v249, v7
v_mfma_f32_16x16x32_bf16 v[12:15], a[132:135], a[52:55], v[12:15]
v_exp_f32 v53, v53
v_mfma_f32_16x16x32_bf16 v[8:11], a[136:139], a[64:67], v[8:11]
ds_read_b128 a[132:135], v75 offset:16384
v_cvt_pk_bf16_f32 v52, v52, v53
v_mfma_f32_16x16x32_bf16 v[12:15], a[136:139], a[68:71], v[12:15]
v_exp_f32 v50, v50
v_mfma_f32_16x16x32_bf16 v[8:11], a[140:143], a[80:83], v[8:11]
ds_read_b128 a[136:139], v72 offset:16640
v_mfma_f32_16x16x32_bf16 v[12:15], a[140:143], a[84:87], v[12:15]
s_waitcnt lgkmcnt(3)
buffer_load_dwordx4 v246, s[56:59], s71 offen offset:2048 lds
v_mfma_f32_16x16x32_bf16 v[8:11], a[144:147], a[96:99], v[8:11]
ds_read_b128 a[140:143], v73 offset:16640
v_mfma_f32_16x16x32_bf16 v[12:15], a[144:147], a[100:103], v[12:15]
v_exp_f32 v51, v51
v_mfma_f32_16x16x32_bf16 v[8:11], a[148:151], a[112:115], v[8:11]
ds_read_b128 a[144:147], v74 offset:16640
v_cvt_pk_bf16_f32 v49, v50, v51
v_mfma_f32_16x16x32_bf16 v[12:15], a[148:151], a[116:119], v[12:15]
v_exp_f32 v54, v54
v_mfma_f32_16x16x32_bf16 v[16:19], a[152:155], a[0:3], v[64:67]
ds_read_b128 a[148:151], v75 offset:16640
v_mfma_f32_16x16x32_bf16 v[20:23], a[152:155], a[4:7], v[68:71]
buffer_load_dwordx4 v247, s[56:59], s71 offen offset:3072 lds
v_mfma_f32_16x16x32_bf16 v[16:19], a[156:159], a[16:19], v[16:19]
ds_read_b128 a[152:155], v72 offset:24576
v_mfma_f32_16x16x32_bf16 v[20:23], a[156:159], a[20:23], v[20:23]
s_waitcnt lgkmcnt(3)
v_max3_f32 v248, v248, v8, v9
v_max3_f32 v248, v248, v10, v11
v_mfma_f32_16x16x32_bf16 v[16:19], a[128:131], a[32:35], v[16:19]
ds_read_b128 a[156:159], v73 offset:24576
v_max3_f32 v249, v249, v12, v13
v_mfma_f32_16x16x32_bf16 v[20:23], a[128:131], a[36:39], v[20:23]
s_waitcnt vmcnt(18)
s_waitcnt vmcnt(8)
buffer_store_dword v233, v235, s[64:67], s83 offen offset:0 sc1
v_mfma_f32_16x16x32_bf16 v[16:19], a[132:135], a[48:51], v[16:19]
ds_read_b128 a[128:131], v74 offset:24576
v_max3_f32 v249, v249, v14, v15
v_mfma_f32_16x16x32_bf16 v[20:23], a[132:135], a[52:55], v[20:23]
v_exp_f32 v55, v55
v_mfma_f32_16x16x32_bf16 v[16:19], a[136:139], a[64:67], v[16:19]
ds_read_b128 a[132:135], v75 offset:24576
v_cvt_pk_bf16_f32 v53, v54, v55
v_mfma_f32_16x16x32_bf16 v[20:23], a[136:139], a[68:71], v[20:23]
v_exp_f32 v56, v56
v_mfma_f32_16x16x32_bf16 v[16:19], a[140:143], a[80:83], v[16:19]
ds_read_b128 a[136:139], v72 offset:24832
v_add_u32 v72, 0x10000, v72
v_mfma_f32_16x16x32_bf16 v[20:23], a[140:143], a[84:87], v[20:23]
s_waitcnt lgkmcnt(3)
v_exp_f32 v57, v57
v_mfma_f32_16x16x32_bf16 v[16:19], a[144:147], a[96:99], v[16:19]
ds_read_b128 a[140:143], v73 offset:24832
v_cvt_pk_bf16_f32 v50, v56, v57
v_mfma_f32_16x16x32_bf16 v[20:23], a[144:147], a[100:103], v[20:23]
v_exp_f32 v60, v60
v_mfma_f32_16x16x32_bf16 v[16:19], a[148:151], a[112:115], v[16:19]
ds_read_b128 a[144:147], v74 offset:24832
v_add_u32 v73, 0x10000, v73
v_mfma_f32_16x16x32_bf16 v[20:23], a[148:151], a[116:119], v[20:23]
v_exp_f32 v61, v61
v_mfma_f32_16x16x32_bf16 v[24:27], a[152:155], a[0:3], v[64:67]
ds_read_b128 a[148:151], v75 offset:24832
v_cvt_pk_bf16_f32 v54, v60, v61
v_mfma_f32_16x16x32_bf16 v[28:31], a[152:155], a[4:7], v[68:71]
v_exp_f32 v58, v58
v_mfma_f32_16x16x32_bf16 v[24:27], a[156:159], a[16:19], v[24:27]
s_barrier
v_exp_f32 v59, v59
v_mfma_f32_16x16x32_bf16 v[28:31], a[156:159], a[20:23], v[28:31]
s_waitcnt lgkmcnt(2)
v_max3_f32 v248, v248, v16, v17
v_max3_f32 v248, v248, v18, v19
v_mfma_f32_16x16x32_bf16 v[24:27], a[128:131], a[32:35], v[24:27]
ds_read_b64_tr_b16 a[156:157], v82 offset:0
v_max3_f32 v249, v249, v20, v21
v_mfma_f32_16x16x32_bf16 v[28:31], a[128:131], a[36:39], v[28:31]
s_add_u32 s71, s75, 131072
ds_read_b64_tr_b16 a[158:159], v82 offset:8192
v_max3_f32 v249, v249, v22, v23
v_mfma_f32_16x16x32_bf16 v[24:27], a[132:135], a[48:51], v[24:27]
s_add_u32 m0, s77, 0
ds_read_b64_tr_b16 a[128:129], v83 offset:0
ds_read_b64_tr_b16 a[130:131], v83 offset:8192
v_mfma_f32_16x16x32_bf16 v[28:31], a[132:135], a[52:55], v[28:31]
buffer_load_dwordx4 v224, s[52:55], s71 offen offset:0 lds
v_mfma_f32_16x16x32_bf16 v[24:27], a[136:139], a[64:67], v[24:27]
ds_read_b64_tr_b16 a[132:133], v84 offset:0
v_cvt_pk_bf16_f32 v51, v58, v59
v_mfma_f32_16x16x32_bf16 v[28:31], a[136:139], a[68:71], v[28:31]
ds_read_b64_tr_b16 a[134:135], v84 offset:8192
v_add_u32 v74, 0x10000, v74
v_mfma_f32_16x16x32_bf16 v[24:27], a[140:143], a[80:83], v[24:27]
ds_read_b64_tr_b16 a[136:137], v85 offset:0
v_add_u32 v75, 0x10000, v75
v_mfma_f32_16x16x32_bf16 v[28:31], a[140:143], a[84:87], v[28:31]
s_waitcnt lgkmcnt(5)
ds_read_b64_tr_b16 a[138:139], v85 offset:8192
v_mfma_f32_16x16x32_bf16 v[24:27], a[144:147], a[96:99], v[24:27]
ds_read_b64_tr_b16 a[140:141], v86 offset:0
ds_read_b64_tr_b16 a[142:143], v86 offset:8192
v_mfma_f32_16x16x32_bf16 v[28:31], a[144:147], a[100:103], v[28:31]
buffer_load_dwordx4 v225, s[52:55], s71 offen offset:1024 lds
v_mfma_f32_16x16x32_bf16 v[24:27], a[148:151], a[112:115], v[24:27]
ds_read_b64_tr_b16 a[144:145], v87 offset:0
v_mfma_f32_16x16x32_bf16 v[28:31], a[148:151], a[116:119], v[28:31]
ds_read_b64_tr_b16 a[146:147], v87 offset:8192
v_mfma_f32_16x16x32_bf16 a[192:195], a[200:203], v[32:35], a[192:195]
ds_read_b64_tr_b16 a[148:149], v88 offset:0
v_mfma_f32_16x16x32_bf16 a[196:199], a[200:203], v[36:39], a[196:199]
ds_read_b64_tr_b16 a[150:151], v88 offset:8192
v_mfma_f32_16x16x32_bf16 v[96:99], a[156:159], v[32:35], v[96:99]
ds_read_b64_tr_b16 a[152:153], v89 offset:0
ds_read_b64_tr_b16 a[154:155], v89 offset:8192
v_mfma_f32_16x16x32_bf16 v[100:103], a[156:159], v[36:39], v[100:103]
s_waitcnt lgkmcnt(6)
buffer_load_dwordx4 v226, s[52:55], s71 offen offset:2048 lds
v_mfma_f32_16x16x32_bf16 v[104:107], a[128:131], v[32:35], v[104:107]
ds_read_b64_tr_b16 a[156:157], v82 offset:256
v_max3_f32 v248, v248, v24, v25
v_mfma_f32_16x16x32_bf16 v[108:111], a[128:131], v[36:39], v[108:111]
ds_read_b64_tr_b16 a[158:159], v82 offset:8448
v_max3_f32 v248, v248, v26, v27
v_mfma_f32_16x16x32_bf16 v[112:115], a[132:135], v[32:35], v[112:115]
ds_read_b64_tr_b16 a[128:129], v83 offset:256
v_max3_f32 v249, v249, v28, v29
v_mfma_f32_16x16x32_bf16 v[116:119], a[132:135], v[36:39], v[116:119]
ds_read_b64_tr_b16 a[130:131], v83 offset:8448
v_max3_f32 v249, v249, v30, v31
v_mfma_f32_16x16x32_bf16 v[120:123], a[136:139], v[32:35], v[120:123]
ds_read_b64_tr_b16 a[132:133], v84 offset:256
ds_read_b64_tr_b16 a[134:135], v84 offset:8448
v_mfma_f32_16x16x32_bf16 v[124:127], a[136:139], v[36:39], v[124:127]
buffer_load_dwordx4 v227, s[52:55], s71 offen offset:3072 lds
v_mfma_f32_16x16x32_bf16 v[128:131], a[140:143], v[32:35], v[128:131]
ds_read_b64_tr_b16 a[136:137], v85 offset:256
v_max_f32 v250, v248, v249
v_mfma_f32_16x16x32_bf16 v[132:135], a[140:143], v[36:39], v[132:135]
s_waitcnt lgkmcnt(5)
ds_read_b64_tr_b16 a[138:139], v85 offset:8448
v_mfma_f32_16x16x32_bf16 v[136:139], a[144:147], v[32:35], v[136:139]
s_add_u32 s71, s75, 135168
ds_read_b64_tr_b16 a[140:141], v86 offset:256
v_cmp_lt_f32 vcc, s85, v250
v_mfma_f32_16x16x32_bf16 v[140:143], a[144:147], v[36:39], v[140:143]
s_add_u32 m0, s77, 4096
ds_read_b64_tr_b16 a[142:143], v86 offset:8448
v_mfma_f32_16x16x32_bf16 v[144:147], a[148:151], v[32:35], v[144:147]
s_cbranch_vccnz L_rareA2
L_backA2:
ds_read_b64_tr_b16 a[144:145], v87 offset:256
ds_read_b64_tr_b16 a[146:147], v87 offset:8448
v_mfma_f32_16x16x32_bf16 v[148:151], a[148:151], v[36:39], v[148:151]
buffer_load_dwordx4 v228, s[52:55], s71 offen offset:0 lds
v_mfma_f32_16x16x32_bf16 v[152:155], a[152:155], v[32:35], v[152:155]
ds_read_b64_tr_b16 a[148:149], v88 offset:256
v_mfma_f32_16x16x32_bf16 v[156:159], a[152:155], v[36:39], v[156:159]
ds_read_b64_tr_b16 a[150:151], v88 offset:8448
v_mfma_f32_16x16x32_bf16 v[160:163], a[156:159], v[32:35], v[160:163]
ds_read_b64_tr_b16 a[152:153], v89 offset:256
v_mfma_f32_16x16x32_bf16 v[164:167], a[156:159], v[36:39], v[164:167]
s_waitcnt lgkmcnt(5)
ds_read_b64_tr_b16 a[154:155], v89 offset:8448
v_mfma_f32_16x16x32_bf16 v[168:171], a[128:131], v[32:35], v[168:171]
v_exp_f32 v62, v62
v_mfma_f32_16x16x32_bf16 v[172:175], a[128:131], v[36:39], v[172:175]
buffer_load_dwordx4 v229, s[52:55], s71 offen offset:1024 lds
v_mfma_f32_16x16x32_bf16 v[176:179], a[132:135], v[32:35], v[176:179]
ds_read_b64_tr_b16 a[128:129], v82 offset:16384
v_mfma_f32_16x16x32_bf16 v[180:183], a[132:135], v[36:39], v[180:183]
ds_read_b64_tr_b16 a[130:131], v82 offset:24576
v_mfma_f32_16x16x32_bf16 v[184:187], a[136:139], v[32:35], v[184:187]
ds_read_b64_tr_b16 a[132:133], v83 offset:16384
v_mfma_f32_16x16x32_bf16 v[188:191], a[136:139], v[36:39], v[188:191]
ds_read_b64_tr_b16 a[134:135], v83 offset:24576
v_mfma_f32_16x16x32_bf16 v[192:195], a[140:143], v[32:35], v[192:195]
ds_read_b64_tr_b16 a[136:137], v84 offset:16384
ds_read_b64_tr_b16 a[138:139], v84 offset:24576
v_mfma_f32_16x16x32_bf16 v[196:199], a[140:143], v[36:39], v[196:199]
s_waitcnt lgkmcnt(6)
buffer_load_dwordx4 v230, s[52:55], s71 offen offset:2048 lds
v_mfma_f32_16x16x32_bf16 v[200:203], a[144:147], v[32:35], v[200:203]
ds_read_b64_tr_b16 a[140:141], v85 offset:16384
v_mfma_f32_16x16x32_bf16 v[204:207], a[144:147], v[36:39], v[204:207]
ds_read_b64_tr_b16 a[142:143], v85 offset:24576
v_mfma_f32_16x16x32_bf16 v[208:211], a[148:151], v[32:35], v[208:211]
ds_read_b64_tr_b16 a[144:145], v86 offset:16384
v_mfma_f32_16x16x32_bf16 v[212:215], a[148:151], v[36:39], v[212:215]
ds_read_b64_tr_b16 a[146:147], v86 offset:24576
v_exp_f32 v63, v63
v_mfma_f32_16x16x32_bf16 v[216:219], a[152:155], v[32:35], v[216:219]
ds_read_b64_tr_b16 a[148:149], v87 offset:16384
ds_read_b64_tr_b16 a[150:151], v87 offset:24576
v_cvt_pk_bf16_f32 v55, v62, v63
v_mfma_f32_16x16x32_bf16 v[220:223], a[152:155], v[36:39], v[220:223]
buffer_load_dwordx4 v231, s[52:55], s71 offen offset:3072 lds
buffer_load_dword v236, v234, s[64:67], s82 offen offset:0 sc1
v_mfma_f32_16x16x32_bf16 a[192:195], a[200:203], v[48:51], a[192:195]
ds_read_b64_tr_b16 a[152:153], v88 offset:16384
v_exp_f32 v0, v0
v_mfma_f32_16x16x32_bf16 a[196:199], a[200:203], v[52:55], a[196:199]
s_waitcnt lgkmcnt(5)
ds_read_b64_tr_b16 a[154:155], v88 offset:24576
v_exp_f32 v1, v1
v_mfma_f32_16x16x32_bf16 v[96:99], a[128:131], v[48:51], v[96:99]
ds_read_b64_tr_b16 a[156:157], v89 offset:16384
v_exp_f32 v4, v4
v_mfma_f32_16x16x32_bf16 v[100:103], a[128:131], v[52:55], v[100:103]
ds_read_b64_tr_b16 a[158:159], v89 offset:24576
v_exp_f32 v5, v5
v_mfma_f32_16x16x32_bf16 v[104:107], a[132:135], v[48:51], v[104:107]
ds_read_b64_tr_b16 a[128:129], v82 offset:16640
v_exp_f32 v2, v2
v_mfma_f32_16x16x32_bf16 v[108:111], a[132:135], v[52:55], v[108:111]
ds_read_b64_tr_b16 a[130:131], v82 offset:24832
v_add_u32 v82, 0x10000, v82
v_mfma_f32_16x16x32_bf16 v[112:115], a[136:139], v[48:51], v[112:115]
ds_read_b64_tr_b16 a[132:133], v83 offset:16640
v_exp_f32 v3, v3
v_mfma_f32_16x16x32_bf16 v[116:119], a[136:139], v[52:55], v[116:119]
ds_read_b64_tr_b16 a[134:135], v83 offset:24832
v_add_u32 v83, 0x10000, v83
v_mfma_f32_16x16x32_bf16 v[120:123], a[140:143], v[48:51], v[120:123]
ds_read_b64_tr_b16 a[136:137], v84 offset:16640
v_exp_f32 v6, v6
v_mfma_f32_16x16x32_bf16 v[124:127], a[140:143], v[52:55], v[124:127]
s_waitcnt lgkmcnt(5)
ds_read_b64_tr_b16 a[138:139], v84 offset:24832
v_add_u32 v84, 0x10000, v84
v_mfma_f32_16x16x32_bf16 v[128:131], a[144:147], v[48:51], v[128:131]
ds_read_b64_tr_b16 a[140:141], v85 offset:16640
v_exp_f32 v7, v7
v_mfma_f32_16x16x32_bf16 v[132:135], a[144:147], v[52:55], v[132:135]
ds_read_b64_tr_b16 a[142:143], v85 offset:24832
v_add_u32 v85, 0x10000, v85
v_mfma_f32_16x16x32_bf16 v[136:139], a[148:151], v[48:51], v[136:139]
ds_read_b64_tr_b16 a[144:145], v86 offset:16640
v_cvt_pk_bf16_f32 v0, v0, v1
v_mfma_f32_16x16x32_bf16 v[140:143], a[148:151], v[52:55], v[140:143]
ds_read_b64_tr_b16 a[146:147], v86 offset:24832
v_add_u32 v86, 0x10000, v86
v_mfma_f32_16x16x32_bf16 v[144:147], a[152:155], v[48:51], v[144:147]
ds_read_b64_tr_b16 a[148:149], v87 offset:16640
v_cvt_pk_bf16_f32 v4, v4, v5
v_mfma_f32_16x16x32_bf16 v[148:151], a[152:155], v[52:55], v[148:151]
ds_read_b64_tr_b16 a[150:151], v87 offset:24832
v_add_u32 v87, 0x10000, v87
v_mfma_f32_16x16x32_bf16 v[152:155], a[156:159], v[48:51], v[152:155]
ds_read_b64_tr_b16 a[152:153], v88 offset:16640
v_cvt_pk_bf16_f32 v1, v2, v3
v_mfma_f32_16x16x32_bf16 v[156:159], a[156:159], v[52:55], v[156:159]
s_waitcnt lgkmcnt(5)
ds_read_b64_tr_b16 a[154:155], v88 offset:24832
v_cvt_pk_bf16_f32 v5, v6, v7
v_mfma_f32_16x16x32_bf16 v[160:163], a[128:131], v[48:51], v[160:163]
ds_read_b64_tr_b16 a[156:157], v89 offset:16640
v_add_u32 v88, 0x10000, v88
v_mfma_f32_16x16x32_bf16 v[164:167], a[128:131], v[52:55], v[164:167]
ds_read_b64_tr_b16 a[158:159], v89 offset:24832
v_add_u32 v89, 0x10000, v89
v_mfma_f32_16x16x32_bf16 v[168:171], a[132:135], v[48:51], v[168:171]
s_barrier
ds_read_b128 a[128:131], v72 offset:0
v_mfma_f32_16x16x32_bf16 v[172:175], a[132:135], v[52:55], v[172:175]
v_exp_f32 v8, v8
v_mfma_f32_16x16x32_bf16 v[176:179], a[136:139], v[48:51], v[176:179]
ds_read_b128 a[132:135], v73 offset:0
v_mfma_f32_16x16x32_bf16 v[180:183], a[136:139], v[52:55], v[180:183]
s_add_u32 s71, s75, 98304
v_exp_f32 v9, v9
v_mfma_f32_16x16x32_bf16 v[184:187], a[140:143], v[48:51], v[184:187]
s_add_u32 m0, s77, 32768
ds_read_b128 a[136:139], v74 offset:0
v_cvt_pk_bf16_f32 v2, v8, v9
v_mfma_f32_16x16x32_bf16 v[188:191], a[140:143], v[52:55], v[188:191]
s_waitcnt lgkmcnt(3)
buffer_load_dwordx4 v244, s[56:59], s71 offen offset:0 lds
v_mfma_f32_16x16x32_bf16 v[192:195], a[144:147], v[48:51], v[192:195]
ds_read_b128 a[140:143], v75 offset:0
v_mfma_f32_16x16x32_bf16 v[196:199], a[144:147], v[52:55], v[196:199]
v_exp_f32 v12, v12
v_mfma_f32_16x16x32_bf16 v[200:203], a[148:151], v[48:51], v[200:203]
ds_read_b128 a[144:147], v72 offset:256
v_mfma_f32_16x16x32_bf16 v[204:207], a[148:151], v[52:55], v[204:207]
v_exp_f32 v13, v13
v_mfma_f32_16x16x32_bf16 v[208:211], a[152:155], v[48:51], v[208:211]
ds_read_b128 a[148:151], v73 offset:256
v_cvt_pk_bf16_f32 v6, v12, v13
v_mfma_f32_16x16x32_bf16 v[212:215], a[152:155], v[52:55], v[212:215]
buffer_load_dwordx4 v245, s[56:59], s71 offen offset:1024 lds
v_mfma_f32_16x16x32_bf16 v[216:219], a[156:159], v[48:51], v[216:219]
s_cmp_lg_u32 s84, 0
ds_read_b128 a[152:155], v74 offset:256
v_mfma_f32_16x16x32_bf16 v[220:223], a[156:159], v[52:55], v[220:223]
s_cbranch_scc1 L_rareB2
L_backB2:
s_waitcnt lgkmcnt(3)
v_mfma_f32_16x16x32_bf16 v[32:35], a[128:131], a[0:3], v[64:67]
ds_read_b128 a[156:159], v75 offset:256
v_mfma_f32_16x16x32_bf16 v[36:39], a[128:131], a[4:7], v[68:71]
v_exp_f32 v10, v10
v_mfma_f32_16x16x32_bf16 v[32:35], a[132:135], a[16:19], v[32:35]
ds_read_b128 a[128:131], v72 offset:8192
v_mfma_f32_16x16x32_bf16 v[36:39], a[132:135], a[20:23], v[36:39]
buffer_load_dwordx4 v246, s[56:59], s71 offen offset:2048 lds
v_mfma_f32_16x16x32_bf16 v[32:35], a[136:139], a[32:35], v[32:35]
ds_read_b128 a[132:135], v73 offset:8192
v_mfma_f32_16x16x32_bf16 v[36:39], a[136:139], a[36:39], v[36:39]
s_waitcnt vmcnt(3)
v_cmp_ne_u32 vcc, v236, v232
v_mfma_f32_16x16x32_bf16 v[32:35], a[140:143], a[48:51], v[32:35]
s_waitcnt lgkmcnt(2)
ds_read_b128 a[136:139], v74 offset:8192
v_mfma_f32_16x16x32_bf16 v[36:39], a[140:143], a[52:55], v[36:39]
s_cbranch_vccnz L_spm
L_bkm:
v_exp_f32 v11, v11
v_mfma_f32_16x16x32_bf16 v[32:35], a[144:147], a[64:67], v[32:35]
ds_read_b128 a[140:143], v75 offset:8192
v_cvt_pk_bf16_f32 v3, v10, v11
v_mfma_f32_16x16x32_bf16 v[36:39], a[144:147], a[68:71], v[36:39]
buffer_load_dwordx4 v247, s[56:59], s71 offen offset:3072 lds
buffer_load_dword v237, v235, s[64:67], s96 offen offset:0 sc1
v_mfma_f32_16x16x32_bf16 v[32:35], a[148:151], a[80:83], v[32:35]
ds_read_b128 a[144:147], v72 offset:8448
v_mfma_f32_16x16x32_bf16 v[36:39], a[148:151], a[84:87], v[36:39]
v_exp_f32 v14, v14
v_mfma_f32_16x16x32_bf16 v[32:35], a[152:155], a[96:99], v[32:35]
ds_read_b128 a[148:151], v73 offset:8448
v_mfma_f32_16x16x32_bf16 v[36:39], a[152:155], a[100:103], v[36:39]
s_add_u32 s71, s75, 102400
v_exp_f32 v15, v15
v_mfma_f32_16x16x32_bf16 v[32:35], a[156:159], a[112:115], v[32:35]
s_add_u32 m0, s77, 36864
ds_read_b128 a[152:155], v74 offset:8448
v_cvt_pk_bf16_f32 v7, v14, v15
v_mfma_f32_16x16x32_bf16 v[36:39], a[156:159], a[116:119], v[36:39]
s_waitcnt lgkmcnt(3)
buffer_load_dwordx4 v244, s[56:59], s71 offen offset:0 lds
v_mfma_f32_16x16x32_bf16 v[40:43], a[128:131], a[0:3], v[64:67]
ds_read_b128 a[156:159], v75 offset:8448
v_mfma_f32_16x16x32_bf16 v[44:47], a[128:131], a[4:7], v[68:71]
v_exp_f32 v16, v16
v_mfma_f32_16x16x32_bf16 v[40:43], a[132:135], a[16:19], v[40:43]
ds_read_b128 a[128:131], v72 offset:16384
v_mfma_f32_16x16x32_bf16 v[44:47], a[132:135], a[20:23], v[44:47]
v_max3_f32 v248, v32, v33, v34
v_max_f32 v248, v248, v35
v_mfma_f32_16x16x32_bf16 v[40:43], a[136:139], a[32:35], v[40:43]
ds_read_b128 a[132:135], v73 offset:16384
v_max3_f32 v249, v36, v37, v38
v_mfma_f32_16x16x32_bf16 v[44:47], a[136:139], a[36:39], v[44:47]
buffer_load_dwordx4 v245, s[56:59], s71 offen offset:1024 lds
v_mfma_f32_16x16x32_bf16 v[40:43], a[140:143], a[48:51], v[40:43]
ds_read_b128 a[136:139], v74 offset:16384
v_max_f32 v249, v249, v39
v_mfma_f32_16x16x32_bf16 v[44:47], a[140:143], a[52:55], v[44:47]
s_waitcnt lgkmcnt(3)
v_exp_f32 v17, v17
v_mfma_f32_16x16x32_bf16 v[40:43], a[144:147], a[64:67], v[40:43]
ds_read_b128 a[140:143], v75 offset:16384
v_cvt_pk_bf16_f32 v16, v16, v17
v_mfma_f32_16x16x32_bf16 v[44:47], a[144:147], a[68:71], v[44:47]
v_exp_f32 v20, v20
v_mfma_f32_16x16x32_bf16 v[40:43], a[148:151], a[80:83], v[40:43]
ds_read_b128 a[144:147], v72 offset:16640
v_mfma_f32_16x16x32_bf16 v[44:47], a[148:151], a[84:87], v[44:47]
buffer_load_dwordx4 v246, s[56:59], s71 offen offset:2048 lds
v_mfma_f32_16x16x32_bf16 v[40:43], a[152:155], a[96:99], v[40:43]
ds_read_b128 a[148:151], v73 offset:16640
v_mfma_f32_16x16x32_bf16 v[44:47], a[152:155], a[100:103], v[44:47]
v_exp_f32 v21, v21
v_mfma_f32_16x16x32_bf16 v[40:43], a[156:159], a[112:115], v[40:43]
ds_read_b128 a[152:155], v74 offset:16640
v_cvt_pk_bf16_f32 v20, v20, v21
v_mfma_f32_16x16x32_bf16 v[44:47], a[156:159], a[116:119], v[44:47]
s_waitcnt lgkmcnt(3)
v_exp_f32 v18, v18
v_mfma_f32_16x16x32_bf16 v[48:51], a[128:131], a[0:3], v[64:67]
ds_read_b128 a[156:159], v75 offset:16640
v_mfma_f32_16x16x32_bf16 v[52:55], a[128:131], a[4:7], v[68:71]
buffer_load_dwordx4 v247, s[56:59], s71 offen offset:3072 lds
v_mfma_f32_16x16x32_bf16 v[48:51], a[132:135], a[16:19], v[48:51]
ds_read_b128 a[128:131], v72 offset:24576
v_mfma_f32_16x16x32_bf16 v[52:55], a[132:135], a[20:23], v[52:55]
v_max3_f32 v248, v248, v40, v41
v_max3_f32 v248, v248, v42, v43
v_mfma_f32_16x16x32_bf16 v[48:51], a[136:139], a[32:35], v[48:51]
ds_read_b128 a[132:135], v73 offset:24576
v_max3_f32 v249, v249, v44, v45
v_mfma_f32_16x16x32_bf16 v[52:55], a[136:139], a[36:39], v[52:55]
v_max3_f32 v249, v249, v46, v47
v_mfma_f32_16x16x32_bf16 v[48:51], a[140:143], a[48:51], v[48:51]
ds_read_b128 a[136:139], v74 offset:24576
v_mfma_f32_16x16x32_bf16 v[52:55], a[140:143], a[52:55], v[52:55]
s_waitcnt lgkmcnt(3)
v_exp_f32 v19, v19
v_mfma_f32_16x16x32_bf16 v[48:51], a[144:147], a[64:67], v[48:51]
ds_read_b128 a[140:143], v75 offset:24576
v_cvt_pk_bf16_f32 v17, v18, v19
v_mfma_f32_16x16x32_bf16 v[52:55], a[144:147], a[68:71], v[52:55]
v_exp_f32 v22, v22
v_mfma_f32_16x16x32_bf16 v[48:51], a[148:151], a[80:83], v[48:51]
ds_read_b128 a[144:147], v72 offset:24832
v_add_u32 v72, 0xfffe8000, v72
v_mfma_f32_16x16x32_bf16 v[52:55], a[148:151], a[84:87], v[52:55]
v_exp_f32 v23, v23
v_mfma_f32_16x16x32_bf16 v[48:51], a[152:155], a[96:99], v[48:51]
ds_read_b128 a[148:151], v73 offset:24832
v_cvt_pk_bf16_f32 v21, v22, v23
v_mfma_f32_16x16x32_bf16 v[52:55], a[152:155], a[100:103], v[52:55]
v_exp_f32 v24, v24
v_mfma_f32_16x16x32_bf16 v[48:51], a[156:159], a[112:115], v[48:51]
ds_read_b128 a[152:155], v74 offset:24832
v_add_u32 v73, 0xfffe8000, v73
v_mfma_f32_16x16x32_bf16 v[52:55], a[156:159], a[116:119], v[52:55]
s_waitcnt lgkmcnt(3)
v_exp_f32 v25, v25
v_mfma_f32_16x16x32_bf16 v[56:59], a[128:131], a[0:3], v[64:67]
ds_read_b128 a[156:159], v75 offset:24832
v_cvt_pk_bf16_f32 v18, v24, v25
v_mfma_f32_16x16x32_bf16 v[60:63], a[128:131], a[4:7], v[68:71]
v_add_u32 v74, 0xfffe8000, v74
s_waitcnt vmcnt(4)
v_cmp_ne_u32 vcc, v237, v233
v_mfma_f32_16x16x32_bf16 v[56:59], a[132:135], a[16:19], v[56:59]
s_barrier
v_exp_f32 v28, v28
v_mfma_f32_16x16x32_bf16 v[60:63], a[132:135], a[20:23], v[60:63]
s_cbranch_vccnz L_re
L_reb:
buffer_load_dwordx4 a[8:11], v238, s[64:67], s92 offen offset:0 sc1
v_mfma_f32_16x16x32_bf16 v[56:59], a[136:139], a[32:35], v[56:59]
ds_read_b64_tr_b16 a[132:133], v82 offset:0
v_max3_f32 v248, v248, v48, v49
v_mfma_f32_16x16x32_bf16 v[60:63], a[136:139], a[36:39], v[60:63]
s_add_u32 s71, s75, 163840
ds_read_b64_tr_b16 a[134:135], v82 offset:8192
v_max3_f32 v248, v248, v50, v51
v_mfma_f32_16x16x32_bf16 v[56:59], a[140:143], a[48:51], v[56:59]
s_add_u32 m0, s77, 65536
ds_read_b64_tr_b16 a[136:137], v83 offset:0
ds_read_b64_tr_b16 a[138:139], v83 offset:8192
v_mfma_f32_16x16x32_bf16 v[60:63], a[140:143], a[52:55], v[60:63]
s_waitcnt lgkmcnt(4)
buffer_load_dwordx4 v224, s[52:55], s71 offen offset:0 lds
v_mfma_f32_16x16x32_bf16 v[56:59], a[144:147], a[64:67], v[56:59]
ds_read_b64_tr_b16 a[140:141], v84 offset:0
ds_read_b64_tr_b16 a[142:143], v84 offset:8192
v_mfma_f32_16x16x32_bf16 v[60:63], a[144:147], a[68:71], v[60:63]
buffer_load_dwordx4 a[12:15], v238, s[64:67], s92 offen offset:1024 sc1
v_mfma_f32_16x16x32_bf16 v[56:59], a[148:151], a[80:83], v[56:59]
ds_read_b64_tr_b16 a[144:145], v85 offset:0
v_max3_f32 v249, v249, v52, v53
v_mfma_f32_16x16x32_bf16 v[60:63], a[148:151], a[84:87], v[60:63]
ds_read_b64_tr_b16 a[146:147], v85 offset:8192
v_max3_f32 v249, v249, v54, v55
v_mfma_f32_16x16x32_bf16 v[56:59], a[152:155], a[96:99], v[56:59]
ds_read_b64_tr_b16 a[148:149], v86 offset:0
ds_read_b64_tr_b16 a[150:151], v86 offset:8192
v_mfma_f32_16x16x32_bf16 v[60:63], a[152:155], a[100:103], v[60:63]
buffer_load_dwordx4 v225, s[52:55], s71 offen offset:1024 lds
v_mfma_f32_16x16x32_bf16 v[56:59], a[156:159], a[112:115], v[56:59]
ds_read_b64_tr_b16 a[152:153], v87 offset:0
ds_read_b64_tr_b16 a[154:155], v87 offset:8192
v_mfma_f32_16x16x32_bf16 v[60:63], a[156:159], a[116:119], v[60:63]
s_waitcnt lgkmcnt(6)
buffer_load_dwordx4 a[24:27], v238, s[64:67], s92 offen offset:2048 sc1
v_mfma_f32_16x16x32_bf16 a[192:195], a[200:203], v[0:3], a[192:195]
ds_read_b64_tr_b16 a[156:157], v88 offset:0
v_add_u32 v75, 0xfffe8000, v75
v_mfma_f32_16x16x32_bf16 a[196:199], a[200:203], v[4:7], a[196:199]
ds_read_b64_tr_b16 a[158:159], v88 offset:8192
v_mfma_f32_16x16x32_bf16 v[96:99], a[132:135], v[0:3], v[96:99]
ds_read_b64_tr_b16 a[128:129], v89 offset:0
ds_read_b64_tr_b16 a[130:131], v89 offset:8192
v_mfma_f32_16x16x32_bf16 v[100:103], a[132:135], v[4:7], v[100:103]
buffer_load_dwordx4 v226, s[52:55], s71 offen offset:2048 lds
v_mfma_f32_16x16x32_bf16 v[104:107], a[136:139], v[0:3], v[104:107]
ds_read_b64_tr_b16 a[132:133], v82 offset:256
ds_read_b64_tr_b16 a[134:135], v82 offset:8448
v_mfma_f32_16x16x32_bf16 v[108:111], a[136:139], v[4:7], v[108:111]
buffer_load_dwordx2 a[56:57], v238, s[64:67], s93 offen offset:0 sc1
v_mfma_f32_16x16x32_bf16 v[112:115], a[140:143], v[0:3], v[112:115]
ds_read_b64_tr_b16 a[136:137], v83 offset:256
v_add_u32 v238, 28672, v238
v_mfma_f32_16x16x32_bf16 v[116:119], a[140:143], v[4:7], v[116:119]
s_waitcnt lgkmcnt(5)
ds_read_b64_tr_b16 a[138:139], v83 offset:8448
v_max3_f32 v248, v248, v56, v57
v_mfma_f32_16x16x32_bf16 v[120:123], a[144:147], v[0:3], v[120:123]
ds_read_b64_tr_b16 a[140:141], v84 offset:256
ds_read_b64_tr_b16 a[142:143], v84 offset:8448
v_mfma_f32_16x16x32_bf16 v[124:127], a[144:147], v[4:7], v[124:127]
buffer_load_dwordx4 v227, s[52:55], s71 offen offset:3072 lds
v_mfma_f32_16x16x32_bf16 v[128:131], a[148:151], v[0:3], v[128:131]
ds_read_b64_tr_b16 a[144:145], v85 offset:256
ds_read_b64_tr_b16 a[146:147], v85 offset:8448
v_mfma_f32_16x16x32_bf16 v[132:135], a[148:151], v[4:7], v[132:135]
buffer_load_dwordx4 a[28:31], v238, s[64:67], s92 offen offset:0 sc1
v_max3_f32 v248, v248, v58, v59
v_max3_f32 v249, v249, v60, v61
v_max3_f32 v249, v249, v62, v63
v_max_f32 v250, v248, v249
v_mfma_f32_16x16x32_bf16 v[136:139], a[152:155], v[0:3], v[136:139]
s_add_u32 s71, s75, 167936
ds_read_b64_tr_b16 a[148:149], v86 offset:256
v_cmp_lt_f32 vcc, s85, v250
v_mfma_f32_16x16x32_bf16 v[140:143], a[152:155], v[4:7], v[140:143]
s_add_u32 m0, s77, 69632
ds_read_b64_tr_b16 a[150:151], v86 offset:8448
v_mfma_f32_16x16x32_bf16 v[144:147], a[156:159], v[0:3], v[144:147]
s_cbranch_vccnz L_rareA3
L_backA3:
ds_read_b64_tr_b16 a[152:153], v87 offset:256
ds_read_b64_tr_b16 a[154:155], v87 offset:8448
v_mfma_f32_16x16x32_bf16 v[148:151], a[156:159], v[4:7], v[148:151]
s_waitcnt lgkmcnt(6)
buffer_load_dwordx4 v228, s[52:55], s71 offen offset:0 lds
v_mfma_f32_16x16x32_bf16 v[152:155], a[128:131], v[0:3], v[152:155]
ds_read_b64_tr_b16 a[156:157], v88 offset:256
ds_read_b64_tr_b16 a[158:159], v88 offset:8448
v_mfma_f32_16x16x32_bf16 v[156:159], a[128:131], v[4:7], v[156:159]
buffer_load_dwordx4 a[40:43], v238, s[64:67], s92 offen offset:1024 sc1
v_mfma_f32_16x16x32_bf16 v[160:163], a[132:135], v[0:3], v[160:163]
ds_read_b64_tr_b16 a[128:129], v89 offset:256
v_mfma_f32_16x16x32_bf16 v[164:167], a[132:135], v[4:7], v[164:167]
ds_read_b64_tr_b16 a[130:131], v89 offset:8448
v_mfma_f32_16x16x32_bf16 v[168:171], a[136:139], v[0:3], v[168:171]
v_exp_f32 v29, v29
v_mfma_f32_16x16x32_bf16 v[172:175], a[136:139], v[4:7], v[172:175]
buffer_load_dwordx4 v229, s[52:55], s71 offen offset:1024 lds
v_mfma_f32_16x16x32_bf16 v[176:179], a[140:143], v[0:3], v[176:179]
ds_read_b64_tr_b16 a[136:137], v82 offset:16384
ds_read_b64_tr_b16 a[138:139], v82 offset:24576
v_mfma_f32_16x16x32_bf16 v[180:183], a[140:143], v[4:7], v[180:183]
s_waitcnt lgkmcnt(4)
buffer_load_dwordx4 a[44:47], v238, s[64:67], s92 offen offset:2048 sc1
v_mfma_f32_16x16x32_bf16 v[184:187], a[144:147], v[0:3], v[184:187]
ds_read_b64_tr_b16 a[140:141], v83 offset:16384
v_cvt_pk_bf16_f32 v22, v28, v29
v_mfma_f32_16x16x32_bf16 v[188:191], a[144:147], v[4:7], v[188:191]
ds_read_b64_tr_b16 a[142:143], v83 offset:24576
v_mfma_f32_16x16x32_bf16 v[192:195], a[148:151], v[0:3], v[192:195]
ds_read_b64_tr_b16 a[144:145], v84 offset:16384
ds_read_b64_tr_b16 a[146:147], v84 offset:24576
v_mfma_f32_16x16x32_bf16 v[196:199], a[148:151], v[4:7], v[196:199]
buffer_load_dwordx4 v230, s[52:55], s71 offen offset:2048 lds
v_mfma_f32_16x16x32_bf16 v[200:203], a[152:155], v[0:3], v[200:203]
ds_read_b64_tr_b16 a[148:149], v85 offset:16384
ds_read_b64_tr_b16 a[150:151], v85 offset:24576
v_mfma_f32_16x16x32_bf16 v[204:207], a[152:155], v[4:7], v[204:207]
buffer_load_dwordx2 a[58:59], v238, s[64:67], s93 offen offset:0 sc1
v_mfma_f32_16x16x32_bf16 v[208:211], a[156:159], v[0:3], v[208:211]
ds_read_b64_tr_b16 a[152:153], v86 offset:16384
v_add_u32 v238, 28672, v238
v_exp_f32 v26, v26
v_exp_f32 v27, v27
v_mfma_f32_16x16x32_bf16 v[212:215], a[156:159], v[4:7], v[212:215]
s_waitcnt lgkmcnt(5)
ds_read_b64_tr_b16 a[154:155], v86 offset:24576
v_exp_f32 v30, v30
v_exp_f32 v31, v31
v_cvt_pk_bf16_f32 v19, v26, v27
v_mfma_f32_16x16x32_bf16 v[216:219], a[128:131], v[0:3], v[216:219]
ds_read_b64_tr_b16 a[156:157], v87 offset:16384
ds_read_b64_tr_b16 a[158:159], v87 offset:24576
v_cvt_pk_bf16_f32 v23, v30, v31
v_mfma_f32_16x16x32_bf16 v[220:223], a[128:131], v[4:7], v[220:223]
buffer_load_dwordx4 v231, s[52:55], s71 offen offset:3072 lds
v_mfma_f32_16x16x32_bf16 a[192:195], a[200:203], v[16:19], a[192:195]
ds_read_b64_tr_b16 a[128:129], v88 offset:16384
ds_read_b64_tr_b16 a[130:131], v88 offset:24576
v_mfma_f32_16x16x32_bf16 a[196:199], a[200:203], v[20:23], a[196:199]
buffer_load_dwordx4 a[60:63], v238, s[64:67], s92 offen offset:0 sc1
v_mfma_f32_16x16x32_bf16 v[96:99], a[136:139], v[16:19], v[96:99]
ds_read_b64_tr_b16 a[132:133], v89 offset:16384
v_exp_f32 v32, v32
v_mfma_f32_16x16x32_bf16 v[100:103], a[136:139], v[20:23], v[100:103]
ds_read_b64_tr_b16 a[134:135], v89 offset:24576
v_exp_f32 v33, v33
v_mfma_f32_16x16x32_bf16 v[104:107], a[140:143], v[16:19], v[104:107]
ds_read_b64_tr_b16 a[136:137], v82 offset:16640
v_exp_f32 v36, v36
v_mfma_f32_16x16x32_bf16 v[108:111], a[140:143], v[20:23], v[108:111]
s_waitcnt lgkmcnt(5)
ds_read_b64_tr_b16 a[138:139], v82 offset:24832
v_add_u32 v82, 0xfffe8000, v82
v_mfma_f32_16x16x32_bf16 v[112:115], a[144:147], v[16:19], v[112:115]
ds_read_b64_tr_b16 a[140:141], v83 offset:16640
ds_read_b64_tr_b16 a[142:143], v83 offset:24832
v_mfma_f32_16x16x32_bf16 v[116:119], a[144:147], v[20:23], v[116:119]
buffer_load_dwordx4 a[72:75], v238, s[64:67], s92 offen offset:1024 sc1
v_mfma_f32_16x16x32_bf16 v[120:123], a[148:151], v[16:19], v[120:123]
ds_read_b64_tr_b16 a[144:145], v84 offset:16640
v_add_u32 v83, 0xfffe8000, v83
v_mfma_f32_16x16x32_bf16 v[124:127], a[148:151], v[20:23], v[124:127]
ds_read_b64_tr_b16 a[146:147], v84 offset:24832
v_add_u32 v84, 0xfffe8000, v84
v_mfma_f32_16x16x32_bf16 v[128:131], a[152:155], v[16:19], v[128:131]
ds_read_b64_tr_b16 a[148:149], v85 offset:16640
v_exp_f32 v37, v37
v_mfma_f32_16x16x32_bf16 v[132:135], a[152:155], v[20:23], v[132:135]
ds_read_b64_tr_b16 a[150:151], v85 offset:24832
v_add_u32 v85, 0xfffe8000, v85
v_mfma_f32_16x16x32_bf16 v[136:139], a[156:159], v[16:19], v[136:139]
ds_read_b64_tr_b16 a[152:153], v86 offset:16640
ds_read_b64_tr_b16 a[154:155], v86 offset:24832
v_mfma_f32_16x16x32_bf16 v[140:143], a[156:159], v[20:23], v[140:143]
s_waitcnt lgkmcnt(6)
buffer_load_dwordx4 a[76:79], v238, s[64:67], s92 offen offset:2048 sc1
v_mfma_f32_16x16x32_bf16 v[144:147], a[128:131], v[16:19], v[144:147]
ds_read_b64_tr_b16 a[156:157], v87 offset:16640
v_add_u32 v86, 0xfffe8000, v86
v_mfma_f32_16x16x32_bf16 v[148:151], a[128:131], v[20:23], v[148:151]
ds_read_b64_tr_b16 a[158:159], v87 offset:24832
v_exp_f32 v34, v34
v_mfma_f32_16x16x32_bf16 v[152:155], a[132:135], v[16:19], v[152:155]
ds_read_b64_tr_b16 a[128:129], v88 offset:16640
v_exp_f32 v35, v35
v_mfma_f32_16x16x32_bf16 v[156:159], a[132:135], v[20:23], v[156:159]
ds_read_b64_tr_b16 a[130:131], v88 offset:24832
v_exp_f32 v38, v38
v_mfma_f32_16x16x32_bf16 v[160:163], a[136:139], v[16:19], v[160:163]
ds_read_b64_tr_b16 a[132:133], v89 offset:16640
ds_read_b64_tr_b16 a[134:135], v89 offset:24832
v_mfma_f32_16x16x32_bf16 v[164:167], a[136:139], v[20:23], v[164:167]
buffer_load_dwordx2 a[108:109], v238, s[64:67], s93 offen offset:0 sc1
v_mfma_f32_16x16x32_bf16 v[168:171], a[140:143], v[16:19], v[168:171]
s_barrier
ds_read_b128 a[136:139], v72 offset:0
v_add_u32 v238, 28672, v238
v_mfma_f32_16x16x32_bf16 v[172:175], a[140:143], v[20:23], v[172:175]
s_waitcnt lgkmcnt(5)
v_exp_f32 v39, v39
v_mfma_f32_16x16x32_bf16 v[176:179], a[144:147], v[16:19], v[176:179]
ds_read_b128 a[140:143], v73 offset:0
v_exp_f32 v40, v40
v_mfma_f32_16x16x32_bf16 v[180:183], a[144:147], v[20:23], v[180:183]
s_add_u32 s71, s75, 131072
v_exp_f32 v41, v41
v_mfma_f32_16x16x32_bf16 v[184:187], a[148:151], v[16:19], v[184:187]
s_add_u32 m0, s77, 98304
ds_read_b128 a[144:147], v74 offset:0
v_add_u32 v87, 0xfffe8000, v87
v_mfma_f32_16x16x32_bf16 v[188:191], a[148:151], v[20:23], v[188:191]
buffer_load_dwordx4 v244, s[56:59], s71 offen offset:0 lds
v_mfma_f32_16x16x32_bf16 v[192:195], a[152:155], v[16:19], v[192:195]
ds_read_b128 a[148:151], v75 offset:0
v_cvt_pk_bf16_f32 v32, v32, v33
v_mfma_f32_16x16x32_bf16 v[196:199], a[152:155], v[20:23], v[196:199]
v_exp_f32 v44, v44
v_mfma_f32_16x16x32_bf16 v[200:203], a[156:159], v[16:19], v[200:203]
ds_read_b128 a[152:155], v72 offset:256
v_cvt_pk_bf16_f32 v36, v36, v37
v_mfma_f32_16x16x32_bf16 v[204:207], a[156:159], v[20:23], v[204:207]
s_waitcnt lgkmcnt(3)
v_exp_f32 v45, v45
v_mfma_f32_16x16x32_bf16 v[208:211], a[128:131], v[16:19], v[208:211]
ds_read_b128 a[156:159], v73 offset:256
v_cvt_pk_bf16_f32 v33, v34, v35
v_mfma_f32_16x16x32_bf16 v[212:215], a[128:131], v[20:23], v[212:215]
buffer_load_dwordx4 v245, s[56:59], s71 offen offset:1024 lds
v_mfma_f32_16x16x32_bf16 v[216:219], a[132:135], v[16:19], v[216:219]
s_cmp_lg_u32 s84, 0
ds_read_b128 a[128:131], v74 offset:256
v_cvt_pk_bf16_f32 v37, v38, v39
v_mfma_f32_16x16x32_bf16 v[220:223], a[132:135], v[20:23], v[220:223]
s_cbranch_scc1 L_rareB3
L_backB3:
v_exp_f32 v42, v42
v_mfma_f32_16x16x32_bf16 v[0:3], a[136:139], a[0:3], v[64:67]
ds_read_b128 a[132:135], v75 offset:256
v_cvt_pk_bf16_f32 v34, v40, v41
v_mfma_f32_16x16x32_bf16 v[4:7], a[136:139], a[4:7], v[68:71]
v_exp_f32 v43, v43
v_mfma_f32_16x16x32_bf16 v[0:3], a[140:143], a[16:19], v[0:3]
ds_read_b128 a[136:139], v72 offset:8192
v_cvt_pk_bf16_f32 v38, v44, v45
v_mfma_f32_16x16x32_bf16 v[4:7], a[140:143], a[20:23], v[4:7]
s_waitcnt lgkmcnt(3)
buffer_load_dwordx4 v246, s[56:59], s71 offen offset:2048 lds
v_mfma_f32_16x16x32_bf16 v[0:3], a[144:147], a[32:35], v[0:3]
ds_read_b128 a[140:143], v73 offset:8192
v_cvt_pk_bf16_f32 v35, v42, v43
v_mfma_f32_16x16x32_bf16 v[4:7], a[144:147], a[36:39], v[4:7]
v_exp_f32 v46, v46
v_mfma_f32_16x16x32_bf16 v[0:3], a[148:151], a[48:51], v[0:3]
ds_read_b128 a[144:147], v74 offset:8192
v_add_u32 v88, 0xfffe8000, v88
v_mfma_f32_16x16x32_bf16 v[4:7], a[148:151], a[52:55], v[4:7]
v_exp_f32 v47, v47
v_mfma_f32_16x16x32_bf16 v[0:3], a[152:155], a[64:67], v[0:3]
ds_read_b128 a[148:151], v75 offset:8192
v_cvt_pk_bf16_f32 v39, v46, v47
v_mfma_f32_16x16x32_bf16 v[4:7], a[152:155], a[68:71], v[4:7]
buffer_load_dwordx4 v247, s[56:59], s71 offen offset:3072 lds
v_mfma_f32_16x16x32_bf16 v[0:3], a[156:159], a[80:83], v[0:3]
ds_read_b128 a[152:155], v72 offset:8448
v_add_u32 v89, 0xfffe8000, v89
v_mfma_f32_16x16x32_bf16 v[4:7], a[156:159], a[84:87], v[4:7]
s_waitcnt lgkmcnt(3)
v_exp_f32 v48, v48
v_mfma_f32_16x16x32_bf16 v[0:3], a[128:131], a[96:99], v[0:3]
ds_read_b128 a[156:159], v73 offset:8448
v_mfma_f32_16x16x32_bf16 v[4:7], a[128:131], a[100:103], v[4:7]
s_add_u32 s71, s75, 135168
v_exp_f32 v49, v49
v_mfma_f32_16x16x32_bf16 v[0:3], a[132:135], a[112:115], v[0:3]
s_add_u32 m0, s77, 102400
ds_read_b128 a[128:131], v74 offset:8448
v_cvt_pk_bf16_f32 v48, v48, v49
v_mfma_f32_16x16x32_bf16 v[4:7], a[132:135], a[116:119], v[4:7]
buffer_load_dwordx4 v244, s[56:59], s71 offen offset:0 lds
v_mfma_f32_16x16x32_bf16 v[8:11], a[136:139], a[0:3], v[64:67]
ds_read_b128 a[132:135], v75 offset:8448
v_mfma_f32_16x16x32_bf16 v[12:15], a[136:139], a[4:7], v[68:71]
v_exp_f32 v52, v52
v_mfma_f32_16x16x32_bf16 v[8:11], a[140:143], a[16:19], v[8:11]
ds_read_b128 a[136:139], v72 offset:16384
v_mfma_f32_16x16x32_bf16 v[12:15], a[140:143], a[20:23], v[12:15]
s_waitcnt lgkmcnt(3)
v_max3_f32 v248, v0, v1, v2
v_max_f32 v248, v248, v3
v_mfma_f32_16x16x32_bf16 v[8:11], a[144:147], a[32:35], v[8:11]
ds_read_b128 a[140:143], v73 offset:16384
v_max3_f32 v249, v4, v5, v6
v_mfma_f32_16x16x32_bf16 v[12:15], a[144:147], a[36:39], v[12:15]
buffer_load_dwordx4 v245, s[56:59], s71 offen offset:1024 lds
v_mfma_f32_16x16x32_bf16 v[8:11], a[148:151], a[48:51], v[8:11]
ds_read_b128 a[144:147], v74 offset:16384
v_max_f32 v249, v249, v7
v_mfma_f32_16x16x32_bf16 v[12:15], a[148:151], a[52:55], v[12:15]
v_exp_f32 v53, v53
v_mfma_f32_16x16x32_bf16 v[8:11], a[152:155], a[64:67], v[8:11]
ds_read_b128 a[148:151], v75 offset:16384
v_cvt_pk_bf16_f32 v52, v52, v53
v_mfma_f32_16x16x32_bf16 v[12:15], a[152:155], a[68:71], v[12:15]
v_exp_f32 v50, v50
v_mfma_f32_16x16x32_bf16 v[8:11], a[156:159], a[80:83], v[8:11]
ds_read_b128 a[152:155], v72 offset:16640
v_mfma_f32_16x16x32_bf16 v[12:15], a[156:159], a[84:87], v[12:15]
s_waitcnt lgkmcnt(3)
buffer_load_dwordx4 v246, s[56:59], s71 offen offset:2048 lds
v_mfma_f32_16x16x32_bf16 v[8:11], a[128:131], a[96:99], v[8:11]
ds_read_b128 a[156:159], v73 offset:16640
v_mfma_f32_16x16x32_bf16 v[12:15], a[128:131], a[100:103], v[12:15]
v_exp_f32 v51, v51
v_mfma_f32_16x16x32_bf16 v[8:11], a[132:135], a[112:115], v[8:11]
ds_read_b128 a[128:131], v74 offset:16640
v_cvt_pk_bf16_f32 v49, v50, v51
v_mfma_f32_16x16x32_bf16 v[12:15], a[132:135], a[116:119], v[12:15]
v_exp_f32 v54, v54
v_mfma_f32_16x16x32_bf16 v[16:19], a[136:139], a[0:3], v[64:67]
ds_read_b128 a[132:135], v75 offset:16640
v_mfma_f32_16x16x32_bf16 v[20:23], a[136:139], a[4:7], v[68:71]
buffer_load_dwordx4 v247, s[56:59], s71 offen offset:3072 lds
v_mfma_f32_16x16x32_bf16 v[16:19], a[140:143], a[16:19], v[16:19]
ds_read_b128 a[136:139], v72 offset:24576
v_mfma_f32_16x16x32_bf16 v[20:23], a[140:143], a[20:23], v[20:23]
s_waitcnt lgkmcnt(3)
v_max3_f32 v248, v248, v8, v9
v_max3_f32 v248, v248, v10, v11
v_mfma_f32_16x16x32_bf16 v[16:19], a[144:147], a[32:35], v[16:19]
ds_read_b128 a[140:143], v73 offset:24576
v_max3_f32 v249, v249, v12, v13
v_mfma_f32_16x16x32_bf16 v[20:23], a[144:147], a[36:39], v[20:23]
v_max3_f32 v249, v249, v14, v15
v_mfma_f32_16x16x32_bf16 v[16:19], a[148:151], a[48:51], v[16:19]
ds_read_b128 a[144:147], v74 offset:24576
v_mfma_f32_16x16x32_bf16 v[20:23], a[148:151], a[52:55], v[20:23]
v_exp_f32 v55, v55
v_mfma_f32_16x16x32_bf16 v[16:19], a[152:155], a[64:67], v[16:19]
ds_read_b128 a[148:151], v75 offset:24576
v_cvt_pk_bf16_f32 v53, v54, v55
v_mfma_f32_16x16x32_bf16 v[20:23], a[152:155], a[68:71], v[20:23]
v_exp_f32 v56, v56
v_mfma_f32_16x16x32_bf16 v[16:19], a[156:159], a[80:83], v[16:19]
ds_read_b128 a[152:155], v72 offset:24832
v_add_u32 v72, 0x10000, v72
v_mfma_f32_16x16x32_bf16 v[20:23], a[156:159], a[84:87], v[20:23]
s_waitcnt lgkmcnt(3)
v_exp_f32 v57, v57
v_mfma_f32_16x16x32_bf16 v[16:19], a[128:131], a[96:99], v[16:19]
ds_read_b128 a[156:159], v73 offset:24832
v_cvt_pk_bf16_f32 v50, v56, v57
v_mfma_f32_16x16x32_bf16 v[20:23], a[128:131], a[100:103], v[20:23]
v_exp_f32 v60, v60
v_mfma_f32_16x16x32_bf16 v[16:19], a[132:135], a[112:115], v[16:19]
ds_read_b128 a[128:131], v74 offset:24832
v_add_u32 v73, 0x10000, v73
v_mfma_f32_16x16x32_bf16 v[20:23], a[132:135], a[116:119], v[20:23]
v_exp_f32 v61, v61
v_mfma_f32_16x16x32_bf16 v[24:27], a[136:139], a[0:3], v[64:67]
ds_read_b128 a[132:135], v75 offset:24832
v_cvt_pk_bf16_f32 v54, v60, v61
v_mfma_f32_16x16x32_bf16 v[28:31], a[136:139], a[4:7], v[68:71]
s_waitcnt vmcnt(28)
v_exp_f32 v58, v58
v_mfma_f32_16x16x32_bf16 v[24:27], a[140:143], a[16:19], v[24:27]
s_barrier
v_exp_f32 v59, v59
v_mfma_f32_16x16x32_bf16 v[28:31], a[140:143], a[20:23], v[28:31]
s_waitcnt lgkmcnt(2)
buffer_load_dwordx4 a[88:91], v238, s[64:67], s92 offen offset:0 sc1
v_mfma_f32_16x16x32_bf16 v[24:27], a[144:147], a[32:35], v[24:27]
ds_read_b64_tr_b16 a[140:141], v82 offset:0
v_max3_f32 v248, v248, v16, v17
v_mfma_f32_16x16x32_bf16 v[28:31], a[144:147], a[36:39], v[28:31]
s_add_u32 s71, s75, 196608
ds_read_b64_tr_b16 a[142:143], v82 offset:8192
v_max3_f32 v248, v248, v18, v19
v_mfma_f32_16x16x32_bf16 v[24:27], a[148:151], a[48:51], v[24:27]
s_add_u32 m0, s77, 131072
ds_read_b64_tr_b16 a[144:145], v83 offset:0
ds_read_b64_tr_b16 a[146:147], v83 offset:8192
v_mfma_f32_16x16x32_bf16 v[28:31], a[148:151], a[52:55], v[28:31]
buffer_load_dwordx4 v224, s[52:55], s71 offen offset:0 lds
v_mfma_f32_16x16x32_bf16 v[24:27], a[152:155], a[64:67], v[24:27]
ds_read_b64_tr_b16 a[148:149], v84 offset:0
ds_read_b64_tr_b16 a[150:151], v84 offset:8192
v_mfma_f32_16x16x32_bf16 v[28:31], a[152:155], a[68:71], v[28:31]
buffer_load_dwordx4 a[92:95], v238, s[64:67], s92 offen offset:1024 sc1
v_mfma_f32_16x16x32_bf16 v[24:27], a[156:159], a[80:83], v[24:27]
ds_read_b64_tr_b16 a[152:153], v85 offset:0
v_max3_f32 v249, v249, v20, v21
v_mfma_f32_16x16x32_bf16 v[28:31], a[156:159], a[84:87], v[28:31]
s_waitcnt lgkmcnt(5)
ds_read_b64_tr_b16 a[154:155], v85 offset:8192
v_max3_f32 v249, v249, v22, v23
v_mfma_f32_16x16x32_bf16 v[24:27], a[128:131], a[96:99], v[24:27]
ds_read_b64_tr_b16 a[156:157], v86 offset:0
ds_read_b64_tr_b16 a[158:159], v86 offset:8192
v_mfma_f32_16x16x32_bf16 v[28:31], a[128:131], a[100:103], v[28:31]
buffer_load_dwordx4 v225, s[52:55], s71 offen offset:1024 lds
v_mfma_f32_16x16x32_bf16 v[24:27], a[132:135], a[112:115], v[24:27]
ds_read_b64_tr_b16 a[128:129], v87 offset:0
ds_read_b64_tr_b16 a[130:131], v87 offset:8192
v_mfma_f32_16x16x32_bf16 v[28:31], a[132:135], a[116:119], v[28:31]
buffer_load_dwordx4 a[104:107], v238, s[64:67], s92 offen offset:2048 sc1
v_mfma_f32_16x16x32_bf16 a[192:195], a[200:203], v[32:35], a[192:195]
ds_read_b64_tr_b16 a[132:133], v88 offset:0
v_cvt_pk_bf16_f32 v51, v58, v59
v_mfma_f32_16x16x32_bf16 a[196:199], a[200:203], v[36:39], a[196:199]
ds_read_b64_tr_b16 a[134:135], v88 offset:8192
v_add_u32 v74, 0x10000, v74
v_mfma_f32_16x16x32_bf16 v[96:99], a[140:143], v[32:35], v[96:99]
ds_read_b64_tr_b16 a[136:137], v89 offset:0
ds_read_b64_tr_b16 a[138:139], v89 offset:8192
v_mfma_f32_16x16x32_bf16 v[100:103], a[140:143], v[36:39], v[100:103]
s_waitcnt lgkmcnt(6)
buffer_load_dwordx4 v226, s[52:55], s71 offen offset:2048 lds
v_mfma_f32_16x16x32_bf16 v[104:107], a[144:147], v[32:35], v[104:107]
ds_read_b64_tr_b16 a[140:141], v82 offset:256
ds_read_b64_tr_b16 a[142:143], v82 offset:8448
v_mfma_f32_16x16x32_bf16 v[108:111], a[144:147], v[36:39], v[108:111]
buffer_load_dwordx2 a[110:111], v238, s[64:67], s93 offen offset:0 sc1
v_mfma_f32_16x16x32_bf16 v[112:115], a[148:151], v[32:35], v[112:115]
ds_read_b64_tr_b16 a[144:145], v83 offset:256
v_add_u32 v238, 28672, v238
v_mfma_f32_16x16x32_bf16 v[116:119], a[148:151], v[36:39], v[116:119]
ds_read_b64_tr_b16 a[146:147], v83 offset:8448
v_max3_f32 v248, v248, v24, v25
v_mfma_f32_16x16x32_bf16 v[120:123], a[152:155], v[32:35], v[120:123]
ds_read_b64_tr_b16 a[148:149], v84 offset:256
ds_read_b64_tr_b16 a[150:151], v84 offset:8448
v_mfma_f32_16x16x32_bf16 v[124:127], a[152:155], v[36:39], v[124:127]
buffer_load_dwordx4 v227, s[52:55], s71 offen offset:3072 lds
v_mfma_f32_16x16x32_bf16 v[128:131], a[156:159], v[32:35], v[128:131]
ds_read_b64_tr_b16 a[152:153], v85 offset:256
ds_read_b64_tr_b16 a[154:155], v85 offset:8448
v_mfma_f32_16x16x32_bf16 v[132:135], a[156:159], v[36:39], v[132:135]
s_waitcnt lgkmcnt(6)
buffer_load_dwordx4 a[120:123], v238, s[64:67], s92 offen offset:0 sc1
v_max3_f32 v248, v248, v26, v27
v_max3_f32 v249, v249, v28, v29
v_max3_f32 v249, v249, v30, v31
v_max_f32 v250, v248, v249
v_mfma_f32_16x16x32_bf16 v[136:139], a[128:131], v[32:35], v[136:139]
s_add_u32 s71, s75, 200704
ds_read_b64_tr_b16 a[156:157], v86 offset:256
v_cmp_lt_f32 vcc, s85, v250
v_mfma_f32_16x16x32_bf16 v[140:143], a[128:131], v[36:39], v[140:143]
s_add_u32 m0, s77, 135168
ds_read_b64_tr_b16 a[158:159], v86 offset:8448
v_add_u32 v75, 0x10000, v75
v_mfma_f32_16x16x32_bf16 v[144:147], a[132:135], v[32:35], v[144:147]
s_cbranch_vccnz L_rareA4
L_backA4:
ds_read_b64_tr_b16 a[128:129], v87 offset:256
ds_read_b64_tr_b16 a[130:131], v87 offset:8448
v_mfma_f32_16x16x32_bf16 v[148:151], a[132:135], v[36:39], v[148:151]
buffer_load_dwordx4 v228, s[52:55], s71 offen offset:0 lds
v_mfma_f32_16x16x32_bf16 v[152:155], a[136:139], v[32:35], v[152:155]
ds_read_b64_tr_b16 a[132:133], v88 offset:256
ds_read_b64_tr_b16 a[134:135], v88 offset:8448
v_mfma_f32_16x16x32_bf16 v[156:159], a[136:139], v[36:39], v[156:159]
buffer_load_dwordx4 a[124:127], v238, s[64:67], s92 offen offset:1024 sc1
v_mfma_f32_16x16x32_bf16 v[160:163], a[140:143], v[32:35], v[160:163]
ds_read_b64_tr_b16 a[136:137], v89 offset:256
v_mfma_f32_16x16x32_bf16 v[164:167], a[140:143], v[36:39], v[164:167]
s_waitcnt lgkmcnt(5)
ds_read_b64_tr_b16 a[138:139], v89 offset:8448
v_mfma_f32_16x16x32_bf16 v[168:171], a[144:147], v[32:35], v[168:171]
v_exp_f32 v62, v62
v_mfma_f32_16x16x32_bf16 v[172:175], a[144:147], v[36:39], v[172:175]
buffer_load_dwordx4 v229, s[52:55], s71 offen offset:1024 lds
v_mfma_f32_16x16x32_bf16 v[176:179], a[148:151], v[32:35], v[176:179]
ds_read_b64_tr_b16 a[144:145], v82 offset:16384
ds_read_b64_tr_b16 a[146:147], v82 offset:24576
v_mfma_f32_16x16x32_bf16 v[180:183], a[148:151], v[36:39], v[180:183]
buffer_load_dwordx4 a[208:211], v238, s[64:67], s92 offen offset:2048 sc1
v_mfma_f32_16x16x32_bf16 v[184:187], a[152:155], v[32:35], v[184:187]
ds_read_b64_tr_b16 a[148:149], v83 offset:16384
v_mfma_f32_16x16x32_bf16 v[188:191], a[152:155], v[36:39], v[188:191]
ds_read_b64_tr_b16 a[150:151], v83 offset:24576
v_mfma_f32_16x16x32_bf16 v[192:195], a[156:159], v[32:35], v[192:195]
ds_read_b64_tr_b16 a[152:153], v84 offset:16384
ds_read_b64_tr_b16 a[154:155], v84 offset:24576
v_mfma_f32_16x16x32_bf16 v[196:199], a[156:159], v[36:39], v[196:199]
s_waitcnt lgkmcnt(6)
buffer_load_dwordx4 v230, s[52:55], s71 offen offset:2048 lds
v_mfma_f32_16x16x32_bf16 v[200:203], a[128:131], v[32:35], v[200:203]
ds_read_b64_tr_b16 a[156:157], v85 offset:16384
ds_read_b64_tr_b16 a[158:159], v85 offset:24576
v_mfma_f32_16x16x32_bf16 v[204:207], a[128:131], v[36:39], v[204:207]
buffer_load_dwordx2 a[224:225], v238, s[64:67], s93 offen offset:0 sc1
v_mfma_f32_16x16x32_bf16 v[208:211], a[132:135], v[32:35], v[208:211]
ds_read_b64_tr_b16 a[128:129], v86 offset:16384
v_add_u32 v238, 28672, v238
v_mfma_f32_16x16x32_bf16 v[212:215], a[132:135], v[36:39], v[212:215]
ds_read_b64_tr_b16 a[130:131], v86 offset:24576
v_exp_f32 v63, v63
v_mfma_f32_16x16x32_bf16 v[216:219], a[136:139], v[32:35], v[216:219]
ds_read_b64_tr_b16 a[132:133], v87 offset:16384
ds_read_b64_tr_b16 a[134:135], v87 offset:24576
v_cvt_pk_bf16_f32 v55, v62, v63
v_mfma_f32_16x16x32_bf16 v[220:223], a[136:139], v[36:39], v[220:223]
buffer_load_dwordx4 v231, s[52:55], s71 offen offset:3072 lds
v_mfma_f32_16x16x32_bf16 a[192:195], a[200:203], v[48:51], a[192:195]
ds_read_b64_tr_b16 a[136:137], v88 offset:16384
ds_read_b64_tr_b16 a[138:139], v88 offset:24576
v_mfma_f32_16x16x32_bf16 a[196:199], a[200:203], v[52:55], a[196:199]
s_waitcnt lgkmcnt(6)
buffer_load_dwordx4 a[212:215], v238, s[64:67], s92 offen offset:0 sc1
v_mfma_f32_16x16x32_bf16 v[96:99], a[144:147], v[48:51], v[96:99]
ds_read_b64_tr_b16 a[140:141], v89 offset:16384
v_exp_f32 v0, v0
v_mfma_f32_16x16x32_bf16 v[100:103], a[144:147], v[52:55], v[100:103]
ds_read_b64_tr_b16 a[142:143], v89 offset:24576
v_exp_f32 v1, v1
v_mfma_f32_16x16x32_bf16 v[104:107], a[148:151], v[48:51], v[104:107]
ds_read_b64_tr_b16 a[144:145], v82 offset:16640
v_exp_f32 v4, v4
v_mfma_f32_16x16x32_bf16 v[108:111], a[148:151], v[52:55], v[108:111]
ds_read_b64_tr_b16 a[146:147], v82 offset:24832
v_add_u32 v82, 0x10000, v82
v_mfma_f32_16x16x32_bf16 v[112:115], a[152:155], v[48:51], v[112:115]
ds_read_b64_tr_b16 a[148:149], v83 offset:16640
ds_read_b64_tr_b16 a[150:151], v83 offset:24832
v_mfma_f32_16x16x32_bf16 v[116:119], a[152:155], v[52:55], v[116:119]
buffer_load_dwordx4 a[216:219], v238, s[64:67], s92 offen offset:1024 sc1
v_mfma_f32_16x16x32_bf16 v[120:123], a[156:159], v[48:51], v[120:123]
ds_read_b64_tr_b16 a[152:153], v84 offset:16640
v_add_u32 v83, 0x10000, v83
v_mfma_f32_16x16x32_bf16 v[124:127], a[156:159], v[52:55], v[124:127]
s_waitcnt lgkmcnt(5)
ds_read_b64_tr_b16 a[154:155], v84 offset:24832
v_add_u32 v84, 0x10000, v84
v_mfma_f32_16x16x32_bf16 v[128:131], a[128:131], v[48:51], v[128:131]
ds_read_b64_tr_b16 a[156:157], v85 offset:16640
v_exp_f32 v5, v5
v_mfma_f32_16x16x32_bf16 v[132:135], a[128:131], v[52:55], v[132:135]
ds_read_b64_tr_b16 a[158:159], v85 offset:24832
v_add_u32 v85, 0x10000, v85
v_mfma_f32_16x16x32_bf16 v[136:139], a[132:135], v[48:51], v[136:139]
ds_read_b64_tr_b16 a[128:129], v86 offset:16640
ds_read_b64_tr_b16 a[130:131], v86 offset:24832
v_mfma_f32_16x16x32_bf16 v[140:143], a[132:135], v[52:55], v[140:143]
buffer_load_dwordx4 a[220:223], v238, s[64:67], s92 offen offset:2048 sc1
v_mfma_f32_16x16x32_bf16 v[144:147], a[136:139], v[48:51], v[144:147]
ds_read_b64_tr_b16 a[132:133], v87 offset:16640
v_add_u32 v86, 0x10000, v86
v_mfma_f32_16x16x32_bf16 v[148:151], a[136:139], v[52:55], v[148:151]
ds_read_b64_tr_b16 a[134:135], v87 offset:24832
v_exp_f32 v2, v2
v_mfma_f32_16x16x32_bf16 v[152:155], a[140:143], v[48:51], v[152:155]
ds_read_b64_tr_b16 a[136:137], v88 offset:16640
v_exp_f32 v3, v3
v_mfma_f32_16x16x32_bf16 v[156:159], a[140:143], v[52:55], v[156:159]
s_waitcnt lgkmcnt(5)
ds_read_b64_tr_b16 a[138:139], v88 offset:24832
v_exp_f32 v6, v6
v_mfma_f32_16x16x32_bf16 v[160:163], a[144:147], v[48:51], v[160:163]
ds_read_b64_tr_b16 a[140:141], v89 offset:16640
ds_read_b64_tr_b16 a[142:143], v89 offset:24832
v_mfma_f32_16x16x32_bf16 v[164:167], a[144:147], v[52:55], v[164:167]
s_waitcnt vmcnt(31)
buffer_load_dwordx2 a[226:227], v238, s[64:67], s93 offen offset:0 sc1
v_mfma_f32_16x16x32_bf16 v[168:171], a[148:151], v[48:51], v[168:171]
s_barrier
ds_read_b128 a[144:147], v72 offset:0
v_add_u32 v238, 28672, v238
v_mfma_f32_16x16x32_bf16 v[172:175], a[148:151], v[52:55], v[172:175]
v_exp_f32 v7, v7
v_mfma_f32_16x16x32_bf16 v[176:179], a[152:155], v[48:51], v[176:179]
ds_read_b128 a[148:151], v73 offset:0
v_exp_f32 v8, v8
v_mfma_f32_16x16x32_bf16 v[180:183], a[152:155], v[52:55], v[180:183]
s_add_u32 s71, s75, 163840
v_exp_f32 v9, v9
v_mfma_f32_16x16x32_bf16 v[184:187], a[156:159], v[48:51], v[184:187]
s_add_u32 m0, s77, 0
ds_read_b128 a[152:155], v74 offset:0
v_add_u32 v87, 0x10000, v87
v_mfma_f32_16x16x32_bf16 v[188:191], a[156:159], v[52:55], v[188:191]
s_waitcnt lgkmcnt(3)
buffer_load_dwordx4 v244, s[56:59], s71 offen offset:0 lds
v_mfma_f32_16x16x32_bf16 v[192:195], a[128:131], v[48:51], v[192:195]
ds_read_b128 a[156:159], v75 offset:0
v_cvt_pk_bf16_f32 v0, v0, v1
v_mfma_f32_16x16x32_bf16 v[196:199], a[128:131], v[52:55], v[196:199]
v_exp_f32 v12, v12
v_mfma_f32_16x16x32_bf16 v[200:203], a[132:135], v[48:51], v[200:203]
ds_read_b128 a[128:131], v72 offset:256
v_cvt_pk_bf16_f32 v4, v4, v5
v_mfma_f32_16x16x32_bf16 v[204:207], a[132:135], v[52:55], v[204:207]
v_exp_f32 v13, v13
v_mfma_f32_16x16x32_bf16 v[208:211], a[136:139], v[48:51], v[208:211]
ds_read_b128 a[132:135], v73 offset:256
v_cvt_pk_bf16_f32 v1, v2, v3
v_mfma_f32_16x16x32_bf16 v[212:215], a[136:139], v[52:55], v[212:215]
buffer_load_dwordx4 v245, s[56:59], s71 offen offset:1024 lds
v_mfma_f32_16x16x32_bf16 v[216:219], a[140:143], v[48:51], v[216:219]
s_cmp_lg_u32 s84, 0
ds_read_b128 a[136:139], v74 offset:256
v_cvt_pk_bf16_f32 v5, v6, v7
v_mfma_f32_16x16x32_bf16 v[220:223], a[140:143], v[52:55], v[220:223]
s_cbranch_scc1 L_rareB4
L_backB4:
s_waitcnt lgkmcnt(3)
v_cvt_pk_bf16_f32 v2, v8, v9
v_mfma_f32_16x16x32_bf16 v[32:35], a[144:147], a[0:3], v[64:67]
ds_read_b128 a[140:143], v75 offset:256
v_cvt_pk_bf16_f32 v6, v12, v13
v_mfma_f32_16x16x32_bf16 v[36:39], a[144:147], a[4:7], v[68:71]
v_exp_f32 v10, v10
v_mfma_f32_16x16x32_bf16 v[32:35], a[148:151], a[16:19], v[32:35]
ds_read_b128 a[144:147], v72 offset:8192
v_add_u32 v88, 0x10000, v88
v_mfma_f32_16x16x32_bf16 v[36:39], a[148:151], a[20:23], v[36:39]
buffer_load_dwordx4 v246, s[56:59], s71 offen offset:2048 lds
v_mfma_f32_16x16x32_bf16 v[32:35], a[152:155], a[32:35], v[32:35]
ds_read_b128 a[148:151], v73 offset:8192
v_add_u32 v89, 0x10000, v89
v_mfma_f32_16x16x32_bf16 v[36:39], a[152:155], a[36:39], v[36:39]
v_exp_f32 v11, v11
v_mfma_f32_16x16x32_bf16 v[32:35], a[156:159], a[48:51], v[32:35]
ds_read_b128 a[152:155], v74 offset:8192
v_cvt_pk_bf16_f32 v3, v10, v11
v_mfma_f32_16x16x32_bf16 v[36:39], a[156:159], a[52:55], v[36:39]
s_waitcnt lgkmcnt(3)
v_exp_f32 v14, v14
v_mfma_f32_16x16x32_bf16 v[32:35], a[128:131], a[64:67], v[32:35]
ds_read_b128 a[156:159], v75 offset:8192
v_mfma_f32_16x16x32_bf16 v[36:39], a[128:131], a[68:71], v[36:39]
buffer_load_dwordx4 v247, s[56:59], s71 offen offset:3072 lds
v_mfma_f32_16x16x32_bf16 v[32:35], a[132:135], a[80:83], v[32:35]
ds_read_b128 a[128:131], v72 offset:8448
v_mfma_f32_16x16x32_bf16 v[36:39], a[132:135], a[84:87], v[36:39]
v_exp_f32 v15, v15
v_mfma_f32_16x16x32_bf16 v[32:35], a[136:139], a[96:99], v[32:35]
ds_read_b128 a[132:135], v73 offset:8448
v_cvt_pk_bf16_f32 v7, v14, v15
v_mfma_f32_16x16x32_bf16 v[36:39], a[136:139], a[100:103], v[36:39]
s_add_u32 s71, s75, 167936
v_exp_f32 v16, v16
v_mfma_f32_16x16x32_bf16 v[32:35], a[140:143], a[112:115], v[32:35]
s_add_u32 m0, s77, 4096
ds_read_b128 a[136:139], v74 offset:8448
v_mfma_f32_16x16x32_bf16 v[36:39], a[140:143], a[116:119], v[36:39]
s_waitcnt lgkmcnt(3)
buffer_load_dwordx4 v244, s[56:59], s71 offen offset:0 lds
v_mfma_f32_16x16x32_bf16 v[40:43], a[144:147], a[0:3], v[64:67]
ds_read_b128 a[140:143], v75 offset:8448
v_mfma_f32_16x16x32_bf16 v[44:47], a[144:147], a[4:7], v[68:71]
v_exp_f32 v17, v17
v_mfma_f32_16x16x32_bf16 v[40:43], a[148:151], a[16:19], v[40:43]
ds_read_b128 a[144:147], v72 offset:16384
v_cvt_pk_bf16_f32 v16, v16, v17
v_mfma_f32_16x16x32_bf16 v[44:47], a[148:151], a[20:23], v[44:47]
v_max3_f32 v248, v32, v33, v34
v_max_f32 v248, v248, v35
v_mfma_f32_16x16x32_bf16 v[40:43], a[152:155], a[32:35], v[40:43]
ds_read_b128 a[148:151], v73 offset:16384
v_max3_f32 v249, v36, v37, v38
v_mfma_f32_16x16x32_bf16 v[44:47], a[152:155], a[36:39], v[44:47]
buffer_load_dwordx4 v245, s[56:59], s71 offen offset:1024 lds
v_mfma_f32_16x16x32_bf16 v[40:43], a[156:159], a[48:51], v[40:43]
ds_read_b128 a[152:155], v74 offset:16384
v_max_f32 v249, v249, v39
v_mfma_f32_16x16x32_bf16 v[44:47], a[156:159], a[52:55], v[44:47]
s_waitcnt lgkmcnt(3)
v_exp_f32 v20, v20
v_mfma_f32_16x16x32_bf16 v[40:43], a[128:131], a[64:67], v[40:43]
ds_read_b128 a[156:159], v75 offset:16384
v_mfma_f32_16x16x32_bf16 v[44:47], a[128:131], a[68:71], v[44:47]
v_exp_f32 v21, v21
v_mfma_f32_16x16x32_bf16 v[40:43], a[132:135], a[80:83], v[40:43]
ds_read_b128 a[128:131], v72 offset:16640
v_cvt_pk_bf16_f32 v20, v20, v21
v_mfma_f32_16x16x32_bf16 v[44:47], a[132:135], a[84:87], v[44:47]
buffer_load_dwordx4 v246, s[56:59], s71 offen offset:2048 lds
v_mfma_f32_16x16x32_bf16 v[40:43], a[136:139], a[96:99], v[40:43]
ds_read_b128 a[132:135], v73 offset:16640
v_mfma_f32_16x16x32_bf16 v[44:47], a[136:139], a[100:103], v[44:47]
v_exp_f32 v18, v18
v_mfma_f32_16x16x32_bf16 v[40:43], a[140:143], a[112:115], v[40:43]
ds_read_b128 a[136:139], v74 offset:16640
v_mfma_f32_16x16x32_bf16 v[44:47], a[140:143], a[116:119], v[44:47]
s_waitcnt lgkmcnt(3)
v_exp_f32 v19, v19
v_mfma_f32_16x16x32_bf16 v[48:51], a[144:147], a[0:3], v[64:67]
ds_read_b128 a[140:143], v75 offset:16640
v_cvt_pk_bf16_f32 v17, v18, v19
v_mfma_f32_16x16x32_bf16 v[52:55], a[144:147], a[4:7], v[68:71]
buffer_load_dwordx4 v247, s[56:59], s71 offen offset:3072 lds
v_mfma_f32_16x16x32_bf16 v[48:51], a[148:151], a[16:19], v[48:51]
ds_read_b128 a[144:147], v72 offset:24576
v_mfma_f32_16x16x32_bf16 v[52:55], a[148:151], a[20:23], v[52:55]
v_max3_f32 v248, v248, v40, v41
v_max3_f32 v248, v248, v42, v43
v_mfma_f32_16x16x32_bf16 v[48:51], a[152:155], a[32:35], v[48:51]
ds_read_b128 a[148:151], v73 offset:24576
v_max3_f32 v249, v249, v44, v45
v_mfma_f32_16x16x32_bf16 v[52:55], a[152:155], a[36:39], v[52:55]
v_max3_f32 v249, v249, v46, v47
v_mfma_f32_16x16x32_bf16 v[48:51], a[156:159], a[48:51], v[48:51]
ds_read_b128 a[152:155], v74 offset:24576
v_mfma_f32_16x16x32_bf16 v[52:55], a[156:159], a[52:55], v[52:55]
s_waitcnt lgkmcnt(3)
v_exp_f32 v22, v22
v_mfma_f32_16x16x32_bf16 v[48:51], a[128:131], a[64:67], v[48:51]
ds_read_b128 a[156:159], v75 offset:24576
v_mfma_f32_16x16x32_bf16 v[52:55], a[128:131], a[68:71], v[52:55]
v_exp_f32 v23, v23
v_mfma_f32_16x16x32_bf16 v[48:51], a[132:135], a[80:83], v[48:51]
ds_read_b128 a[128:131], v72 offset:24832
v_cvt_pk_bf16_f32 v21, v22, v23
v_mfma_f32_16x16x32_bf16 v[52:55], a[132:135], a[84:87], v[52:55]
v_exp_f32 v24, v24
v_mfma_f32_16x16x32_bf16 v[48:51], a[136:139], a[96:99], v[48:51]
ds_read_b128 a[132:135], v73 offset:24832
v_add_u32 v72, 0x10000, v72
v_mfma_f32_16x16x32_bf16 v[52:55], a[136:139], a[100:103], v[52:55]
v_exp_f32 v25, v25
v_mfma_f32_16x16x32_bf16 v[48:51], a[140:143], a[112:115], v[48:51]
ds_read_b128 a[136:139], v74 offset:24832
v_cvt_pk_bf16_f32 v18, v24, v25
v_mfma_f32_16x16x32_bf16 v[52:55], a[140:143], a[116:119], v[52:55]
s_waitcnt lgkmcnt(3)
v_exp_f32 v28, v28
v_mfma_f32_16x16x32_bf16 v[56:59], a[144:147], a[0:3], v[64:67]
ds_read_b128 a[140:143], v75 offset:24832
v_add_u32 v73, 0x10000, v73
v_mfma_f32_16x16x32_bf16 v[60:63], a[144:147], a[4:7], v[68:71]
s_waitcnt vmcnt(28)
v_exp_f32 v29, v29
v_mfma_f32_16x16x32_bf16 v[56:59], a[148:151], a[16:19], v[56:59]
s_barrier
v_exp_f32 v26, v26
v_mfma_f32_16x16x32_bf16 v[60:63], a[148:151], a[20:23], v[60:63]
buffer_load_dwordx4 a[228:231], v238, s[64:67], s92 offen offset:0 sc1
v_mfma_f32_16x16x32_bf16 v[56:59], a[152:155], a[32:35], v[56:59]
ds_read_b64_tr_b16 a[148:149], v82 offset:0
v_max3_f32 v248, v248, v48, v49
v_mfma_f32_16x16x32_bf16 v[60:63], a[152:155], a[36:39], v[60:63]
s_add_u32 s71, s75, 229376
ds_read_b64_tr_b16 a[150:151], v82 offset:8192
v_max3_f32 v248, v248, v50, v51
v_mfma_f32_16x16x32_bf16 v[56:59], a[156:159], a[48:51], v[56:59]
s_add_u32 m0, s77, 32768
ds_read_b64_tr_b16 a[152:153], v83 offset:0
ds_read_b64_tr_b16 a[154:155], v83 offset:8192
v_mfma_f32_16x16x32_bf16 v[60:63], a[156:159], a[52:55], v[60:63]
s_waitcnt lgkmcnt(4)
buffer_load_dwordx4 v224, s[52:55], s71 offen offset:0 lds
v_mfma_f32_16x16x32_bf16 v[56:59], a[128:131], a[64:67], v[56:59]
ds_read_b64_tr_b16 a[156:157], v84 offset:0
ds_read_b64_tr_b16 a[158:159], v84 offset:8192
v_mfma_f32_16x16x32_bf16 v[60:63], a[128:131], a[68:71], v[60:63]
buffer_load_dwordx4 a[232:235], v238, s[64:67], s92 offen offset:1024 sc1
v_mfma_f32_16x16x32_bf16 v[56:59], a[132:135], a[80:83], v[56:59]
ds_read_b64_tr_b16 a[128:129], v85 offset:0
v_max3_f32 v249, v249, v52, v53
v_mfma_f32_16x16x32_bf16 v[60:63], a[132:135], a[84:87], v[60:63]
ds_read_b64_tr_b16 a[130:131], v85 offset:8192
v_max3_f32 v249, v249, v54, v55
v_mfma_f32_16x16x32_bf16 v[56:59], a[136:139], a[96:99], v[56:59]
ds_read_b64_tr_b16 a[132:133], v86 offset:0
ds_read_b64_tr_b16 a[134:135], v86 offset:8192
v_mfma_f32_16x16x32_bf16 v[60:63], a[136:139], a[100:103], v[60:63]
buffer_load_dwordx4 v225, s[52:55], s71 offen offset:1024 lds
v_mfma_f32_16x16x32_bf16 v[56:59], a[140:143], a[112:115], v[56:59]
ds_read_b64_tr_b16 a[136:137], v87 offset:0
ds_read_b64_tr_b16 a[138:139], v87 offset:8192
v_mfma_f32_16x16x32_bf16 v[60:63], a[140:143], a[116:119], v[60:63]
s_waitcnt lgkmcnt(6)
buffer_load_dwordx4 a[236:239], v238, s[64:67], s92 offen offset:2048 sc1
v_mfma_f32_16x16x32_bf16 a[192:195], a[200:203], v[0:3], a[192:195]
ds_read_b64_tr_b16 a[140:141], v88 offset:0
v_cvt_pk_bf16_f32 v22, v28, v29
v_mfma_f32_16x16x32_bf16 a[196:199], a[200:203], v[4:7], a[196:199]
ds_read_b64_tr_b16 a[142:143], v88 offset:8192
v_add_u32 v74, 0x10000, v74
v_mfma_f32_16x16x32_bf16 v[96:99], a[148:151], v[0:3], v[96:99]
ds_read_b64_tr_b16 a[144:145], v89 offset:0
ds_read_b64_tr_b16 a[146:147], v89 offset:8192
v_mfma_f32_16x16x32_bf16 v[100:103], a[148:151], v[4:7], v[100:103]
buffer_load_dwordx4 v226, s[52:55], s71 offen offset:2048 lds
v_mfma_f32_16x16x32_bf16 v[104:107], a[152:155], v[0:3], v[104:107]
ds_read_b64_tr_b16 a[148:149], v82 offset:256
ds_read_b64_tr_b16 a[150:151], v82 offset:8448
v_mfma_f32_16x16x32_bf16 v[108:111], a[152:155], v[4:7], v[108:111]
buffer_load_dwordx2 a[252:253], v238, s[64:67], s93 offen offset:0 sc1
v_mfma_f32_16x16x32_bf16 v[112:115], a[156:159], v[0:3], v[112:115]
ds_read_b64_tr_b16 a[152:153], v83 offset:256
v_add_u32 v238, 28672, v238
v_mfma_f32_16x16x32_bf16 v[116:119], a[156:159], v[4:7], v[116:119]
s_waitcnt lgkmcnt(5)
ds_read_b64_tr_b16 a[154:155], v83 offset:8448
v_max3_f32 v248, v248, v56, v57
v_mfma_f32_16x16x32_bf16 v[120:123], a[128:131], v[0:3], v[120:123]
ds_read_b64_tr_b16 a[156:157], v84 offset:256
ds_read_b64_tr_b16 a[158:159], v84 offset:8448
v_mfma_f32_16x16x32_bf16 v[124:127], a[128:131], v[4:7], v[124:127]
buffer_load_dwordx4 v227, s[52:55], s71 offen offset:3072 lds
v_mfma_f32_16x16x32_bf16 v[128:131], a[132:135], v[0:3], v[128:131]
ds_read_b64_tr_b16 a[128:129], v85 offset:256
ds_read_b64_tr_b16 a[130:131], v85 offset:8448
v_mfma_f32_16x16x32_bf16 v[132:135], a[132:135], v[4:7], v[132:135]
buffer_load_dwordx4 a[240:243], v238, s[64:67], s92 offen offset:0 sc1
v_max3_f32 v248, v248, v58, v59
v_max3_f32 v249, v249, v60, v61
v_max3_f32 v249, v249, v62, v63
v_max_f32 v250, v248, v249
v_mfma_f32_16x16x32_bf16 v[136:139], a[136:139], v[0:3], v[136:139]
s_add_u32 s71, s75, 233472
ds_read_b64_tr_b16 a[132:133], v86 offset:256
v_cmp_lt_f32 vcc, s85, v250
v_mfma_f32_16x16x32_bf16 v[140:143], a[136:139], v[4:7], v[140:143]
s_add_u32 m0, s77, 36864
ds_read_b64_tr_b16 a[134:135], v86 offset:8448
v_add_u32 v75, 0x10000, v75
v_mfma_f32_16x16x32_bf16 v[144:147], a[140:143], v[0:3], v[144:147]
s_cbranch_vccnz L_rareA5
L_backA5:
ds_read_b64_tr_b16 a[136:137], v87 offset:256
ds_read_b64_tr_b16 a[138:139], v87 offset:8448
v_mfma_f32_16x16x32_bf16 v[148:151], a[140:143], v[4:7], v[148:151]
s_waitcnt lgkmcnt(6)
buffer_load_dwordx4 v228, s[52:55], s71 offen offset:0 lds
v_mfma_f32_16x16x32_bf16 v[152:155], a[144:147], v[0:3], v[152:155]
ds_read_b64_tr_b16 a[140:141], v88 offset:256
ds_read_b64_tr_b16 a[142:143], v88 offset:8448
v_mfma_f32_16x16x32_bf16 v[156:159], a[144:147], v[4:7], v[156:159]
buffer_load_dwordx4 a[244:247], v238, s[64:67], s92 offen offset:1024 sc1
v_mfma_f32_16x16x32_bf16 v[160:163], a[148:151], v[0:3], v[160:163]
ds_read_b64_tr_b16 a[144:145], v89 offset:256
v_mfma_f32_16x16x32_bf16 v[164:167], a[148:151], v[4:7], v[164:167]
ds_read_b64_tr_b16 a[146:147], v89 offset:8448
v_mfma_f32_16x16x32_bf16 v[168:171], a[152:155], v[0:3], v[168:171]
v_exp_f32 v27, v27
v_mfma_f32_16x16x32_bf16 v[172:175], a[152:155], v[4:7], v[172:175]
buffer_load_dwordx4 v229, s[52:55], s71 offen offset:1024 lds
v_mfma_f32_16x16x32_bf16 v[176:179], a[156:159], v[0:3], v[176:179]
ds_read_b64_tr_b16 a[152:153], v82 offset:16384
ds_read_b64_tr_b16 a[154:155], v82 offset:24576
v_mfma_f32_16x16x32_bf16 v[180:183], a[156:159], v[4:7], v[180:183]
s_waitcnt lgkmcnt(4)
buffer_load_dwordx4 a[248:251], v238, s[64:67], s92 offen offset:2048 sc1
v_mfma_f32_16x16x32_bf16 v[184:187], a[128:131], v[0:3], v[184:187]
ds_read_b64_tr_b16 a[156:157], v83 offset:16384
v_cvt_pk_bf16_f32 v19, v26, v27
v_mfma_f32_16x16x32_bf16 v[188:191], a[128:131], v[4:7], v[188:191]
ds_read_b64_tr_b16 a[158:159], v83 offset:24576
v_mfma_f32_16x16x32_bf16 v[192:195], a[132:135], v[0:3], v[192:195]
ds_read_b64_tr_b16 a[128:129], v84 offset:16384
ds_read_b64_tr_b16 a[130:131], v84 offset:24576
v_mfma_f32_16x16x32_bf16 v[196:199], a[132:135], v[4:7], v[196:199]
buffer_load_dwordx4 v230, s[52:55], s71 offen offset:2048 lds
v_mfma_f32_16x16x32_bf16 v[200:203], a[136:139], v[0:3], v[200:203]
ds_read_b64_tr_b16 a[132:133], v85 offset:16384
ds_read_b64_tr_b16 a[134:135], v85 offset:24576
v_mfma_f32_16x16x32_bf16 v[204:207], a[136:139], v[4:7], v[204:207]
buffer_load_dwordx2 a[254:255], v238, s[64:67], s93 offen offset:0 sc1
v_mfma_f32_16x16x32_bf16 v[208:211], a[140:143], v[0:3], v[208:211]
ds_read_b64_tr_b16 a[136:137], v86 offset:16384
v_add_u32 v238, 28672, v238
v_mfma_f32_16x16x32_bf16 v[212:215], a[140:143], v[4:7], v[212:215]
s_waitcnt lgkmcnt(5)
ds_read_b64_tr_b16 a[138:139], v86 offset:24576
v_exp_f32 v30, v30
v_exp_f32 v31, v31
v_mfma_f32_16x16x32_bf16 v[216:219], a[144:147], v[0:3], v[216:219]
ds_read_b64_tr_b16 a[140:141], v87 offset:16384
ds_read_b64_tr_b16 a[142:143], v87 offset:24576
v_cvt_pk_bf16_f32 v23, v30, v31
v_mfma_f32_16x16x32_bf16 v[220:223], a[144:147], v[4:7], v[220:223]
buffer_load_dwordx4 v231, s[52:55], s71 offen offset:3072 lds
v_mfma_f32_16x16x32_bf16 a[192:195], a[200:203], v[16:19], a[192:195]
ds_read_b64_tr_b16 a[144:145], v88 offset:16384
ds_read_b64_tr_b16 a[146:147], v88 offset:24576
v_mfma_f32_16x16x32_bf16 a[196:199], a[200:203], v[20:23], a[196:199]
buffer_load_dwordx4 a[176:179], v238, s[64:67], s92 offen offset:0 sc1
v_mfma_f32_16x16x32_bf16 v[96:99], a[152:155], v[16:19], v[96:99]
ds_read_b64_tr_b16 a[148:149], v89 offset:16384
v_exp_f32 v32, v32
v_mfma_f32_16x16x32_bf16 v[100:103], a[152:155], v[20:23], v[100:103]
ds_read_b64_tr_b16 a[150:151], v89 offset:24576
v_exp_f32 v33, v33
v_mfma_f32_16x16x32_bf16 v[104:107], a[156:159], v[16:19], v[104:107]
ds_read_b64_tr_b16 a[152:153], v82 offset:16640
v_exp_f32 v36, v36
v_mfma_f32_16x16x32_bf16 v[108:111], a[156:159], v[20:23], v[108:111]
s_waitcnt lgkmcnt(5)
ds_read_b64_tr_b16 a[154:155], v82 offset:24832
v_add_u32 v82, 0xfffe8000, v82
v_mfma_f32_16x16x32_bf16 v[112:115], a[128:131], v[16:19], v[112:115]
ds_read_b64_tr_b16 a[156:157], v83 offset:16640
ds_read_b64_tr_b16 a[158:159], v83 offset:24832
v_mfma_f32_16x16x32_bf16 v[116:119], a[128:131], v[20:23], v[116:119]
buffer_load_dwordx4 a[180:183], v238, s[64:67], s92 offen offset:1024 sc1
v_mfma_f32_16x16x32_bf16 v[120:123], a[132:135], v[16:19], v[120:123]
ds_read_b64_tr_b16 a[128:129], v84 offset:16640
v_add_u32 v83, 0xfffe8000, v83
v_mfma_f32_16x16x32_bf16 v[124:127], a[132:135], v[20:23], v[124:127]
ds_read_b64_tr_b16 a[130:131], v84 offset:24832
v_add_u32 v84, 0xfffe8000, v84
v_mfma_f32_16x16x32_bf16 v[128:131], a[136:139], v[16:19], v[128:131]
ds_read_b64_tr_b16 a[132:133], v85 offset:16640
v_exp_f32 v37, v37
v_mfma_f32_16x16x32_bf16 v[132:135], a[136:139], v[20:23], v[132:135]
ds_read_b64_tr_b16 a[134:135], v85 offset:24832
v_add_u32 v85, 0xfffe8000, v85
v_mfma_f32_16x16x32_bf16 v[136:139], a[140:143], v[16:19], v[136:139]
ds_read_b64_tr_b16 a[136:137], v86 offset:16640
ds_read_b64_tr_b16 a[138:139], v86 offset:24832
v_mfma_f32_16x16x32_bf16 v[140:143], a[140:143], v[20:23], v[140:143]
s_waitcnt lgkmcnt(6)
buffer_load_dwordx4 a[160:163], v238, s[64:67], s92 offen offset:2048 sc1
v_mfma_f32_16x16x32_bf16 v[144:147], a[144:147], v[16:19], v[144:147]
ds_read_b64_tr_b16 a[140:141], v87 offset:16640
v_add_u32 v86, 0xfffe8000, v86
v_mfma_f32_16x16x32_bf16 v[148:151], a[144:147], v[20:23], v[148:151]
ds_read_b64_tr_b16 a[142:143], v87 offset:24832
v_exp_f32 v34, v34
v_mfma_f32_16x16x32_bf16 v[152:155], a[148:151], v[16:19], v[152:155]
ds_read_b64_tr_b16 a[144:145], v88 offset:16640
v_exp_f32 v35, v35
v_mfma_f32_16x16x32_bf16 v[156:159], a[148:151], v[20:23], v[156:159]
ds_read_b64_tr_b16 a[146:147], v88 offset:24832
v_exp_f32 v38, v38
v_mfma_f32_16x16x32_bf16 v[160:163], a[152:155], v[16:19], v[160:163]
ds_read_b64_tr_b16 a[148:149], v89 offset:16640
v_exp_f32 v39, v39
v_mfma_f32_16x16x32_bf16 v[164:167], a[152:155], v[20:23], v[164:167]
s_waitcnt vmcnt(31)
ds_read_b64_tr_b16 a[150:151], v89 offset:24832
v_add_u32 v87, 0xfffe8000, v87
v_mfma_f32_16x16x32_bf16 v[168:171], a[156:159], v[16:19], v[168:171]
s_barrier
ds_read_b128 a[152:155], v72 offset:0
v_cvt_pk_bf16_f32 v32, v32, v33
v_mfma_f32_16x16x32_bf16 v[172:175], a[156:159], v[20:23], v[172:175]
s_waitcnt lgkmcnt(5)
v_exp_f32 v40, v40
v_mfma_f32_16x16x32_bf16 v[176:179], a[128:131], v[16:19], v[176:179]
ds_read_b128 a[156:159], v73 offset:0
v_cvt_pk_bf16_f32 v36, v36, v37
v_mfma_f32_16x16x32_bf16 v[180:183], a[128:131], v[20:23], v[180:183]
s_add_u32 s71, s75, 196608
v_exp_f32 v41, v41
v_mfma_f32_16x16x32_bf16 v[184:187], a[132:135], v[16:19], v[184:187]
s_add_u32 m0, s77, 65536
ds_read_b128 a[128:131], v74 offset:0
v_cvt_pk_bf16_f32 v33, v34, v35
v_mfma_f32_16x16x32_bf16 v[188:191], a[132:135], v[20:23], v[188:191]
buffer_load_dwordx4 v244, s[56:59], s71 offen offset:0 lds
v_mfma_f32_16x16x32_bf16 v[192:195], a[136:139], v[16:19], v[192:195]
ds_read_b128 a[132:135], v75 offset:0
v_cvt_pk_bf16_f32 v37, v38, v39
v_mfma_f32_16x16x32_bf16 v[196:199], a[136:139], v[20:23], v[196:199]
v_exp_f32 v44, v44
v_mfma_f32_16x16x32_bf16 v[200:203], a[140:143], v[16:19], v[200:203]
ds_read_b128 a[136:139], v72 offset:256
v_cvt_pk_bf16_f32 v34, v40, v41
v_mfma_f32_16x16x32_bf16 v[204:207], a[140:143], v[20:23], v[204:207]
s_waitcnt lgkmcnt(3)
v_exp_f32 v45, v45
v_mfma_f32_16x16x32_bf16 v[208:211], a[144:147], v[16:19], v[208:211]
ds_read_b128 a[140:143], v73 offset:256
v_cvt_pk_bf16_f32 v38, v44, v45
v_mfma_f32_16x16x32_bf16 v[212:215], a[144:147], v[20:23], v[212:215]
buffer_load_dwordx4 v245, s[56:59], s71 offen offset:1024 lds
v_mfma_f32_16x16x32_bf16 v[216:219], a[148:151], v[16:19], v[216:219]
s_cmp_lg_u32 s84, 0
ds_read_b128 a[144:147], v74 offset:256
v_add_u32 v88, 0xfffe8000, v88
v_mfma_f32_16x16x32_bf16 v[220:223], a[148:151], v[20:23], v[220:223]
s_cbranch_scc1 L_rareB5
L_backB5:
v_exp_f32 v42, v42
v_mfma_f32_16x16x32_bf16 v[0:3], a[152:155], a[0:3], v[64:67]
ds_read_b128 a[148:151], v75 offset:256
v_add_u32 v89, 0xfffe8000, v89
v_mfma_f32_16x16x32_bf16 v[4:7], a[152:155], a[4:7], v[68:71]
v_exp_f32 v43, v43
v_mfma_f32_16x16x32_bf16 v[0:3], a[156:159], a[16:19], v[0:3]
ds_read_b128 a[152:155], v72 offset:8192
v_cvt_pk_bf16_f32 v35, v42, v43
v_mfma_f32_16x16x32_bf16 v[4:7], a[156:159], a[20:23], v[4:7]
s_waitcnt lgkmcnt(3)
buffer_load_dwordx4 v246, s[56:59], s71 offen offset:2048 lds
v_mfma_f32_16x16x32_bf16 v[0:3], a[128:131], a[32:35], v[0:3]
ds_read_b128 a[156:159], v73 offset:8192
v_accvgpr_read_b32 v224, a185
v_mfma_f32_16x16x32_bf16 v[4:7], a[128:131], a[36:39], v[4:7]
v_exp_f32 v46, v46
v_mfma_f32_16x16x32_bf16 v[0:3], a[132:135], a[48:51], v[0:3]
ds_read_b128 a[128:131], v74 offset:8192
v_xor_b32 v225, 32, v224
v_mfma_f32_16x16x32_bf16 v[4:7], a[132:135], a[52:55], v[4:7]
v_exp_f32 v47, v47
v_mfma_f32_16x16x32_bf16 v[0:3], a[136:139], a[64:67], v[0:3]
ds_read_b128 a[132:135], v75 offset:8192
v_cvt_pk_bf16_f32 v39, v46, v47
v_mfma_f32_16x16x32_bf16 v[4:7], a[136:139], a[68:71], v[4:7]
buffer_load_dwordx4 v247, s[56:59], s71 offen offset:3072 lds
v_mfma_f32_16x16x32_bf16 v[0:3], a[140:143], a[80:83], v[0:3]
ds_read_b128 a[136:139], v72 offset:8448
v_xor_b32 v226, 64, v224
v_mfma_f32_16x16x32_bf16 v[4:7], a[140:143], a[84:87], v[4:7]
s_waitcnt lgkmcnt(3)
v_exp_f32 v48, v48
v_mfma_f32_16x16x32_bf16 v[0:3], a[144:147], a[96:99], v[0:3]
ds_read_b128 a[140:143], v73 offset:8448
v_xor_b32 v227, 96, v224
v_mfma_f32_16x16x32_bf16 v[4:7], a[144:147], a[100:103], v[4:7]
s_add_u32 s71, s75, 200704
v_exp_f32 v49, v49
v_mfma_f32_16x16x32_bf16 v[0:3], a[148:151], a[112:115], v[0:3]
s_add_u32 m0, s77, 69632
ds_read_b128 a[144:147], v74 offset:8448
v_cvt_pk_bf16_f32 v48, v48, v49
v_mfma_f32_16x16x32_bf16 v[4:7], a[148:151], a[116:119], v[4:7]
buffer_load_dwordx4 v244, s[56:59], s71 offen offset:0 lds
v_mfma_f32_16x16x32_bf16 v[8:11], a[152:155], a[0:3], v[64:67]
ds_read_b128 a[148:151], v75 offset:8448
v_mfma_f32_16x16x32_bf16 v[12:15], a[152:155], a[4:7], v[68:71]
v_exp_f32 v52, v52
v_mfma_f32_16x16x32_bf16 v[8:11], a[156:159], a[16:19], v[8:11]
ds_read_b128 a[152:155], v72 offset:16384
v_mfma_f32_16x16x32_bf16 v[12:15], a[156:159], a[20:23], v[12:15]
s_waitcnt lgkmcnt(3)
v_max3_f32 v248, v0, v1, v2
v_max_f32 v248, v248, v3
v_mfma_f32_16x16x32_bf16 v[8:11], a[128:131], a[32:35], v[8:11]
ds_read_b128 a[156:159], v73 offset:16384
v_max3_f32 v249, v4, v5, v6
v_mfma_f32_16x16x32_bf16 v[12:15], a[128:131], a[36:39], v[12:15]
buffer_load_dwordx4 v245, s[56:59], s71 offen offset:1024 lds
v_mfma_f32_16x16x32_bf16 v[8:11], a[132:135], a[48:51], v[8:11]
ds_read_b128 a[128:131], v74 offset:16384
v_max_f32 v249, v249, v7
v_mfma_f32_16x16x32_bf16 v[12:15], a[132:135], a[52:55], v[12:15]
v_exp_f32 v53, v53
v_mfma_f32_16x16x32_bf16 v[8:11], a[136:139], a[64:67], v[8:11]
ds_read_b128 a[132:135], v75 offset:16384
v_cvt_pk_bf16_f32 v52, v52, v53
v_mfma_f32_16x16x32_bf16 v[12:15], a[136:139], a[68:71], v[12:15]
v_exp_f32 v50, v50
v_mfma_f32_16x16x32_bf16 v[8:11], a[140:143], a[80:83], v[8:11]
ds_read_b128 a[136:139], v72 offset:16640
v_mfma_f32_16x16x32_bf16 v[12:15], a[140:143], a[84:87], v[12:15]
s_waitcnt lgkmcnt(3)
buffer_load_dwordx4 v246, s[56:59], s71 offen offset:2048 lds
v_mfma_f32_16x16x32_bf16 v[8:11], a[144:147], a[96:99], v[8:11]
ds_read_b128 a[140:143], v73 offset:16640
v_mfma_f32_16x16x32_bf16 v[12:15], a[144:147], a[100:103], v[12:15]
v_exp_f32 v51, v51
v_mfma_f32_16x16x32_bf16 v[8:11], a[148:151], a[112:115], v[8:11]
ds_read_b128 a[144:147], v74 offset:16640
v_cvt_pk_bf16_f32 v49, v50, v51
v_mfma_f32_16x16x32_bf16 v[12:15], a[148:151], a[116:119], v[12:15]
v_exp_f32 v54, v54
v_mfma_f32_16x16x32_bf16 v[16:19], a[152:155], a[0:3], v[64:67]
ds_read_b128 a[148:151], v75 offset:16640
v_mfma_f32_16x16x32_bf16 v[20:23], a[152:155], a[4:7], v[68:71]
buffer_load_dwordx4 v247, s[56:59], s71 offen offset:3072 lds
v_mfma_f32_16x16x32_bf16 v[16:19], a[156:159], a[16:19], v[16:19]
ds_read_b128 a[152:155], v72 offset:24576
v_mfma_f32_16x16x32_bf16 v[20:23], a[156:159], a[20:23], v[20:23]
s_waitcnt lgkmcnt(3)
v_max3_f32 v248, v248, v8, v9
v_max3_f32 v248, v248, v10, v11
v_mfma_f32_16x16x32_bf16 v[16:19], a[128:131], a[32:35], v[16:19]
ds_read_b128 a[156:159], v73 offset:24576
v_max3_f32 v249, v249, v12, v13
v_mfma_f32_16x16x32_bf16 v[20:23], a[128:131], a[36:39], v[20:23]
v_max3_f32 v249, v249, v14, v15
v_mfma_f32_16x16x32_bf16 v[16:19], a[132:135], a[48:51], v[16:19]
ds_read_b128 a[128:131], v74 offset:24576
v_mfma_f32_16x16x32_bf16 v[20:23], a[132:135], a[52:55], v[20:23]
v_exp_f32 v55, v55
v_mfma_f32_16x16x32_bf16 v[16:19], a[136:139], a[64:67], v[16:19]
ds_read_b128 a[132:135], v75 offset:24576
v_cvt_pk_bf16_f32 v53, v54, v55
v_mfma_f32_16x16x32_bf16 v[20:23], a[136:139], a[68:71], v[20:23]
v_exp_f32 v56, v56
v_mfma_f32_16x16x32_bf16 v[16:19], a[140:143], a[80:83], v[16:19]
ds_read_b128 a[136:139], v72 offset:24832
v_add_u32 v72, 0xfffe8000, v72
v_mfma_f32_16x16x32_bf16 v[20:23], a[140:143], a[84:87], v[20:23]
s_waitcnt lgkmcnt(3)
v_exp_f32 v57, v57
v_mfma_f32_16x16x32_bf16 v[16:19], a[144:147], a[96:99], v[16:19]
ds_read_b128 a[140:143], v73 offset:24832
v_cvt_pk_bf16_f32 v50, v56, v57
v_mfma_f32_16x16x32_bf16 v[20:23], a[144:147], a[100:103], v[20:23]
v_exp_f32 v60, v60
v_mfma_f32_16x16x32_bf16 v[16:19], a[148:151], a[112:115], v[16:19]
ds_read_b128 a[144:147], v74 offset:24832
v_add_u32 v73, 0xfffe8000, v73
v_mfma_f32_16x16x32_bf16 v[20:23], a[148:151], a[116:119], v[20:23]
v_exp_f32 v61, v61
v_mfma_f32_16x16x32_bf16 v[24:27], a[152:155], a[0:3], v[64:67]
ds_read_b128 a[148:151], v75 offset:24832
v_cvt_pk_bf16_f32 v54, v60, v61
v_mfma_f32_16x16x32_bf16 v[28:31], a[152:155], a[4:7], v[68:71]
s_waitcnt vmcnt(27)
v_exp_f32 v58, v58
v_mfma_f32_16x16x32_bf16 v[24:27], a[156:159], a[16:19], v[24:27]
s_barrier
v_exp_f32 v59, v59
v_mfma_f32_16x16x32_bf16 v[28:31], a[156:159], a[20:23], v[28:31]
s_waitcnt lgkmcnt(2)
v_max3_f32 v248, v248, v16, v17
v_max3_f32 v248, v248, v18, v19
v_mfma_f32_16x16x32_bf16 v[24:27], a[128:131], a[32:35], v[24:27]
ds_read_b64_tr_b16 a[156:157], v82 offset:0
v_max3_f32 v249, v249, v20, v21
v_mfma_f32_16x16x32_bf16 v[28:31], a[128:131], a[36:39], v[28:31]
s_add_u32 s71, s75, 229376
ds_read_b64_tr_b16 a[158:159], v82 offset:8192
v_max3_f32 v249, v249, v22, v23
v_mfma_f32_16x16x32_bf16 v[24:27], a[132:135], a[48:51], v[24:27]
s_add_u32 m0, s77, 98304
ds_read_b64_tr_b16 a[128:129], v83 offset:0
ds_read_b64_tr_b16 a[130:131], v83 offset:8192
v_mfma_f32_16x16x32_bf16 v[28:31], a[132:135], a[52:55], v[28:31]
buffer_load_dwordx4 v244, s[56:59], s71 offen offset:0 lds
v_mfma_f32_16x16x32_bf16 v[24:27], a[136:139], a[64:67], v[24:27]
ds_read_b64_tr_b16 a[132:133], v84 offset:0
v_cvt_pk_bf16_f32 v51, v58, v59
v_mfma_f32_16x16x32_bf16 v[28:31], a[136:139], a[68:71], v[28:31]
ds_read_b64_tr_b16 a[134:135], v84 offset:8192
v_add_u32 v74, 0xfffe8000, v74
v_mfma_f32_16x16x32_bf16 v[24:27], a[140:143], a[80:83], v[24:27]
ds_read_b64_tr_b16 a[136:137], v85 offset:0
v_add_u32 v75, 0xfffe8000, v75
v_mfma_f32_16x16x32_bf16 v[28:31], a[140:143], a[84:87], v[28:31]
s_waitcnt lgkmcnt(5)
ds_read_b64_tr_b16 a[138:139], v85 offset:8192
v_mfma_f32_16x16x32_bf16 v[24:27], a[144:147], a[96:99], v[24:27]
ds_read_b64_tr_b16 a[140:141], v86 offset:0
ds_read_b64_tr_b16 a[142:143], v86 offset:8192
v_mfma_f32_16x16x32_bf16 v[28:31], a[144:147], a[100:103], v[28:31]
buffer_load_dwordx4 v245, s[56:59], s71 offen offset:1024 lds
v_mfma_f32_16x16x32_bf16 v[24:27], a[148:151], a[112:115], v[24:27]
ds_read_b64_tr_b16 a[144:145], v87 offset:0
v_mfma_f32_16x16x32_bf16 v[28:31], a[148:151], a[116:119], v[28:31]
ds_read_b64_tr_b16 a[146:147], v87 offset:8192
v_mfma_f32_16x16x32_bf16 a[192:195], a[200:203], v[32:35], a[192:195]
ds_read_b64_tr_b16 a[148:149], v88 offset:0
v_mfma_f32_16x16x32_bf16 a[196:199], a[200:203], v[36:39], a[196:199]
ds_read_b64_tr_b16 a[150:151], v88 offset:8192
v_mfma_f32_16x16x32_bf16 v[96:99], a[156:159], v[32:35], v[96:99]
ds_read_b64_tr_b16 a[152:153], v89 offset:0
ds_read_b64_tr_b16 a[154:155], v89 offset:8192
v_mfma_f32_16x16x32_bf16 v[100:103], a[156:159], v[36:39], v[100:103]
s_waitcnt lgkmcnt(6)
buffer_load_dwordx4 v246, s[56:59], s71 offen offset:2048 lds
v_mfma_f32_16x16x32_bf16 v[104:107], a[128:131], v[32:35], v[104:107]
ds_read_b64_tr_b16 a[156:157], v82 offset:256
v_max3_f32 v248, v248, v24, v25
v_mfma_f32_16x16x32_bf16 v[108:111], a[128:131], v[36:39], v[108:111]
ds_read_b64_tr_b16 a[158:159], v82 offset:8448
v_max3_f32 v248, v248, v26, v27
v_mfma_f32_16x16x32_bf16 v[112:115], a[132:135], v[32:35], v[112:115]
ds_read_b64_tr_b16 a[128:129], v83 offset:256
v_max3_f32 v249, v249, v28, v29
v_mfma_f32_16x16x32_bf16 v[116:119], a[132:135], v[36:39], v[116:119]
ds_read_b64_tr_b16 a[130:131], v83 offset:8448
v_max3_f32 v249, v249, v30, v31
v_mfma_f32_16x16x32_bf16 v[120:123], a[136:139], v[32:35], v[120:123]
ds_read_b64_tr_b16 a[132:133], v84 offset:256
ds_read_b64_tr_b16 a[134:135], v84 offset:8448
v_mfma_f32_16x16x32_bf16 v[124:127], a[136:139], v[36:39], v[124:127]
buffer_load_dwordx4 v247, s[56:59], s71 offen offset:3072 lds
v_mfma_f32_16x16x32_bf16 v[128:131], a[140:143], v[32:35], v[128:131]
ds_read_b64_tr_b16 a[136:137], v85 offset:256
v_max_f32 v250, v248, v249
v_mfma_f32_16x16x32_bf16 v[132:135], a[140:143], v[36:39], v[132:135]
s_waitcnt lgkmcnt(5)
ds_read_b64_tr_b16 a[138:139], v85 offset:8448
v_mfma_f32_16x16x32_bf16 v[136:139], a[144:147], v[32:35], v[136:139]
s_add_u32 s71, s75, 233472
ds_read_b64_tr_b16 a[140:141], v86 offset:256
v_cmp_lt_f32 vcc, s85, v250
v_mfma_f32_16x16x32_bf16 v[140:143], a[144:147], v[36:39], v[140:143]
s_add_u32 m0, s77, 102400
ds_read_b64_tr_b16 a[142:143], v86 offset:8448
v_mfma_f32_16x16x32_bf16 v[144:147], a[148:151], v[32:35], v[144:147]
s_cbranch_vccnz L_rareA6
L_backA6:
ds_read_b64_tr_b16 a[144:145], v87 offset:256
ds_read_b64_tr_b16 a[146:147], v87 offset:8448
v_mfma_f32_16x16x32_bf16 v[148:151], a[148:151], v[36:39], v[148:151]
buffer_load_dwordx4 v244, s[56:59], s71 offen offset:0 lds
v_mfma_f32_16x16x32_bf16 v[152:155], a[152:155], v[32:35], v[152:155]
ds_read_b64_tr_b16 a[148:149], v88 offset:256
v_mfma_f32_16x16x32_bf16 v[156:159], a[152:155], v[36:39], v[156:159]
ds_read_b64_tr_b16 a[150:151], v88 offset:8448
v_mfma_f32_16x16x32_bf16 v[160:163], a[156:159], v[32:35], v[160:163]
ds_read_b64_tr_b16 a[152:153], v89 offset:256
v_mfma_f32_16x16x32_bf16 v[164:167], a[156:159], v[36:39], v[164:167]
s_waitcnt lgkmcnt(5)
ds_read_b64_tr_b16 a[154:155], v89 offset:8448
v_mfma_f32_16x16x32_bf16 v[168:171], a[128:131], v[32:35], v[168:171]
v_exp_f32 v62, v62
v_mfma_f32_16x16x32_bf16 v[172:175], a[128:131], v[36:39], v[172:175]
buffer_load_dwordx4 v245, s[56:59], s71 offen offset:1024 lds
v_mfma_f32_16x16x32_bf16 v[176:179], a[132:135], v[32:35], v[176:179]
ds_read_b64_tr_b16 a[128:129], v82 offset:16384
v_mfma_f32_16x16x32_bf16 v[180:183], a[132:135], v[36:39], v[180:183]
ds_read_b64_tr_b16 a[130:131], v82 offset:24576
v_mfma_f32_16x16x32_bf16 v[184:187], a[136:139], v[32:35], v[184:187]
ds_read_b64_tr_b16 a[132:133], v83 offset:16384
v_mfma_f32_16x16x32_bf16 v[188:191], a[136:139], v[36:39], v[188:191]
ds_read_b64_tr_b16 a[134:135], v83 offset:24576
v_mfma_f32_16x16x32_bf16 v[192:195], a[140:143], v[32:35], v[192:195]
ds_read_b64_tr_b16 a[136:137], v84 offset:16384
ds_read_b64_tr_b16 a[138:139], v84 offset:24576
v_mfma_f32_16x16x32_bf16 v[196:199], a[140:143], v[36:39], v[196:199]
s_waitcnt lgkmcnt(6)
buffer_load_dwordx4 v246, s[56:59], s71 offen offset:2048 lds
v_mfma_f32_16x16x32_bf16 v[200:203], a[144:147], v[32:35], v[200:203]
ds_read_b64_tr_b16 a[140:141], v85 offset:16384
v_mfma_f32_16x16x32_bf16 v[204:207], a[144:147], v[36:39], v[204:207]
ds_read_b64_tr_b16 a[142:143], v85 offset:24576
v_mfma_f32_16x16x32_bf16 v[208:211], a[148:151], v[32:35], v[208:211]
ds_read_b64_tr_b16 a[144:145], v86 offset:16384
v_mfma_f32_16x16x32_bf16 v[212:215], a[148:151], v[36:39], v[212:215]
ds_read_b64_tr_b16 a[146:147], v86 offset:24576
v_exp_f32 v63, v63
v_mfma_f32_16x16x32_bf16 v[216:219], a[152:155], v[32:35], v[216:219]
ds_read_b64_tr_b16 a[148:149], v87 offset:16384
ds_read_b64_tr_b16 a[150:151], v87 offset:24576
v_cvt_pk_bf16_f32 v55, v62, v63
v_mfma_f32_16x16x32_bf16 v[220:223], a[152:155], v[36:39], v[220:223]
buffer_load_dwordx4 v247, s[56:59], s71 offen offset:3072 lds
v_mfma_f32_16x16x32_bf16 a[192:195], a[200:203], v[48:51], a[192:195]
ds_read_b64_tr_b16 a[152:153], v88 offset:16384
v_exp_f32 v0, v0
v_mfma_f32_16x16x32_bf16 a[196:199], a[200:203], v[52:55], a[196:199]
s_waitcnt lgkmcnt(5)
ds_read_b64_tr_b16 a[154:155], v88 offset:24576
v_exp_f32 v1, v1
v_mfma_f32_16x16x32_bf16 v[96:99], a[128:131], v[48:51], v[96:99]
ds_read_b64_tr_b16 a[156:157], v89 offset:16384
v_exp_f32 v4, v4
v_mfma_f32_16x16x32_bf16 v[100:103], a[128:131], v[52:55], v[100:103]
ds_read_b64_tr_b16 a[158:159], v89 offset:24576
v_exp_f32 v5, v5
v_mfma_f32_16x16x32_bf16 v[104:107], a[132:135], v[48:51], v[104:107]
ds_read_b64_tr_b16 a[128:129], v82 offset:16640
v_exp_f32 v2, v2
v_mfma_f32_16x16x32_bf16 v[108:111], a[132:135], v[52:55], v[108:111]
ds_read_b64_tr_b16 a[130:131], v82 offset:24832
v_add_u32 v82, 0x10000, v82
v_mfma_f32_16x16x32_bf16 v[112:115], a[136:139], v[48:51], v[112:115]
ds_read_b64_tr_b16 a[132:133], v83 offset:16640
v_exp_f32 v3, v3
v_mfma_f32_16x16x32_bf16 v[116:119], a[136:139], v[52:55], v[116:119]
ds_read_b64_tr_b16 a[134:135], v83 offset:24832
v_add_u32 v83, 0x10000, v83
v_mfma_f32_16x16x32_bf16 v[120:123], a[140:143], v[48:51], v[120:123]
ds_read_b64_tr_b16 a[136:137], v84 offset:16640
v_exp_f32 v6, v6
v_mfma_f32_16x16x32_bf16 v[124:127], a[140:143], v[52:55], v[124:127]
s_waitcnt lgkmcnt(5)
ds_read_b64_tr_b16 a[138:139], v84 offset:24832
v_add_u32 v84, 0x10000, v84
v_mfma_f32_16x16x32_bf16 v[128:131], a[144:147], v[48:51], v[128:131]
ds_read_b64_tr_b16 a[140:141], v85 offset:16640
v_exp_f32 v7, v7
v_mfma_f32_16x16x32_bf16 v[132:135], a[144:147], v[52:55], v[132:135]
ds_read_b64_tr_b16 a[142:143], v85 offset:24832
v_add_u32 v85, 0x10000, v85
v_mfma_f32_16x16x32_bf16 v[136:139], a[148:151], v[48:51], v[136:139]
ds_read_b64_tr_b16 a[144:145], v86 offset:16640
v_cvt_pk_bf16_f32 v0, v0, v1
v_mfma_f32_16x16x32_bf16 v[140:143], a[148:151], v[52:55], v[140:143]
ds_read_b64_tr_b16 a[146:147], v86 offset:24832
v_add_u32 v86, 0x10000, v86
v_mfma_f32_16x16x32_bf16 v[144:147], a[152:155], v[48:51], v[144:147]
ds_read_b64_tr_b16 a[148:149], v87 offset:16640
v_cvt_pk_bf16_f32 v4, v4, v5
v_mfma_f32_16x16x32_bf16 v[148:151], a[152:155], v[52:55], v[148:151]
ds_read_b64_tr_b16 a[150:151], v87 offset:24832
v_add_u32 v87, 0x10000, v87
v_mfma_f32_16x16x32_bf16 v[152:155], a[156:159], v[48:51], v[152:155]
ds_read_b64_tr_b16 a[152:153], v88 offset:16640
v_cvt_pk_bf16_f32 v1, v2, v3
v_mfma_f32_16x16x32_bf16 v[156:159], a[156:159], v[52:55], v[156:159]
s_waitcnt lgkmcnt(5)
ds_read_b64_tr_b16 a[154:155], v88 offset:24832
v_cvt_pk_bf16_f32 v5, v6, v7
v_mfma_f32_16x16x32_bf16 v[160:163], a[128:131], v[48:51], v[160:163]
ds_read_b64_tr_b16 a[156:157], v89 offset:16640
v_add_u32 v88, 0x10000, v88
v_mfma_f32_16x16x32_bf16 v[164:167], a[128:131], v[52:55], v[164:167]
s_waitcnt vmcnt(19)
ds_read_b64_tr_b16 a[158:159], v89 offset:24832
v_add_u32 v89, 0x10000, v89
v_mfma_f32_16x16x32_bf16 v[168:171], a[132:135], v[48:51], v[168:171]
s_barrier
ds_read_b128 a[128:131], v72 offset:0
v_mfma_f32_16x16x32_bf16 v[172:175], a[132:135], v[52:55], v[172:175]
v_exp_f32 v8, v8
v_mfma_f32_16x16x32_bf16 v[176:179], a[136:139], v[48:51], v[176:179]
ds_read_b128 a[132:135], v73 offset:0
v_mfma_f32_16x16x32_bf16 v[180:183], a[136:139], v[52:55], v[180:183]
s_add_u32 s71, s76, 0
v_exp_f32 v9, v9
v_mfma_f32_16x16x32_bf16 v[184:187], a[140:143], v[48:51], v[184:187]
s_add_u32 m0, s77, 131072
ds_read_b128 a[136:139], v74 offset:0
v_cvt_pk_bf16_f32 v2, v8, v9
v_mfma_f32_16x16x32_bf16 v[188:191], a[140:143], v[52:55], v[188:191]
s_waitcnt lgkmcnt(3)
buffer_load_dwordx4 v90, s[60:63], s71 offen offset:0 lds
v_mfma_f32_16x16x32_bf16 v[192:195], a[144:147], v[48:51], v[192:195]
ds_read_b128 a[140:143], v75 offset:0
v_mfma_f32_16x16x32_bf16 v[196:199], a[144:147], v[52:55], v[196:199]
v_exp_f32 v12, v12
v_mfma_f32_16x16x32_bf16 v[200:203], a[148:151], v[48:51], v[200:203]
ds_read_b128 a[144:147], v72 offset:256
v_mfma_f32_16x16x32_bf16 v[204:207], a[148:151], v[52:55], v[204:207]
v_exp_f32 v13, v13
v_mfma_f32_16x16x32_bf16 v[208:211], a[152:155], v[48:51], v[208:211]
ds_read_b128 a[148:151], v73 offset:256
v_cvt_pk_bf16_f32 v6, v12, v13
v_mfma_f32_16x16x32_bf16 v[212:215], a[152:155], v[52:55], v[212:215]
buffer_load_dwordx4 v91, s[60:63], s71 offen offset:1024 lds
v_mfma_f32_16x16x32_bf16 v[216:219], a[156:159], v[48:51], v[216:219]
s_cmp_lg_u32 s84, 0
ds_read_b128 a[152:155], v74 offset:256
v_mfma_f32_16x16x32_bf16 v[220:223], a[156:159], v[52:55], v[220:223]
s_cbranch_scc1 L_rareB6
L_backB6:
s_waitcnt lgkmcnt(3)
v_mfma_f32_16x16x32_bf16 v[32:35], a[128:131], a[0:3], v[64:67]
ds_read_b128 a[156:159], v75 offset:256
v_mfma_f32_16x16x32_bf16 v[36:39], a[128:131], a[4:7], v[68:71]
v_exp_f32 v10, v10
v_mfma_f32_16x16x32_bf16 v[32:35], a[132:135], a[16:19], v[32:35]
ds_read_b128 a[128:131], v72 offset:8192
v_mfma_f32_16x16x32_bf16 v[36:39], a[132:135], a[20:23], v[36:39]
buffer_load_dwordx4 v92, s[60:63], s71 offen offset:2048 lds
v_mfma_f32_16x16x32_bf16 v[32:35], a[136:139], a[32:35], v[32:35]
ds_read_b128 a[132:135], v73 offset:8192
v_mfma_f32_16x16x32_bf16 v[36:39], a[136:139], a[36:39], v[36:39]
v_exp_f32 v11, v11
v_mfma_f32_16x16x32_bf16 v[32:35], a[140:143], a[48:51], v[32:35]
ds_read_b128 a[136:139], v74 offset:8192
v_cvt_pk_bf16_f32 v3, v10, v11
v_mfma_f32_16x16x32_bf16 v[36:39], a[140:143], a[52:55], v[36:39]
s_waitcnt lgkmcnt(3)
v_exp_f32 v14, v14
v_mfma_f32_16x16x32_bf16 v[32:35], a[144:147], a[64:67], v[32:35]
ds_read_b128 a[140:143], v75 offset:8192
v_mfma_f32_16x16x32_bf16 v[36:39], a[144:147], a[68:71], v[36:39]
buffer_load_dwordx4 v93, s[60:63], s71 offen offset:3072 lds
v_mfma_f32_16x16x32_bf16 v[32:35], a[148:151], a[80:83], v[32:35]
ds_read_b128 a[144:147], v72 offset:8448
v_mfma_f32_16x16x32_bf16 v[36:39], a[148:151], a[84:87], v[36:39]
v_exp_f32 v15, v15
v_mfma_f32_16x16x32_bf16 v[32:35], a[152:155], a[96:99], v[32:35]
ds_read_b128 a[148:151], v73 offset:8448
v_cvt_pk_bf16_f32 v7, v14, v15
v_mfma_f32_16x16x32_bf16 v[36:39], a[152:155], a[100:103], v[36:39]
s_add_u32 s71, s76, 28672
v_exp_f32 v16, v16
v_mfma_f32_16x16x32_bf16 v[32:35], a[156:159], a[112:115], v[32:35]
s_add_u32 m0, s77, 135168
ds_read_b128 a[152:155], v74 offset:8448
v_mfma_f32_16x16x32_bf16 v[36:39], a[156:159], a[116:119], v[36:39]
s_waitcnt lgkmcnt(3)
buffer_load_dwordx4 v90, s[60:63], s71 offen offset:0 lds
v_mfma_f32_16x16x32_bf16 v[40:43], a[128:131], a[0:3], v[64:67]
ds_read_b128 a[156:159], v75 offset:8448
v_mfma_f32_16x16x32_bf16 v[44:47], a[128:131], a[4:7], v[68:71]
v_exp_f32 v17, v17
v_mfma_f32_16x16x32_bf16 v[40:43], a[132:135], a[16:19], v[40:43]
ds_read_b128 a[128:131], v72 offset:16384
v_cvt_pk_bf16_f32 v16, v16, v17
v_mfma_f32_16x16x32_bf16 v[44:47], a[132:135], a[20:23], v[44:47]
v_max3_f32 v248, v32, v33, v34
v_max_f32 v248, v248, v35
v_mfma_f32_16x16x32_bf16 v[40:43], a[136:139], a[32:35], v[40:43]
ds_read_b128 a[132:135], v73 offset:16384
v_max3_f32 v249, v36, v37, v38
v_mfma_f32_16x16x32_bf16 v[44:47], a[136:139], a[36:39], v[44:47]
buffer_load_dwordx4 v91, s[60:63], s71 offen offset:1024 lds
v_mfma_f32_16x16x32_bf16 v[40:43], a[140:143], a[48:51], v[40:43]
ds_read_b128 a[136:139], v74 offset:16384
v_max_f32 v249, v249, v39
v_mfma_f32_16x16x32_bf16 v[44:47], a[140:143], a[52:55], v[44:47]
s_waitcnt lgkmcnt(3)
v_exp_f32 v20, v20
v_mfma_f32_16x16x32_bf16 v[40:43], a[144:147], a[64:67], v[40:43]
ds_read_b128 a[140:143], v75 offset:16384
v_mfma_f32_16x16x32_bf16 v[44:47], a[144:147], a[68:71], v[44:47]
v_exp_f32 v21, v21
v_mfma_f32_16x16x32_bf16 v[40:43], a[148:151], a[80:83], v[40:43]
ds_read_b128 a[144:147], v72 offset:16640
v_cvt_pk_bf16_f32 v20, v20, v21
v_mfma_f32_16x16x32_bf16 v[44:47], a[148:151], a[84:87], v[44:47]
buffer_load_dwordx4 v92, s[60:63], s71 offen offset:2048 lds
v_mfma_f32_16x16x32_bf16 v[40:43], a[152:155], a[96:99], v[40:43]
ds_read_b128 a[148:151], v73 offset:16640
v_mfma_f32_16x16x32_bf16 v[44:47], a[152:155], a[100:103], v[44:47]
v_exp_f32 v18, v18
v_mfma_f32_16x16x32_bf16 v[40:43], a[156:159], a[112:115], v[40:43]
ds_read_b128 a[152:155], v74 offset:16640
v_mfma_f32_16x16x32_bf16 v[44:47], a[156:159], a[116:119], v[44:47]
s_waitcnt lgkmcnt(3)
v_exp_f32 v19, v19
v_mfma_f32_16x16x32_bf16 v[48:51], a[128:131], a[0:3], v[64:67]
ds_read_b128 a[156:159], v75 offset:16640
v_cvt_pk_bf16_f32 v17, v18, v19
v_mfma_f32_16x16x32_bf16 v[52:55], a[128:131], a[4:7], v[68:71]
buffer_load_dwordx4 v93, s[60:63], s71 offen offset:3072 lds
v_mfma_f32_16x16x32_bf16 v[48:51], a[132:135], a[16:19], v[48:51]
ds_read_b128 a[128:131], v72 offset:24576
v_mfma_f32_16x16x32_bf16 v[52:55], a[132:135], a[20:23], v[52:55]
v_max3_f32 v248, v248, v40, v41
v_max3_f32 v248, v248, v42, v43
v_mfma_f32_16x16x32_bf16 v[48:51], a[136:139], a[32:35], v[48:51]
ds_read_b128 a[132:135], v73 offset:24576
v_max3_f32 v249, v249, v44, v45
v_mfma_f32_16x16x32_bf16 v[52:55], a[136:139], a[36:39], v[52:55]
v_max3_f32 v249, v249, v46, v47
v_mfma_f32_16x16x32_bf16 v[48:51], a[140:143], a[48:51], v[48:51]
ds_read_b128 a[136:139], v74 offset:24576
v_mfma_f32_16x16x32_bf16 v[52:55], a[140:143], a[52:55], v[52:55]
s_waitcnt lgkmcnt(3)
v_exp_f32 v22, v22
v_mfma_f32_16x16x32_bf16 v[48:51], a[144:147], a[64:67], v[48:51]
ds_read_b128 a[140:143], v75 offset:24576
v_mfma_f32_16x16x32_bf16 v[52:55], a[144:147], a[68:71], v[52:55]
v_exp_f32 v23, v23
v_mfma_f32_16x16x32_bf16 v[48:51], a[148:151], a[80:83], v[48:51]
ds_read_b128 a[144:147], v72 offset:24832
v_cvt_pk_bf16_f32 v21, v22, v23
v_mfma_f32_16x16x32_bf16 v[52:55], a[148:151], a[84:87], v[52:55]
v_exp_f32 v24, v24
v_mfma_f32_16x16x32_bf16 v[48:51], a[152:155], a[96:99], v[48:51]
ds_read_b128 a[148:151], v73 offset:24832
v_mfma_f32_16x16x32_bf16 v[52:55], a[152:155], a[100:103], v[52:55]
v_exp_f32 v25, v25
v_mfma_f32_16x16x32_bf16 v[48:51], a[156:159], a[112:115], v[48:51]
ds_read_b128 a[152:155], v74 offset:24832
v_cvt_pk_bf16_f32 v18, v24, v25
v_mfma_f32_16x16x32_bf16 v[52:55], a[156:159], a[116:119], v[52:55]
s_waitcnt lgkmcnt(3)
v_exp_f32 v28, v28
v_mfma_f32_16x16x32_bf16 v[56:59], a[128:131], a[0:3], v[64:67]
ds_read_b128 a[156:159], v75 offset:24832
v_mfma_f32_16x16x32_bf16 v[60:63], a[128:131], a[4:7], v[68:71]
s_waitcnt vmcnt(16)
v_exp_f32 v29, v29
v_mfma_f32_16x16x32_bf16 v[56:59], a[132:135], a[16:19], v[56:59]
s_barrier
v_exp_f32 v26, v26
v_mfma_f32_16x16x32_bf16 v[60:63], a[132:135], a[20:23], v[60:63]
v_max3_f32 v248, v248, v48, v49
v_max3_f32 v248, v248, v50, v51
v_mfma_f32_16x16x32_bf16 v[56:59], a[136:139], a[32:35], v[56:59]
ds_read_b64_tr_b16 a[132:133], v82 offset:0
v_max3_f32 v249, v249, v52, v53
v_mfma_f32_16x16x32_bf16 v[60:63], a[136:139], a[36:39], v[60:63]
s_add_u32 s71, s76, 128
ds_read_b64_tr_b16 a[134:135], v82 offset:8192
v_max3_f32 v249, v249, v54, v55
v_mfma_f32_16x16x32_bf16 v[56:59], a[140:143], a[48:51], v[56:59]
s_add_u32 m0, s77, 0
ds_read_b64_tr_b16 a[136:137], v83 offset:0
ds_read_b64_tr_b16 a[138:139], v83 offset:8192
v_mfma_f32_16x16x32_bf16 v[60:63], a[140:143], a[52:55], v[60:63]
s_waitcnt lgkmcnt(4)
buffer_load_dwordx4 v90, s[60:63], s71 offen offset:0 lds
v_mfma_f32_16x16x32_bf16 v[56:59], a[144:147], a[64:67], v[56:59]
ds_read_b64_tr_b16 a[140:141], v84 offset:0
v_cvt_pk_bf16_f32 v22, v28, v29
v_mfma_f32_16x16x32_bf16 v[60:63], a[144:147], a[68:71], v[60:63]
ds_read_b64_tr_b16 a[142:143], v84 offset:8192
v_mfma_f32_16x16x32_bf16 v[56:59], a[148:151], a[80:83], v[56:59]
ds_read_b64_tr_b16 a[144:145], v85 offset:0
v_mfma_f32_16x16x32_bf16 v[60:63], a[148:151], a[84:87], v[60:63]
ds_read_b64_tr_b16 a[146:147], v85 offset:8192
v_mfma_f32_16x16x32_bf16 v[56:59], a[152:155], a[96:99], v[56:59]
ds_read_b64_tr_b16 a[148:149], v86 offset:0
ds_read_b64_tr_b16 a[150:151], v86 offset:8192
v_mfma_f32_16x16x32_bf16 v[60:63], a[152:155], a[100:103], v[60:63]
buffer_load_dwordx4 v91, s[60:63], s71 offen offset:1024 lds
v_mfma_f32_16x16x32_bf16 v[56:59], a[156:159], a[112:115], v[56:59]
ds_read_b64_tr_b16 a[152:153], v87 offset:0
v_mfma_f32_16x16x32_bf16 v[60:63], a[156:159], a[116:119], v[60:63]
s_waitcnt lgkmcnt(5)
ds_read_b64_tr_b16 a[154:155], v87 offset:8192
v_mfma_f32_16x16x32_bf16 a[192:195], a[200:203], v[0:3], a[192:195]
ds_read_b64_tr_b16 a[156:157], v88 offset:0
ds_read_b64_tr_b16 a[158:159], v88 offset:8192
v_mfma_f32_16x16x32_bf16 a[196:199], a[200:203], v[4:7], a[196:199]
buffer_load_dwordx2 a[4:5], v238, s[64:67], s93 offen offset:0 sc1
v_mfma_f32_16x16x32_bf16 v[96:99], a[132:135], v[0:3], v[96:99]
ds_read_b64_tr_b16 a[128:129], v89 offset:0
ds_read_b64_tr_b16 a[130:131], v89 offset:8192
v_add_u32 v238, 28672, v238
v_mfma_f32_16x16x32_bf16 v[100:103], a[132:135], v[4:7], v[100:103]
buffer_load_dwordx4 v92, s[60:63], s71 offen offset:2048 lds
v_mfma_f32_16x16x32_bf16 v[104:107], a[136:139], v[0:3], v[104:107]
ds_read_b64_tr_b16 a[132:133], v82 offset:256
ds_read_b64_tr_b16 a[134:135], v82 offset:8448
v_mfma_f32_16x16x32_bf16 v[108:111], a[136:139], v[4:7], v[108:111]
buffer_load_dwordx4 a[164:167], v238, s[64:67], s92 offen offset:0 sc1
v_mfma_f32_16x16x32_bf16 v[112:115], a[140:143], v[0:3], v[112:115]
ds_read_b64_tr_b16 a[136:137], v83 offset:256
v_max3_f32 v248, v248, v56, v57
v_mfma_f32_16x16x32_bf16 v[116:119], a[140:143], v[4:7], v[116:119]
s_waitcnt lgkmcnt(5)
ds_read_b64_tr_b16 a[138:139], v83 offset:8448
v_max3_f32 v248, v248, v58, v59
v_mfma_f32_16x16x32_bf16 v[120:123], a[144:147], v[0:3], v[120:123]
ds_read_b64_tr_b16 a[140:141], v84 offset:256
ds_read_b64_tr_b16 a[142:143], v84 offset:8448
v_mfma_f32_16x16x32_bf16 v[124:127], a[144:147], v[4:7], v[124:127]
buffer_load_dwordx4 v93, s[60:63], s71 offen offset:3072 lds
v_mfma_f32_16x16x32_bf16 v[128:131], a[148:151], v[0:3], v[128:131]
ds_read_b64_tr_b16 a[144:145], v85 offset:256
ds_read_b64_tr_b16 a[146:147], v85 offset:8448
v_mfma_f32_16x16x32_bf16 v[132:135], a[148:151], v[4:7], v[132:135]
buffer_load_dwordx4 a[204:207], v238, s[64:67], s92 offen offset:1024 sc1
v_max3_f32 v249, v249, v60, v61
v_max3_f32 v249, v249, v62, v63
v_max_f32 v250, v248, v249
v_mfma_f32_16x16x32_bf16 v[136:139], a[152:155], v[0:3], v[136:139]
s_add_u32 s71, s76, 28800
ds_read_b64_tr_b16 a[148:149], v86 offset:256
v_cmp_lt_f32 vcc, s85, v250
v_mfma_f32_16x16x32_bf16 v[140:143], a[152:155], v[4:7], v[140:143]
s_add_u32 m0, s77, 4096
ds_read_b64_tr_b16 a[150:151], v86 offset:8448
v_mfma_f32_16x16x32_bf16 v[144:147], a[156:159], v[0:3], v[144:147]
s_cbranch_vccnz L_rareA7
L_backA7:
ds_read_b64_tr_b16 a[152:153], v87 offset:256
ds_read_b64_tr_b16 a[154:155], v87 offset:8448
v_mfma_f32_16x16x32_bf16 v[148:151], a[156:159], v[4:7], v[148:151]
s_waitcnt lgkmcnt(6)
buffer_load_dwordx4 v90, s[60:63], s71 offen offset:0 lds
v_mfma_f32_16x16x32_bf16 v[152:155], a[128:131], v[0:3], v[152:155]
ds_read_b64_tr_b16 a[156:157], v88 offset:256
ds_read_b64_tr_b16 a[158:159], v88 offset:8448
v_mfma_f32_16x16x32_bf16 v[156:159], a[128:131], v[4:7], v[156:159]
buffer_load_dwordx4 a[0:3], v238, s[64:67], s92 offen offset:2048 sc1
v_mfma_f32_16x16x32_bf16 v[160:163], a[132:135], v[0:3], v[160:163]
ds_read_b64_tr_b16 a[128:129], v89 offset:256
v_mfma_f32_16x16x32_bf16 v[164:167], a[132:135], v[4:7], v[164:167]
ds_read_b64_tr_b16 a[130:131], v89 offset:8448
v_mfma_f32_16x16x32_bf16 v[168:171], a[136:139], v[0:3], v[168:171]
v_exp_f32 v27, v27
v_mfma_f32_16x16x32_bf16 v[172:175], a[136:139], v[4:7], v[172:175]
buffer_load_dwordx4 v91, s[60:63], s71 offen offset:1024 lds
v_mfma_f32_16x16x32_bf16 v[176:179], a[140:143], v[0:3], v[176:179]
ds_read_b64_tr_b16 a[136:137], v82 offset:16384
ds_read_b64_tr_b16 a[138:139], v82 offset:24576
v_mfma_f32_16x16x32_bf16 v[180:183], a[140:143], v[4:7], v[180:183]
s_waitcnt lgkmcnt(4)
buffer_load_dwordx2 a[6:7], v238, s[64:67], s93 offen offset:0 sc1
v_mfma_f32_16x16x32_bf16 v[184:187], a[144:147], v[0:3], v[184:187]
ds_read_b64_tr_b16 a[140:141], v83 offset:16384
v_add_u32 v238, 28672, v238
v_mfma_f32_16x16x32_bf16 v[188:191], a[144:147], v[4:7], v[188:191]
ds_read_b64_tr_b16 a[142:143], v83 offset:24576
v_cvt_pk_bf16_f32 v19, v26, v27
v_mfma_f32_16x16x32_bf16 v[192:195], a[148:151], v[0:3], v[192:195]
ds_read_b64_tr_b16 a[144:145], v84 offset:16384
ds_read_b64_tr_b16 a[146:147], v84 offset:24576
v_mfma_f32_16x16x32_bf16 v[196:199], a[148:151], v[4:7], v[196:199]
buffer_load_dwordx4 v92, s[60:63], s71 offen offset:2048 lds
v_mfma_f32_16x16x32_bf16 v[200:203], a[152:155], v[0:3], v[200:203]
ds_read_b64_tr_b16 a[148:149], v85 offset:16384
ds_read_b64_tr_b16 a[150:151], v85 offset:24576
v_mfma_f32_16x16x32_bf16 v[204:207], a[152:155], v[4:7], v[204:207]
buffer_load_dwordx4 a[16:19], v238, s[64:67], s92 offen offset:0 sc1
v_mfma_f32_16x16x32_bf16 v[208:211], a[156:159], v[0:3], v[208:211]
ds_read_b64_tr_b16 a[152:153], v86 offset:16384
v_mfma_f32_16x16x32_bf16 v[212:215], a[156:159], v[4:7], v[212:215]
s_waitcnt lgkmcnt(5)
ds_read_b64_tr_b16 a[154:155], v86 offset:24576
v_exp_f32 v30, v30
v_exp_f32 v31, v31
v_mfma_f32_16x16x32_bf16 v[216:219], a[128:131], v[0:3], v[216:219]
ds_read_b64_tr_b16 a[156:157], v87 offset:16384
ds_read_b64_tr_b16 a[158:159], v87 offset:24576
v_cvt_pk_bf16_f32 v23, v30, v31
v_mfma_f32_16x16x32_bf16 v[220:223], a[128:131], v[4:7], v[220:223]
buffer_load_dwordx4 v93, s[60:63], s71 offen offset:3072 lds
v_mfma_f32_16x16x32_bf16 a[192:195], a[200:203], v[16:19], a[192:195]
ds_read_b64_tr_b16 a[128:129], v88 offset:16384
ds_read_b64_tr_b16 a[130:131], v88 offset:24576
v_mfma_f32_16x16x32_bf16 a[196:199], a[200:203], v[20:23], a[196:199]
buffer_load_dwordx4 a[20:23], v238, s[64:67], s92 offen offset:1024 sc1
v_mfma_f32_16x16x32_bf16 v[96:99], a[136:139], v[16:19], v[96:99]
ds_read_b64_tr_b16 a[132:133], v89 offset:16384
v_exp_f32 v32, v32
v_mfma_f32_16x16x32_bf16 v[100:103], a[136:139], v[20:23], v[100:103]
ds_read_b64_tr_b16 a[134:135], v89 offset:24576
v_exp_f32 v33, v33
v_mfma_f32_16x16x32_bf16 v[104:107], a[140:143], v[16:19], v[104:107]
ds_read_b64_tr_b16 a[136:137], v82 offset:16640
ds_read_b64_tr_b16 a[138:139], v82 offset:24832
v_mfma_f32_16x16x32_bf16 v[108:111], a[140:143], v[20:23], v[108:111]
s_waitcnt lgkmcnt(6)
buffer_load_dwordx4 a[32:35], v238, s[64:67], s92 offen offset:2048 sc1
v_mfma_f32_16x16x32_bf16 v[112:115], a[144:147], v[16:19], v[112:115]
ds_read_b64_tr_b16 a[140:141], v83 offset:16640
v_exp_f32 v36, v36
v_mfma_f32_16x16x32_bf16 v[116:119], a[144:147], v[20:23], v[116:119]
ds_read_b64_tr_b16 a[142:143], v83 offset:24832
v_exp_f32 v37, v37
v_mfma_f32_16x16x32_bf16 v[120:123], a[148:151], v[16:19], v[120:123]
ds_read_b64_tr_b16 a[144:145], v84 offset:16640
ds_read_b64_tr_b16 a[146:147], v84 offset:24832
v_mfma_f32_16x16x32_bf16 v[124:127], a[148:151], v[20:23], v[124:127]
buffer_load_dwordx2 a[64:65], v238, s[64:67], s93 offen offset:0 sc1
v_mfma_f32_16x16x32_bf16 v[128:131], a[152:155], v[16:19], v[128:131]
ds_read_b64_tr_b16 a[148:149], v85 offset:16640
v_add_u32 v238, 28672, v238
v_mfma_f32_16x16x32_bf16 v[132:135], a[152:155], v[20:23], v[132:135]
ds_read_b64_tr_b16 a[150:151], v85 offset:24832
v_exp_f32 v34, v34
v_mfma_f32_16x16x32_bf16 v[136:139], a[156:159], v[16:19], v[136:139]
ds_read_b64_tr_b16 a[152:153], v86 offset:16640
ds_read_b64_tr_b16 a[154:155], v86 offset:24832
v_mfma_f32_16x16x32_bf16 v[140:143], a[156:159], v[20:23], v[140:143]
s_waitcnt lgkmcnt(6)
buffer_load_dwordx4 a[36:39], v238, s[64:67], s92 offen offset:0 sc1
v_mfma_f32_16x16x32_bf16 v[144:147], a[128:131], v[16:19], v[144:147]
ds_read_b64_tr_b16 a[156:157], v87 offset:16640
v_exp_f32 v35, v35
v_mfma_f32_16x16x32_bf16 v[148:151], a[128:131], v[20:23], v[148:151]
ds_read_b64_tr_b16 a[158:159], v87 offset:24832
v_exp_f32 v38, v38
v_mfma_f32_16x16x32_bf16 v[152:155], a[132:135], v[16:19], v[152:155]
ds_read_b64_tr_b16 a[128:129], v88 offset:16640
ds_read_b64_tr_b16 a[130:131], v88 offset:24832
v_mfma_f32_16x16x32_bf16 v[156:159], a[132:135], v[20:23], v[156:159]
buffer_load_dwordx4 a[48:51], v238, s[64:67], s92 offen offset:1024 sc1
v_mfma_f32_16x16x32_bf16 v[160:163], a[136:139], v[16:19], v[160:163]
ds_read_b64_tr_b16 a[132:133], v89 offset:16640
v_exp_f32 v39, v39
v_mfma_f32_16x16x32_bf16 v[164:167], a[136:139], v[20:23], v[164:167]
s_waitcnt vmcnt(27)
ds_read_b64_tr_b16 a[134:135], v89 offset:24832
v_exp_f32 v40, v40
v_mfma_f32_16x16x32_bf16 v[168:171], a[140:143], v[16:19], v[168:171]
s_barrier
v_exp_f32 v41, v41
v_mfma_f32_16x16x32_bf16 v[172:175], a[140:143], v[20:23], v[172:175]
s_waitcnt lgkmcnt(4)
buffer_load_dwordx4 a[52:55], v238, s[64:67], s92 offen offset:2048 sc1
v_mfma_f32_16x16x32_bf16 v[176:179], a[144:147], v[16:19], v[176:179]
ds_read_b64_tr_b16 a[140:141], v82 offset:32768
v_exp_f32 v44, v44
v_mfma_f32_16x16x32_bf16 v[180:183], a[144:147], v[20:23], v[180:183]
s_add_u32 s71, s76, 256
ds_read_b64_tr_b16 a[142:143], v82 offset:40960
v_exp_f32 v45, v45
v_mfma_f32_16x16x32_bf16 v[184:187], a[148:151], v[16:19], v[184:187]
s_add_u32 m0, s77, 32768
ds_read_b64_tr_b16 a[144:145], v83 offset:32768
ds_read_b64_tr_b16 a[146:147], v83 offset:40960
v_mfma_f32_16x16x32_bf16 v[188:191], a[148:151], v[20:23], v[188:191]
buffer_load_dwordx4 v90, s[60:63], s71 offen offset:0 lds
v_mfma_f32_16x16x32_bf16 v[192:195], a[152:155], v[16:19], v[192:195]
ds_read_b64_tr_b16 a[148:149], v84 offset:32768
ds_read_b64_tr_b16 a[150:151], v84 offset:40960
v_mfma_f32_16x16x32_bf16 v[196:199], a[152:155], v[20:23], v[196:199]
buffer_load_dwordx2 a[66:67], v238, s[64:67], s93 offen offset:0 sc1
v_mfma_f32_16x16x32_bf16 v[200:203], a[156:159], v[16:19], v[200:203]
ds_read_b64_tr_b16 a[152:153], v85 offset:32768
v_add_u32 v238, 28672, v238
v_mfma_f32_16x16x32_bf16 v[204:207], a[156:159], v[20:23], v[204:207]
s_waitcnt lgkmcnt(5)
ds_read_b64_tr_b16 a[154:155], v85 offset:40960
v_exp_f32 v42, v42
v_mfma_f32_16x16x32_bf16 v[208:211], a[128:131], v[16:19], v[208:211]
ds_read_b64_tr_b16 a[156:157], v86 offset:32768
ds_read_b64_tr_b16 a[158:159], v86 offset:40960
v_exp_f32 v43, v43
v_mfma_f32_16x16x32_bf16 v[212:215], a[128:131], v[20:23], v[212:215]
buffer_load_dwordx4 v91, s[60:63], s71 offen offset:1024 lds
v_exp_f32 v46, v46
v_exp_f32 v47, v47
v_cvt_pk_bf16_f32 v32, v32, v33
v_cvt_pk_bf16_f32 v36, v36, v37
v_cvt_pk_bf16_f32 v33, v34, v35
v_cvt_pk_bf16_f32 v37, v38, v39
v_cvt_pk_bf16_f32 v34, v40, v41
v_cvt_pk_bf16_f32 v38, v44, v45
v_cvt_pk_bf16_f32 v35, v42, v43
v_mfma_f32_16x16x32_bf16 v[216:219], a[132:135], v[16:19], v[216:219]
s_cmp_lg_u32 s84, 0
ds_read_b64_tr_b16 a[128:129], v87 offset:32768
ds_read_b64_tr_b16 a[130:131], v87 offset:40960
v_cvt_pk_bf16_f32 v39, v46, v47
v_mfma_f32_16x16x32_bf16 v[220:223], a[132:135], v[20:23], v[220:223]
s_cbranch_scc1 L_rareB7
L_backB7:
buffer_load_dwordx4 a[68:71], v238, s[64:67], s92 offen offset:0 sc1
v_mfma_f32_16x16x32_bf16 a[192:195], a[200:203], v[32:35], a[192:195]
ds_read_b64_tr_b16 a[132:133], v88 offset:32768
v_exp_f32 v48, v48
v_mfma_f32_16x16x32_bf16 a[196:199], a[200:203], v[36:39], a[196:199]
ds_read_b64_tr_b16 a[134:135], v88 offset:40960
v_exp_f32 v49, v49
v_mfma_f32_16x16x32_bf16 v[96:99], a[140:143], v[32:35], v[96:99]
ds_read_b64_tr_b16 a[136:137], v89 offset:32768
ds_read_b64_tr_b16 a[138:139], v89 offset:40960
v_mfma_f32_16x16x32_bf16 v[100:103], a[140:143], v[36:39], v[100:103]
s_waitcnt lgkmcnt(6)
buffer_load_dwordx4 v92, s[60:63], s71 offen offset:2048 lds
v_mfma_f32_16x16x32_bf16 v[104:107], a[144:147], v[32:35], v[104:107]
ds_read_b64_tr_b16 a[140:141], v82 offset:33024
ds_read_b64_tr_b16 a[142:143], v82 offset:41216
v_mfma_f32_16x16x32_bf16 v[108:111], a[144:147], v[36:39], v[108:111]
buffer_load_dwordx4 a[80:83], v238, s[64:67], s92 offen offset:1024 sc1
v_mfma_f32_16x16x32_bf16 v[112:115], a[148:151], v[32:35], v[112:115]
ds_read_b64_tr_b16 a[144:145], v83 offset:33024
v_exp_f32 v52, v52
v_mfma_f32_16x16x32_bf16 v[116:119], a[148:151], v[36:39], v[116:119]
ds_read_b64_tr_b16 a[146:147], v83 offset:41216
v_exp_f32 v53, v53
v_mfma_f32_16x16x32_bf16 v[120:123], a[152:155], v[32:35], v[120:123]
ds_read_b64_tr_b16 a[148:149], v84 offset:33024
ds_read_b64_tr_b16 a[150:151], v84 offset:41216
v_mfma_f32_16x16x32_bf16 v[124:127], a[152:155], v[36:39], v[124:127]
buffer_load_dwordx4 v93, s[60:63], s71 offen offset:3072 lds
v_mfma_f32_16x16x32_bf16 v[128:131], a[156:159], v[32:35], v[128:131]
ds_read_b64_tr_b16 a[152:153], v85 offset:33024
ds_read_b64_tr_b16 a[154:155], v85 offset:41216
v_mfma_f32_16x16x32_bf16 v[132:135], a[156:159], v[36:39], v[132:135]
s_waitcnt lgkmcnt(6)
buffer_load_dwordx4 a[84:87], v238, s[64:67], s92 offen offset:2048 sc1
v_mfma_f32_16x16x32_bf16 v[136:139], a[128:131], v[32:35], v[136:139]
ds_read_b64_tr_b16 a[156:157], v86 offset:33024
v_exp_f32 v50, v50
v_mfma_f32_16x16x32_bf16 v[140:143], a[128:131], v[36:39], v[140:143]
s_add_u32 s71, s76, 28928
ds_read_b64_tr_b16 a[158:159], v86 offset:41216
v_exp_f32 v51, v51
v_mfma_f32_16x16x32_bf16 v[144:147], a[132:135], v[32:35], v[144:147]
s_add_u32 m0, s77, 36864
ds_read_b64_tr_b16 a[128:129], v87 offset:33024
ds_read_b64_tr_b16 a[130:131], v87 offset:41216
v_mfma_f32_16x16x32_bf16 v[148:151], a[132:135], v[36:39], v[148:151]
buffer_load_dwordx4 v90, s[60:63], s71 offen offset:0 lds
v_mfma_f32_16x16x32_bf16 v[152:155], a[136:139], v[32:35], v[152:155]
ds_read_b64_tr_b16 a[132:133], v88 offset:33024
ds_read_b64_tr_b16 a[134:135], v88 offset:41216
v_mfma_f32_16x16x32_bf16 v[156:159], a[136:139], v[36:39], v[156:159]
buffer_load_dwordx2 a[116:117], v238, s[64:67], s93 offen offset:0 sc1
v_mfma_f32_16x16x32_bf16 v[160:163], a[140:143], v[32:35], v[160:163]
ds_read_b64_tr_b16 a[136:137], v89 offset:33024
v_add_u32 v238, 28672, v238
v_mfma_f32_16x16x32_bf16 v[164:167], a[140:143], v[36:39], v[164:167]
s_waitcnt lgkmcnt(5)
ds_read_b64_tr_b16 a[138:139], v89 offset:41216
v_exp_f32 v54, v54
v_mfma_f32_16x16x32_bf16 v[168:171], a[144:147], v[32:35], v[168:171]
v_exp_f32 v55, v55
v_mfma_f32_16x16x32_bf16 v[172:175], a[144:147], v[36:39], v[172:175]
buffer_load_dwordx4 v91, s[60:63], s71 offen offset:1024 lds
v_mfma_f32_16x16x32_bf16 v[176:179], a[148:151], v[32:35], v[176:179]
ds_read_b64_tr_b16 a[144:145], v82 offset:49152
ds_read_b64_tr_b16 a[146:147], v82 offset:57344
v_mfma_f32_16x16x32_bf16 v[180:183], a[148:151], v[36:39], v[180:183]
buffer_load_dwordx4 a[96:99], v238, s[64:67], s92 offen offset:0 sc1
v_mfma_f32_16x16x32_bf16 v[184:187], a[152:155], v[32:35], v[184:187]
ds_read_b64_tr_b16 a[148:149], v83 offset:49152
v_exp_f32 v56, v56
v_mfma_f32_16x16x32_bf16 v[188:191], a[152:155], v[36:39], v[188:191]
ds_read_b64_tr_b16 a[150:151], v83 offset:57344
v_exp_f32 v57, v57
v_mfma_f32_16x16x32_bf16 v[192:195], a[156:159], v[32:35], v[192:195]
ds_read_b64_tr_b16 a[152:153], v84 offset:49152
ds_read_b64_tr_b16 a[154:155], v84 offset:57344
v_mfma_f32_16x16x32_bf16 v[196:199], a[156:159], v[36:39], v[196:199]
s_waitcnt lgkmcnt(6)
buffer_load_dwordx4 v92, s[60:63], s71 offen offset:2048 lds
v_mfma_f32_16x16x32_bf16 v[200:203], a[128:131], v[32:35], v[200:203]
ds_read_b64_tr_b16 a[156:157], v85 offset:49152
ds_read_b64_tr_b16 a[158:159], v85 offset:57344
v_mfma_f32_16x16x32_bf16 v[204:207], a[128:131], v[36:39], v[204:207]
buffer_load_dwordx4 a[100:103], v238, s[64:67], s92 offen offset:1024 sc1
v_mfma_f32_16x16x32_bf16 v[208:211], a[132:135], v[32:35], v[208:211]
ds_read_b64_tr_b16 a[128:129], v86 offset:49152
v_exp_f32 v60, v60
v_exp_f32 v61, v61
v_exp_f32 v58, v58
v_exp_f32 v59, v59
v_mfma_f32_16x16x32_bf16 v[212:215], a[132:135], v[36:39], v[212:215]
ds_read_b64_tr_b16 a[130:131], v86 offset:57344
v_exp_f32 v62, v62
v_exp_f32 v63, v63
v_cvt_pk_bf16_f32 v48, v48, v49
v_cvt_pk_bf16_f32 v52, v52, v53
v_cvt_pk_bf16_f32 v49, v50, v51
v_cvt_pk_bf16_f32 v53, v54, v55
v_cvt_pk_bf16_f32 v50, v56, v57
v_cvt_pk_bf16_f32 v54, v60, v61
v_cvt_pk_bf16_f32 v51, v58, v59
v_mfma_f32_16x16x32_bf16 v[216:219], a[136:139], v[32:35], v[216:219]
ds_read_b64_tr_b16 a[132:133], v87 offset:49152
ds_read_b64_tr_b16 a[134:135], v87 offset:57344
v_cvt_pk_bf16_f32 v55, v62, v63
v_mfma_f32_16x16x32_bf16 v[220:223], a[136:139], v[36:39], v[220:223]
buffer_load_dwordx4 v93, s[60:63], s71 offen offset:3072 lds
v_mfma_f32_16x16x32_bf16 a[192:195], a[200:203], v[48:51], a[192:195]
ds_read_b64_tr_b16 a[136:137], v88 offset:49152
ds_read_b64_tr_b16 a[138:139], v88 offset:57344
v_mfma_f32_16x16x32_bf16 a[196:199], a[200:203], v[52:55], a[196:199]
s_waitcnt lgkmcnt(6)
buffer_load_dwordx4 a[112:115], v238, s[64:67], s92 offen offset:2048 sc1
v_mfma_f32_16x16x32_bf16 v[96:99], a[144:147], v[48:51], v[96:99]
ds_read_b64_tr_b16 a[140:141], v89 offset:49152
v_mfma_f32_16x16x32_bf16 v[100:103], a[144:147], v[52:55], v[100:103]
ds_read_b64_tr_b16 a[142:143], v89 offset:57344
v_mfma_f32_16x16x32_bf16 v[104:107], a[148:151], v[48:51], v[104:107]
ds_read_b64_tr_b16 a[144:145], v82 offset:49408
ds_read_b64_tr_b16 a[146:147], v82 offset:57600
v_mfma_f32_16x16x32_bf16 v[108:111], a[148:151], v[52:55], v[108:111]
buffer_load_dwordx2 a[118:119], v238, s[64:67], s93 offen offset:0 sc1
v_mfma_f32_16x16x32_bf16 v[112:115], a[152:155], v[48:51], v[112:115]
ds_read_b64_tr_b16 a[148:149], v83 offset:49408
v_add_u32 v238, 28672, v238
v_mfma_f32_16x16x32_bf16 v[116:119], a[152:155], v[52:55], v[116:119]
ds_read_b64_tr_b16 a[150:151], v83 offset:57600
v_accvgpr_read_b32 v78, a192
v_mfma_f32_16x16x32_bf16 v[120:123], a[156:159], v[48:51], v[120:123]
ds_read_b64_tr_b16 a[152:153], v84 offset:49408
ds_read_b64_tr_b16 a[154:155], v84 offset:57600
v_mfma_f32_16x16x32_bf16 v[124:127], a[156:159], v[52:55], v[124:127]
s_waitcnt lgkmcnt(6)
buffer_load_dwordx4 v[0:3], v238, s[64:67], s92 offen offset:0 sc1
v_accvgpr_read_b32 v79, a196
v_mfma_f32_16x16x32_bf16 v[128:131], a[128:131], v[48:51], v[128:131]
ds_read_b64_tr_b16 a[156:157], v85 offset:49408
v_mfma_f32_16x16x32_bf16 v[132:135], a[128:131], v[52:55], v[132:135]
ds_read_b64_tr_b16 a[158:159], v85 offset:57600
v_rcp_f32 v241, v78
v_rcp_f32 v242, v79
v_mfma_f32_16x16x32_bf16 v[136:139], a[132:135], v[48:51], v[136:139]
ds_read_b64_tr_b16 a[128:129], v86 offset:49408
ds_read_b64_tr_b16 a[130:131], v86 offset:57600
v_mfma_f32_16x16x32_bf16 v[140:143], a[132:135], v[52:55], v[140:143]
buffer_load_dwordx4 v[4:7], v238, s[64:67], s92 offen offset:1024 sc1
v_mfma_f32_16x16x32_bf16 v[144:147], a[136:139], v[48:51], v[144:147]
ds_read_b64_tr_b16 a[132:133], v87 offset:49408
v_mul_f32 v96, v96, v241
v_mfma_f32_16x16x32_bf16 v[148:151], a[136:139], v[52:55], v[148:151]
ds_read_b64_tr_b16 a[134:135], v87 offset:57600
v_mul_f32 v97, v97, v241
v_mfma_f32_16x16x32_bf16 v[152:155], a[140:143], v[48:51], v[152:155]
ds_read_b64_tr_b16 a[136:137], v88 offset:49408
ds_read_b64_tr_b16 a[138:139], v88 offset:57600
v_mfma_f32_16x16x32_bf16 v[156:159], a[140:143], v[52:55], v[156:159]
s_waitcnt lgkmcnt(6)
buffer_load_dwordx4 v[8:11], v238, s[64:67], s92 offen offset:2048 sc1
v_mfma_f32_16x16x32_bf16 v[160:163], a[144:147], v[48:51], v[160:163]
ds_read_b64_tr_b16 a[140:141], v89 offset:49408
v_cvt_pk_bf16_f32 v96, v96, v97
v_mfma_f32_16x16x32_bf16 v[164:167], a[144:147], v[52:55], v[164:167]
s_waitcnt vmcnt(40)
ds_read_b64_tr_b16 a[142:143], v89 offset:57600
v_mul_f32 v98, v98, v241
v_mfma_f32_16x16x32_bf16 v[168:171], a[148:151], v[48:51], v[168:171]
s_barrier
ds_read_b64_tr_b16 a[144:145], v224 offset:0
ds_read_b64_tr_b16 a[146:147], v224 offset:2048
v_mfma_f32_16x16x32_bf16 v[172:175], a[148:151], v[52:55], v[172:175]
buffer_load_dwordx2 v[24:25], v238, s[64:67], s93 offen offset:0 sc1
v_mfma_f32_16x16x32_bf16 v[176:179], a[152:155], v[48:51], v[176:179]
ds_read_b64_tr_b16 a[148:149], v225 offset:0
v_add_u32 v238, 28672, v238
v_mfma_f32_16x16x32_bf16 v[180:183], a[152:155], v[52:55], v[180:183]
s_add_u32 s71, s76, 384
ds_read_b64_tr_b16 a[150:151], v225 offset:2048
v_mul_f32 v99, v99, v241
v_mfma_f32_16x16x32_bf16 v[184:187], a[156:159], v[48:51], v[184:187]
s_add_u32 m0, s77, 65536
ds_read_b64_tr_b16 a[152:153], v226 offset:0
ds_read_b64_tr_b16 a[154:155], v226 offset:2048
v_mfma_f32_16x16x32_bf16 v[188:191], a[156:159], v[52:55], v[188:191]
s_waitcnt lgkmcnt(6)
buffer_load_dwordx4 v90, s[60:63], s71 offen offset:0 lds
v_mfma_f32_16x16x32_bf16 v[192:195], a[128:131], v[48:51], v[192:195]
ds_read_b64_tr_b16 a[156:157], v227 offset:0
ds_read_b64_tr_b16 a[158:159], v227 offset:2048
v_mfma_f32_16x16x32_bf16 v[196:199], a[128:131], v[52:55], v[196:199]
buffer_load_dwordx4 v[12:15], v238, s[64:67], s92 offen offset:0 sc1
v_mfma_f32_16x16x32_bf16 v[200:203], a[132:135], v[48:51], v[200:203]
ds_read_b64_tr_b16 a[128:129], v224 offset:4096
v_cvt_pk_bf16_f32 v97, v98, v99
v_mfma_f32_16x16x32_bf16 v[204:207], a[132:135], v[52:55], v[204:207]
ds_read_b64_tr_b16 a[130:131], v224 offset:6144
v_mul_f32 v104, v104, v241
v_mfma_f32_16x16x32_bf16 v[208:211], a[136:139], v[48:51], v[208:211]
ds_read_b64_tr_b16 a[132:133], v225 offset:4096
ds_read_b64_tr_b16 a[134:135], v225 offset:6144
v_mfma_f32_16x16x32_bf16 v[212:215], a[136:139], v[52:55], v[212:215]
buffer_load_dwordx4 v91, s[60:63], s71 offen offset:1024 lds
v_mul_f32 v105, v105, v241
v_cvt_pk_bf16_f32 v98, v104, v105
v_mul_f32 v106, v106, v241
v_mul_f32 v107, v107, v241
v_cvt_pk_bf16_f32 v99, v106, v107
v_mfma_f32_16x16x32_bf16 v[216:219], a[140:143], v[48:51], v[216:219]
ds_read_b64_tr_b16 a[136:137], v226 offset:4096
ds_read_b64_tr_b16 a[138:139], v226 offset:6144
v_mul_f32 v100, v100, v242
v_mul_f32 v101, v101, v242
v_cvt_pk_bf16_f32 v100, v100, v101
v_mul_f32 v102, v102, v242
v_mul_f32 v103, v103, v242
v_cvt_pk_bf16_f32 v101, v102, v103
v_mul_f32 v108, v108, v242
v_mul_f32 v109, v109, v242
v_cvt_pk_bf16_f32 v102, v108, v109
v_mul_f32 v110, v110, v242
v_mul_f32 v111, v111, v242
v_cvt_pk_bf16_f32 v103, v110, v111
v_mfma_f32_16x16x32_bf16 v[220:223], a[140:143], v[52:55], v[220:223]
s_waitcnt lgkmcnt(6)
buffer_load_dwordx4 v[16:19], v238, s[64:67], s92 offen offset:1024 sc1
v_mfma_f32_16x16x32_bf16 v[32:35], a[144:147], v[96:99], 0
s_add_u32 s81, s80, 0
ds_read_b64_tr_b16 a[140:141], v227 offset:4096
v_mul_f32 v112, v112, v241
v_mfma_f32_16x16x32_bf16 v[36:39], a[144:147], v[100:103], 0
ds_read_b64_tr_b16 a[142:143], v227 offset:6144
v_mul_f32 v113, v113, v241
v_mfma_f32_16x16x32_bf16 v[40:43], a[148:151], v[96:99], 0
ds_read_b64_tr_b16 a[144:145], v224 offset:8192
ds_read_b64_tr_b16 a[146:147], v224 offset:10240
v_mfma_f32_16x16x32_bf16 v[44:47], a[148:151], v[100:103], 0
buffer_load_dwordx4 v92, s[60:63], s71 offen offset:2048 lds
v_mfma_f32_16x16x32_bf16 v[48:51], a[152:155], v[96:99], 0
ds_read_b64_tr_b16 a[148:149], v225 offset:8192
ds_read_b64_tr_b16 a[150:151], v225 offset:10240
v_mfma_f32_16x16x32_bf16 v[52:55], a[152:155], v[100:103], 0
buffer_load_dwordx4 v[20:23], v238, s[64:67], s92 offen offset:2048 sc1
v_cvt_pk_bf16_f32 v112, v112, v113
v_mul_f32 v114, v114, v241
v_mul_f32 v115, v115, v241
v_cvt_pk_bf16_f32 v113, v114, v115
v_mul_f32 v120, v120, v241
v_mul_f32 v121, v121, v241
v_cvt_pk_bf16_f32 v114, v120, v121
v_mul_f32 v122, v122, v241
v_mul_f32 v123, v123, v241
v_cvt_pk_bf16_f32 v115, v122, v123
v_mfma_f32_16x16x32_bf16 v[56:59], a[156:159], v[96:99], 0
ds_read_b64_tr_b16 a[152:153], v226 offset:8192
v_mul_f32 v116, v116, v242
v_mul_f32 v117, v117, v242
v_cvt_pk_bf16_f32 v116, v116, v117
v_mul_f32 v118, v118, v242
v_mul_f32 v119, v119, v242
v_cvt_pk_bf16_f32 v117, v118, v119
v_mul_f32 v124, v124, v242
v_mul_f32 v125, v125, v242
v_cvt_pk_bf16_f32 v118, v124, v125
v_mul_f32 v126, v126, v242
v_mul_f32 v127, v127, v242
v_cvt_pk_bf16_f32 v119, v126, v127
v_mfma_f32_16x16x32_bf16 v[60:63], a[156:159], v[100:103], 0
s_waitcnt lgkmcnt(5)
ds_read_b64_tr_b16 a[154:155], v226 offset:10240
v_mul_f32 v128, v128, v241
v_mfma_f32_16x16x32_bf16 v[32:35], a[128:131], v[112:115], v[32:35]
ds_read_b64_tr_b16 a[156:157], v227 offset:8192
ds_read_b64_tr_b16 a[158:159], v227 offset:10240
v_mfma_f32_16x16x32_bf16 v[36:39], a[128:131], v[116:119], v[36:39]
buffer_load_dwordx4 v93, s[60:63], s71 offen offset:3072 lds
v_mfma_f32_16x16x32_bf16 v[40:43], a[132:135], v[112:115], v[40:43]
ds_read_b64_tr_b16 a[128:129], v224 offset:12288
ds_read_b64_tr_b16 a[130:131], v224 offset:14336
v_mfma_f32_16x16x32_bf16 v[44:47], a[132:135], v[116:119], v[44:47]
buffer_load_dwordx2 v[26:27], v238, s[64:67], s93 offen offset:0 sc1
v_mfma_f32_16x16x32_bf16 v[48:51], a[136:139], v[112:115], v[48:51]
ds_read_b64_tr_b16 a[132:133], v225 offset:12288
v_mul_f32 v129, v129, v241
v_mfma_f32_16x16x32_bf16 v[52:55], a[136:139], v[116:119], v[52:55]
s_add_u32 s71, s76, 29056
ds_read_b64_tr_b16 a[134:135], v225 offset:14336
v_cvt_pk_bf16_f32 v128, v128, v129
v_mul_f32 v130, v130, v241
v_mul_f32 v131, v131, v241
v_cvt_pk_bf16_f32 v129, v130, v131
v_mul_f32 v136, v136, v241
v_mul_f32 v137, v137, v241
v_cvt_pk_bf16_f32 v130, v136, v137
v_mul_f32 v138, v138, v241
v_mul_f32 v139, v139, v241
v_cvt_pk_bf16_f32 v131, v138, v139
v_mfma_f32_16x16x32_bf16 v[56:59], a[140:143], v[112:115], v[56:59]
s_add_u32 m0, s77, 69632
ds_read_b64_tr_b16 a[136:137], v226 offset:12288
ds_read_b64_tr_b16 a[138:139], v226 offset:14336
v_mul_f32 v132, v132, v242
v_mul_f32 v133, v133, v242
v_cvt_pk_bf16_f32 v132, v132, v133
v_mul_f32 v134, v134, v242
v_mul_f32 v135, v135, v242
v_cvt_pk_bf16_f32 v133, v134, v135
v_mul_f32 v140, v140, v242
v_mul_f32 v141, v141, v242
v_cvt_pk_bf16_f32 v134, v140, v141
v_mul_f32 v142, v142, v242
v_mul_f32 v143, v143, v242
v_cvt_pk_bf16_f32 v135, v142, v143
v_mfma_f32_16x16x32_bf16 v[60:63], a[140:143], v[116:119], v[60:63]
s_waitcnt lgkmcnt(6)
buffer_load_dwordx4 v90, s[60:63], s71 offen offset:0 lds
v_mfma_f32_16x16x32_bf16 v[32:35], a[144:147], v[128:131], v[32:35]
ds_read_b64_tr_b16 a[140:141], v227 offset:12288
v_mul_f32 v144, v144, v241
v_mfma_f32_16x16x32_bf16 v[36:39], a[144:147], v[132:135], v[36:39]
ds_read_b64_tr_b16 a[142:143], v227 offset:14336
v_mul_f32 v145, v145, v241
v_mfma_f32_16x16x32_bf16 v[40:43], a[148:151], v[128:131], v[40:43]
ds_read_b64_tr_b16 a[144:145], v224 offset:16384
v_cvt_pk_bf16_f32 v144, v144, v145
v_mfma_f32_16x16x32_bf16 v[44:47], a[148:151], v[132:135], v[44:47]
ds_read_b64_tr_b16 a[146:147], v224 offset:18432
v_mul_f32 v146, v146, v241
v_mfma_f32_16x16x32_bf16 v[48:51], a[152:155], v[128:131], v[48:51]
ds_read_b64_tr_b16 a[148:149], v225 offset:16384
ds_read_b64_tr_b16 a[150:151], v225 offset:18432
v_mfma_f32_16x16x32_bf16 v[52:55], a[152:155], v[132:135], v[52:55]
buffer_load_dwordx4 v91, s[60:63], s71 offen offset:1024 lds
v_mul_f32 v147, v147, v241
v_cvt_pk_bf16_f32 v145, v146, v147
v_mul_f32 v152, v152, v241
v_mul_f32 v153, v153, v241
v_cvt_pk_bf16_f32 v146, v152, v153
v_mul_f32 v154, v154, v241
v_mul_f32 v155, v155, v241
v_cvt_pk_bf16_f32 v147, v154, v155
v_mfma_f32_16x16x32_bf16 v[56:59], a[156:159], v[128:131], v[56:59]
ds_read_b64_tr_b16 a[152:153], v226 offset:16384
v_mul_f32 v148, v148, v242
v_mul_f32 v149, v149, v242
v_cvt_pk_bf16_f32 v148, v148, v149
v_mul_f32 v150, v150, v242
v_mul_f32 v151, v151, v242
v_cvt_pk_bf16_f32 v149, v150, v151
v_mul_f32 v156, v156, v242
v_mul_f32 v157, v157, v242
v_cvt_pk_bf16_f32 v150, v156, v157
v_mul_f32 v158, v158, v242
v_mul_f32 v159, v159, v242
v_cvt_pk_bf16_f32 v151, v158, v159
v_mfma_f32_16x16x32_bf16 v[60:63], a[156:159], v[132:135], v[60:63]
s_waitcnt lgkmcnt(5)
ds_read_b64_tr_b16 a[154:155], v226 offset:18432
v_mul_f32 v160, v160, v241
v_mfma_f32_16x16x32_bf16 v[32:35], a[128:131], v[144:147], v[32:35]
ds_read_b64_tr_b16 a[156:157], v227 offset:16384
v_mul_f32 v161, v161, v241
v_mfma_f32_16x16x32_bf16 v[36:39], a[128:131], v[148:151], v[36:39]
ds_read_b64_tr_b16 a[158:159], v227 offset:18432
v_cvt_pk_bf16_f32 v160, v160, v161
v_mfma_f32_16x16x32_bf16 v[40:43], a[132:135], v[144:147], v[40:43]
ds_read_b64_tr_b16 a[128:129], v224 offset:20480
ds_read_b64_tr_b16 a[130:131], v224 offset:22528
v_mfma_f32_16x16x32_bf16 v[44:47], a[132:135], v[148:151], v[44:47]
buffer_load_dwordx4 v92, s[60:63], s71 offen offset:2048 lds
v_mfma_f32_16x16x32_bf16 v[48:51], a[136:139], v[144:147], v[48:51]
ds_read_b64_tr_b16 a[132:133], v225 offset:20480
v_mul_f32 v162, v162, v241
v_mfma_f32_16x16x32_bf16 v[52:55], a[136:139], v[148:151], v[52:55]
ds_read_b64_tr_b16 a[134:135], v225 offset:22528
v_mul_f32 v163, v163, v241
v_cvt_pk_bf16_f32 v161, v162, v163
v_mul_f32 v168, v168, v241
v_mul_f32 v169, v169, v241
v_cvt_pk_bf16_f32 v162, v168, v169
v_mul_f32 v170, v170, v241
v_mul_f32 v171, v171, v241
v_cvt_pk_bf16_f32 v163, v170, v171
v_mfma_f32_16x16x32_bf16 v[56:59], a[140:143], v[144:147], v[56:59]
ds_read_b64_tr_b16 a[136:137], v226 offset:20480
v_mul_f32 v164, v164, v242
v_mul_f32 v165, v165, v242
v_cvt_pk_bf16_f32 v164, v164, v165
v_mul_f32 v166, v166, v242
v_mul_f32 v167, v167, v242
v_cvt_pk_bf16_f32 v165, v166, v167
v_mul_f32 v172, v172, v242
v_mul_f32 v173, v173, v242
v_cvt_pk_bf16_f32 v166, v172, v173
v_mul_f32 v174, v174, v242
v_mul_f32 v175, v175, v242
v_cvt_pk_bf16_f32 v167, v174, v175
v_mfma_f32_16x16x32_bf16 v[60:63], a[140:143], v[148:151], v[60:63]
s_waitcnt lgkmcnt(5)
ds_read_b64_tr_b16 a[138:139], v226 offset:22528
v_mul_f32 v176, v176, v241
v_mfma_f32_16x16x32_bf16 v[32:35], a[144:147], v[160:163], v[32:35]
ds_read_b64_tr_b16 a[140:141], v227 offset:20480
ds_read_b64_tr_b16 a[142:143], v227 offset:22528
v_mfma_f32_16x16x32_bf16 v[36:39], a[144:147], v[164:167], v[36:39]
buffer_load_dwordx4 v93, s[60:63], s71 offen offset:3072 lds
v_mfma_f32_16x16x32_bf16 v[40:43], a[148:151], v[160:163], v[40:43]
ds_read_b64_tr_b16 a[144:145], v224 offset:24576
v_mul_f32 v177, v177, v241
v_mfma_f32_16x16x32_bf16 v[44:47], a[148:151], v[164:167], v[44:47]
ds_read_b64_tr_b16 a[146:147], v224 offset:26624
v_cvt_pk_bf16_f32 v176, v176, v177
v_mfma_f32_16x16x32_bf16 v[48:51], a[152:155], v[160:163], v[48:51]
ds_read_b64_tr_b16 a[148:149], v225 offset:24576
v_mul_f32 v178, v178, v241
v_mfma_f32_16x16x32_bf16 v[52:55], a[152:155], v[164:167], v[52:55]
ds_read_b64_tr_b16 a[150:151], v225 offset:26624
v_mul_f32 v179, v179, v241
v_cvt_pk_bf16_f32 v177, v178, v179
v_mul_f32 v184, v184, v241
v_mul_f32 v185, v185, v241
v_cvt_pk_bf16_f32 v178, v184, v185
v_mul_f32 v186, v186, v241
v_mul_f32 v187, v187, v241
v_cvt_pk_bf16_f32 v179, v186, v187
v_mfma_f32_16x16x32_bf16 v[56:59], a[156:159], v[160:163], v[56:59]
ds_read_b64_tr_b16 a[152:153], v226 offset:24576
v_mul_f32 v180, v180, v242
v_mul_f32 v181, v181, v242
v_cvt_pk_bf16_f32 v180, v180, v181
v_mul_f32 v182, v182, v242
v_mul_f32 v183, v183, v242
v_cvt_pk_bf16_f32 v181, v182, v183
v_mul_f32 v188, v188, v242
v_mul_f32 v189, v189, v242
v_cvt_pk_bf16_f32 v182, v188, v189
v_mul_f32 v190, v190, v242
v_mul_f32 v191, v191, v242
v_cvt_pk_bf16_f32 v183, v190, v191
v_mfma_f32_16x16x32_bf16 v[60:63], a[156:159], v[164:167], v[60:63]
s_waitcnt lgkmcnt(5)
ds_read_b64_tr_b16 a[154:155], v226 offset:26624
v_mul_f32 v192, v192, v241
v_mfma_f32_16x16x32_bf16 v[32:35], a[128:131], v[176:179], v[32:35]
ds_read_b64_tr_b16 a[156:157], v227 offset:24576
v_mul_f32 v193, v193, v241
v_mfma_f32_16x16x32_bf16 v[36:39], a[128:131], v[180:183], v[36:39]
ds_read_b64_tr_b16 a[158:159], v227 offset:26624
v_cvt_pk_bf16_f32 v192, v192, v193
v_mfma_f32_16x16x32_bf16 v[40:43], a[132:135], v[176:179], v[40:43]
ds_read_b64_tr_b16 a[128:129], v224 offset:28672
v_mul_f32 v194, v194, v241
v_mfma_f32_16x16x32_bf16 v[44:47], a[132:135], v[180:183], v[44:47]
ds_read_b64_tr_b16 a[130:131], v224 offset:30720
v_mul_f32 v195, v195, v241
v_mfma_f32_16x16x32_bf16 v[48:51], a[136:139], v[176:179], v[48:51]
ds_read_b64_tr_b16 a[132:133], v225 offset:28672
v_cvt_pk_bf16_f32 v193, v194, v195
v_mfma_f32_16x16x32_bf16 v[52:55], a[136:139], v[180:183], v[52:55]
ds_read_b64_tr_b16 a[134:135], v225 offset:30720
v_mul_f32 v200, v200, v241
v_mul_f32 v201, v201, v241
v_cvt_pk_bf16_f32 v194, v200, v201
v_mul_f32 v202, v202, v241
v_mul_f32 v203, v203, v241
v_cvt_pk_bf16_f32 v195, v202, v203
v_mfma_f32_16x16x32_bf16 v[56:59], a[140:143], v[176:179], v[56:59]
ds_read_b64_tr_b16 a[136:137], v226 offset:28672
v_mul_f32 v196, v196, v242
v_mul_f32 v197, v197, v242
v_cvt_pk_bf16_f32 v196, v196, v197
v_mul_f32 v198, v198, v242
v_mul_f32 v199, v199, v242
v_cvt_pk_bf16_f32 v197, v198, v199
v_mul_f32 v204, v204, v242
v_mul_f32 v205, v205, v242
v_cvt_pk_bf16_f32 v198, v204, v205
v_mul_f32 v206, v206, v242
v_mul_f32 v207, v207, v242
v_cvt_pk_bf16_f32 v199, v206, v207
v_mfma_f32_16x16x32_bf16 v[60:63], a[140:143], v[180:183], v[60:63]
s_waitcnt lgkmcnt(5)
ds_read_b64_tr_b16 a[138:139], v226 offset:30720
v_add_u32 v224, 0xfffe0000, v224
v_mfma_f32_16x16x32_bf16 v[32:35], a[144:147], v[192:195], v[32:35]
ds_read_b64_tr_b16 a[140:141], v227 offset:28672
v_add_u32 v225, 0xfffe0000, v225
v_mfma_f32_16x16x32_bf16 v[36:39], a[144:147], v[196:199], v[36:39]
s_waitcnt vmcnt(39)
ds_read_b64_tr_b16 a[142:143], v227 offset:30720
v_mul_f32 v208, v208, v241
v_mfma_f32_16x16x32_bf16 v[40:43], a[148:151], v[192:195], v[40:43]
s_barrier
ds_read_b64_tr_b16 a[144:145], v224 offset:0
v_mul_f32 v209, v209, v241
v_mfma_f32_16x16x32_bf16 v[44:47], a[148:151], v[196:199], v[44:47]
ds_read_b64_tr_b16 a[146:147], v224 offset:2048
v_cvt_pk_bf16_f32 v208, v208, v209
v_mfma_f32_16x16x32_bf16 v[48:51], a[152:155], v[192:195], v[48:51]
ds_read_b64_tr_b16 a[148:149], v225 offset:0
v_mul_f32 v210, v210, v241
v_mfma_f32_16x16x32_bf16 v[52:55], a[152:155], v[196:199], v[52:55]
s_add_u32 s71, s76, 512
ds_read_b64_tr_b16 a[150:151], v225 offset:2048
v_mul_f32 v211, v211, v241
v_cvt_pk_bf16_f32 v209, v210, v211
v_mul_f32 v216, v216, v241
v_mul_f32 v217, v217, v241
v_cvt_pk_bf16_f32 v210, v216, v217
v_mul_f32 v218, v218, v241
v_mul_f32 v219, v219, v241
v_cvt_pk_bf16_f32 v211, v218, v219
v_mfma_f32_16x16x32_bf16 v[56:59], a[156:159], v[192:195], v[56:59]
s_add_u32 m0, s77, 98304
ds_read_b64_tr_b16 a[152:153], v224 offset:4096
ds_read_b64_tr_b16 a[154:155], v224 offset:6144
v_mul_f32 v212, v212, v242
v_mul_f32 v213, v213, v242
v_cvt_pk_bf16_f32 v212, v212, v213
v_mul_f32 v214, v214, v242
v_mul_f32 v215, v215, v242
v_cvt_pk_bf16_f32 v213, v214, v215
v_mul_f32 v220, v220, v242
v_mul_f32 v221, v221, v242
v_cvt_pk_bf16_f32 v214, v220, v221
v_mul_f32 v222, v222, v242
v_mul_f32 v223, v223, v242
v_cvt_pk_bf16_f32 v215, v222, v223
v_mfma_f32_16x16x32_bf16 v[60:63], a[156:159], v[196:199], v[60:63]
s_waitcnt lgkmcnt(6)
buffer_load_dwordx4 v90, s[60:63], s71 offen offset:0 lds
v_mfma_f32_16x16x32_bf16 v[32:35], a[128:131], v[208:211], v[32:35]
ds_read_b64_tr_b16 a[156:157], v225 offset:4096
v_add_u32 v226, 0xfffe0000, v226
v_mfma_f32_16x16x32_bf16 v[36:39], a[128:131], v[212:215], v[36:39]
ds_read_b64_tr_b16 a[158:159], v225 offset:6144
v_add_u32 v227, 0xfffe0000, v227
v_mfma_f32_16x16x32_bf16 v[40:43], a[132:135], v[208:211], v[40:43]
ds_read_b64_tr_b16 a[128:129], v224 offset:8192
v_mfma_f32_16x16x32_bf16 v[44:47], a[132:135], v[212:215], v[44:47]
ds_read_b64_tr_b16 a[130:131], v224 offset:10240
v_mfma_f32_16x16x32_bf16 v[48:51], a[136:139], v[208:211], v[48:51]
ds_read_b64_tr_b16 a[132:133], v225 offset:8192
ds_read_b64_tr_b16 a[134:135], v225 offset:10240
v_mfma_f32_16x16x32_bf16 v[52:55], a[136:139], v[212:215], v[52:55]
buffer_load_dwordx4 v91, s[60:63], s71 offen offset:1024 lds
v_mfma_f32_16x16x32_bf16 v[56:59], a[140:143], v[208:211], v[56:59]
ds_read_b64_tr_b16 a[136:137], v224 offset:12288
v_mfma_f32_16x16x32_bf16 v[60:63], a[140:143], v[212:215], v[60:63]
s_waitcnt lgkmcnt(5)
ds_read_b64_tr_b16 a[138:139], v224 offset:14336
v_cvt_pk_bf16_f32 v32, v32, v33
v_mfma_f32_16x16x32_bf16 v[64:67], a[144:147], v[96:99], 0
s_mov_b64 s[40:41], s[22:23]
ds_read_b64_tr_b16 a[140:141], v225 offset:12288
v_cvt_pk_bf16_f32 v33, v34, v35
v_mfma_f32_16x16x32_bf16 v[68:71], a[144:147], v[100:103], 0
s_and_b32 s41, s41, 0xffff
ds_read_b64_tr_b16 a[142:143], v225 offset:14336
v_cvt_pk_bf16_f32 v34, v40, v41
v_mfma_f32_16x16x32_bf16 v[72:75], a[148:151], v[96:99], 0
s_mov_b32 s42, 0x1c0000
ds_read_b64_tr_b16 a[144:145], v224 offset:16384
ds_read_b64_tr_b16 a[146:147], v224 offset:18432
v_mfma_f32_16x16x32_bf16 v[76:79], a[148:151], v[100:103], 0
buffer_load_dwordx4 v92, s[60:63], s71 offen offset:2048 lds
v_mfma_f32_16x16x32_bf16 v[64:67], a[152:155], v[112:115], v[64:67]
ds_read_b64_tr_b16 a[148:149], v225 offset:16384
v_cvt_pk_bf16_f32 v35, v42, v43
v_mfma_f32_16x16x32_bf16 v[68:71], a[152:155], v[116:119], v[68:71]
ds_read_b64_tr_b16 a[150:151], v225 offset:18432
buffer_store_dwordx4 v[32:35], v252, s[64:67], s81 offen offset:0 sc1
v_mfma_f32_16x16x32_bf16 v[72:75], a[156:159], v[112:115], v[72:75]
ds_read_b64_tr_b16 a[152:153], v224 offset:20480
v_cvt_pk_bf16_f32 v36, v36, v37
v_mfma_f32_16x16x32_bf16 v[76:79], a[156:159], v[116:119], v[76:79]
s_waitcnt lgkmcnt(5)
ds_read_b64_tr_b16 a[154:155], v224 offset:22528
v_cvt_pk_bf16_f32 v37, v38, v39
v_mfma_f32_16x16x32_bf16 v[64:67], a[128:131], v[128:131], v[64:67]
ds_read_b64_tr_b16 a[156:157], v225 offset:20480
ds_read_b64_tr_b16 a[158:159], v225 offset:22528
v_mfma_f32_16x16x32_bf16 v[68:71], a[128:131], v[132:135], v[68:71]
buffer_load_dwordx4 v93, s[60:63], s71 offen offset:3072 lds
v_mfma_f32_16x16x32_bf16 v[72:75], a[132:135], v[128:131], v[72:75]
ds_read_b64_tr_b16 a[128:129], v224 offset:24576
v_cvt_pk_bf16_f32 v38, v44, v45
v_mfma_f32_16x16x32_bf16 v[76:79], a[132:135], v[132:135], v[76:79]
ds_read_b64_tr_b16 a[130:131], v224 offset:26624
v_cvt_pk_bf16_f32 v39, v46, v47
v_mfma_f32_16x16x32_bf16 v[64:67], a[136:139], v[144:147], v[64:67]
ds_read_b64_tr_b16 a[132:133], v225 offset:24576
buffer_store_dwordx4 v[36:39], v253, s[64:67], s81 offen offset:0 sc1
v_mfma_f32_16x16x32_bf16 v[68:71], a[136:139], v[148:151], v[68:71]
s_add_u32 s71, s76, 29184
ds_read_b64_tr_b16 a[134:135], v225 offset:26624
v_cvt_pk_bf16_f32 v48, v48, v49
v_mfma_f32_16x16x32_bf16 v[72:75], a[140:143], v[144:147], v[72:75]
s_add_u32 m0, s77, 102400
ds_read_b64_tr_b16 a[136:137], v224 offset:28672
ds_read_b64_tr_b16 a[138:139], v224 offset:30720
v_mfma_f32_16x16x32_bf16 v[76:79], a[140:143], v[148:151], v[76:79]
s_waitcnt lgkmcnt(6)
buffer_load_dwordx4 v90, s[60:63], s71 offen offset:0 lds
v_mfma_f32_16x16x32_bf16 v[64:67], a[144:147], v[160:163], v[64:67]
ds_read_b64_tr_b16 a[140:141], v225 offset:28672
v_cvt_pk_bf16_f32 v49, v50, v51
v_mfma_f32_16x16x32_bf16 v[68:71], a[144:147], v[164:167], v[68:71]
ds_read_b64_tr_b16 a[142:143], v225 offset:30720
v_cvt_pk_bf16_f32 v50, v56, v57
v_mfma_f32_16x16x32_bf16 v[72:75], a[148:151], v[160:163], v[72:75]
ds_read_b64_tr_b16 a[144:145], v226 offset:0
v_cvt_pk_bf16_f32 v51, v58, v59
v_mfma_f32_16x16x32_bf16 v[76:79], a[148:151], v[164:167], v[76:79]
ds_read_b64_tr_b16 a[146:147], v226 offset:2048
buffer_store_dwordx4 v[48:51], v252, s[64:67], s81 offen offset:1024 sc1
v_mfma_f32_16x16x32_bf16 v[64:67], a[152:155], v[176:179], v[64:67]
ds_read_b64_tr_b16 a[148:149], v227 offset:0
ds_read_b64_tr_b16 a[150:151], v227 offset:2048
v_mfma_f32_16x16x32_bf16 v[68:71], a[152:155], v[180:183], v[68:71]
buffer_load_dwordx4 v91, s[60:63], s71 offen offset:1024 lds
v_mfma_f32_16x16x32_bf16 v[72:75], a[156:159], v[176:179], v[72:75]
ds_read_b64_tr_b16 a[152:153], v226 offset:4096
v_cvt_pk_bf16_f32 v52, v52, v53
v_mfma_f32_16x16x32_bf16 v[76:79], a[156:159], v[180:183], v[76:79]
s_waitcnt lgkmcnt(5)
ds_read_b64_tr_b16 a[154:155], v226 offset:6144
v_cvt_pk_bf16_f32 v53, v54, v55
v_mfma_f32_16x16x32_bf16 v[64:67], a[128:131], v[192:195], v[64:67]
ds_read_b64_tr_b16 a[156:157], v227 offset:4096
v_cvt_pk_bf16_f32 v54, v60, v61
v_mfma_f32_16x16x32_bf16 v[68:71], a[128:131], v[196:199], v[68:71]
ds_read_b64_tr_b16 a[158:159], v227 offset:6144
v_cvt_pk_bf16_f32 v55, v62, v63
v_mfma_f32_16x16x32_bf16 v[72:75], a[132:135], v[192:195], v[72:75]
ds_read_b64_tr_b16 a[128:129], v226 offset:8192
ds_read_b64_tr_b16 a[130:131], v226 offset:10240
v_mfma_f32_16x16x32_bf16 v[76:79], a[132:135], v[196:199], v[76:79]
buffer_load_dwordx4 v92, s[60:63], s71 offen offset:2048 lds
v_mfma_f32_16x16x32_bf16 v[64:67], a[136:139], v[208:211], v[64:67]
ds_read_b64_tr_b16 a[132:133], v227 offset:8192
buffer_store_dwordx4 v[52:55], v253, s[64:67], s81 offen offset:1024 sc1
v_mfma_f32_16x16x32_bf16 v[68:71], a[136:139], v[212:215], v[68:71]
s_add_u32 s81, s80, 135168
ds_read_b64_tr_b16 a[134:135], v227 offset:10240
v_mfma_f32_16x16x32_bf16 v[72:75], a[140:143], v[208:211], v[72:75]
ds_read_b64_tr_b16 a[136:137], v226 offset:12288
v_mfma_f32_16x16x32_bf16 v[76:79], a[140:143], v[212:215], v[76:79]
s_waitcnt lgkmcnt(5)
ds_read_b64_tr_b16 a[138:139], v226 offset:14336
v_mfma_f32_16x16x32_bf16 v[32:35], a[144:147], v[96:99], 0
ds_read_b64_tr_b16 a[140:141], v227 offset:12288
ds_read_b64_tr_b16 a[142:143], v227 offset:14336
v_mfma_f32_16x16x32_bf16 v[36:39], a[144:147], v[100:103], 0
buffer_load_dwordx4 v93, s[60:63], s71 offen offset:3072 lds
v_mfma_f32_16x16x32_bf16 v[40:43], a[148:151], v[96:99], 0
ds_read_b64_tr_b16 a[144:145], v226 offset:16384
v_mfma_f32_16x16x32_bf16 v[44:47], a[148:151], v[100:103], 0
ds_read_b64_tr_b16 a[146:147], v226 offset:18432
v_cvt_pk_bf16_f32 v64, v64, v65
v_mfma_f32_16x16x32_bf16 v[32:35], a[152:155], v[112:115], v[32:35]
ds_read_b64_tr_b16 a[148:149], v227 offset:16384
v_cvt_pk_bf16_f32 v65, v66, v67
v_mfma_f32_16x16x32_bf16 v[36:39], a[152:155], v[116:119], v[36:39]
ds_read_b64_tr_b16 a[150:151], v227 offset:18432
v_cvt_pk_bf16_f32 v66, v72, v73
v_mfma_f32_16x16x32_bf16 v[40:43], a[156:159], v[112:115], v[40:43]
ds_read_b64_tr_b16 a[152:153], v226 offset:20480
v_cvt_pk_bf16_f32 v67, v74, v75
v_mfma_f32_16x16x32_bf16 v[44:47], a[156:159], v[116:119], v[44:47]
s_waitcnt lgkmcnt(5)
ds_read_b64_tr_b16 a[154:155], v226 offset:22528
buffer_store_dwordx4 v[64:67], v252, s[64:67], s81 offen offset:0 sc1
v_mfma_f32_16x16x32_bf16 v[32:35], a[128:131], v[128:131], v[32:35]
ds_read_b64_tr_b16 a[156:157], v227 offset:20480
v_cvt_pk_bf16_f32 v68, v68, v69
v_mfma_f32_16x16x32_bf16 v[36:39], a[128:131], v[132:135], v[36:39]
ds_read_b64_tr_b16 a[158:159], v227 offset:22528
v_cvt_pk_bf16_f32 v69, v70, v71
v_mfma_f32_16x16x32_bf16 v[40:43], a[132:135], v[128:131], v[40:43]
ds_read_b64_tr_b16 a[128:129], v226 offset:24576
v_cvt_pk_bf16_f32 v70, v76, v77
v_mfma_f32_16x16x32_bf16 v[44:47], a[132:135], v[132:135], v[44:47]
ds_read_b64_tr_b16 a[130:131], v226 offset:26624
v_cvt_pk_bf16_f32 v71, v78, v79
v_mfma_f32_16x16x32_bf16 v[32:35], a[136:139], v[144:147], v[32:35]
ds_read_b64_tr_b16 a[132:133], v227 offset:24576
buffer_store_dwordx4 v[68:71], v253, s[64:67], s81 offen offset:0 sc1
v_mfma_f32_16x16x32_bf16 v[36:39], a[136:139], v[148:151], v[36:39]
ds_read_b64_tr_b16 a[134:135], v227 offset:26624
v_mfma_f32_16x16x32_bf16 v[40:43], a[140:143], v[144:147], v[40:43]
ds_read_b64_tr_b16 a[136:137], v226 offset:28672
v_mfma_f32_16x16x32_bf16 v[44:47], a[140:143], v[148:151], v[44:47]
s_waitcnt lgkmcnt(5)
ds_read_b64_tr_b16 a[138:139], v226 offset:30720
v_mfma_f32_16x16x32_bf16 v[32:35], a[144:147], v[160:163], v[32:35]
ds_read_b64_tr_b16 a[140:141], v227 offset:28672
v_mfma_f32_16x16x32_bf16 v[36:39], a[144:147], v[164:167], v[36:39]
s_waitcnt vmcnt(32)
ds_read_b64_tr_b16 a[142:143], v227 offset:30720
v_mfma_f32_16x16x32_bf16 v[40:43], a[148:151], v[160:163], v[40:43]
s_barrier
ds_read_b64_tr_b16 a[144:145], v224 offset:32768
v_mfma_f32_16x16x32_bf16 v[44:47], a[148:151], v[164:167], v[44:47]
ds_read_b64_tr_b16 a[146:147], v224 offset:34816
v_mfma_f32_16x16x32_bf16 v[32:35], a[152:155], v[176:179], v[32:35]
ds_read_b64_tr_b16 a[148:149], v225 offset:32768
v_mfma_f32_16x16x32_bf16 v[36:39], a[152:155], v[180:183], v[36:39]
s_add_u32 s71, s76, 640
ds_read_b64_tr_b16 a[150:151], v225 offset:34816
v_mfma_f32_16x16x32_bf16 v[40:43], a[156:159], v[176:179], v[40:43]
s_add_u32 m0, s77, 131072
ds_read_b64_tr_b16 a[152:153], v224 offset:36864
ds_read_b64_tr_b16 a[154:155], v224 offset:38912
v_mfma_f32_16x16x32_bf16 v[44:47], a[156:159], v[180:183], v[44:47]
s_waitcnt lgkmcnt(6)
buffer_load_dwordx4 v90, s[60:63], s71 offen offset:0 lds
v_mfma_f32_16x16x32_bf16 v[32:35], a[128:131], v[192:195], v[32:35]
ds_read_b64_tr_b16 a[156:157], v225 offset:36864
v_mfma_f32_16x16x32_bf16 v[36:39], a[128:131], v[196:199], v[36:39]
ds_read_b64_tr_b16 a[158:159], v225 offset:38912
v_mfma_f32_16x16x32_bf16 v[40:43], a[132:135], v[192:195], v[40:43]
ds_read_b64_tr_b16 a[128:129], v224 offset:40960
v_mfma_f32_16x16x32_bf16 v[44:47], a[132:135], v[196:199], v[44:47]
ds_read_b64_tr_b16 a[130:131], v224 offset:43008
v_mfma_f32_16x16x32_bf16 v[32:35], a[136:139], v[208:211], v[32:35]
ds_read_b64_tr_b16 a[132:133], v225 offset:40960
ds_read_b64_tr_b16 a[134:135], v225 offset:43008
v_mfma_f32_16x16x32_bf16 v[36:39], a[136:139], v[212:215], v[36:39]
buffer_load_dwordx4 v91, s[60:63], s71 offen offset:1024 lds
v_mfma_f32_16x16x32_bf16 v[40:43], a[140:143], v[208:211], v[40:43]
ds_read_b64_tr_b16 a[136:137], v224 offset:45056
v_mfma_f32_16x16x32_bf16 v[44:47], a[140:143], v[212:215], v[44:47]
s_waitcnt lgkmcnt(5)
ds_read_b64_tr_b16 a[138:139], v224 offset:47104
v_mfma_f32_16x16x32_bf16 v[48:51], a[144:147], v[96:99], 0
ds_read_b64_tr_b16 a[140:141], v225 offset:45056
v_mfma_f32_16x16x32_bf16 v[52:55], a[144:147], v[100:103], 0
ds_read_b64_tr_b16 a[142:143], v225 offset:47104
v_mfma_f32_16x16x32_bf16 v[56:59], a[148:151], v[96:99], 0
ds_read_b64_tr_b16 a[144:145], v224 offset:49152
ds_read_b64_tr_b16 a[146:147], v224 offset:51200
v_mfma_f32_16x16x32_bf16 v[60:63], a[148:151], v[100:103], 0
buffer_load_dwordx4 v92, s[60:63], s71 offen offset:2048 lds
v_mfma_f32_16x16x32_bf16 v[48:51], a[152:155], v[112:115], v[48:51]
ds_read_b64_tr_b16 a[148:149], v225 offset:49152
v_cvt_pk_bf16_f32 v32, v32, v33
v_mfma_f32_16x16x32_bf16 v[52:55], a[152:155], v[116:119], v[52:55]
ds_read_b64_tr_b16 a[150:151], v225 offset:51200
v_cvt_pk_bf16_f32 v33, v34, v35
v_mfma_f32_16x16x32_bf16 v[56:59], a[156:159], v[112:115], v[56:59]
ds_read_b64_tr_b16 a[152:153], v224 offset:53248
v_cvt_pk_bf16_f32 v34, v40, v41
v_mfma_f32_16x16x32_bf16 v[60:63], a[156:159], v[116:119], v[60:63]
s_waitcnt lgkmcnt(5)
ds_read_b64_tr_b16 a[154:155], v224 offset:55296
v_cvt_pk_bf16_f32 v35, v42, v43
v_mfma_f32_16x16x32_bf16 v[48:51], a[128:131], v[128:131], v[48:51]
ds_read_b64_tr_b16 a[156:157], v225 offset:53248
ds_read_b64_tr_b16 a[158:159], v225 offset:55296
v_mfma_f32_16x16x32_bf16 v[52:55], a[128:131], v[132:135], v[52:55]
buffer_load_dwordx4 v93, s[60:63], s71 offen offset:3072 lds
v_mfma_f32_16x16x32_bf16 v[56:59], a[132:135], v[128:131], v[56:59]
ds_read_b64_tr_b16 a[128:129], v224 offset:57344
buffer_store_dwordx4 v[32:35], v252, s[64:67], s81 offen offset:1024 sc1
v_mfma_f32_16x16x32_bf16 v[60:63], a[132:135], v[132:135], v[60:63]
ds_read_b64_tr_b16 a[130:131], v224 offset:59392
v_cvt_pk_bf16_f32 v36, v36, v37
v_mfma_f32_16x16x32_bf16 v[48:51], a[136:139], v[144:147], v[48:51]
ds_read_b64_tr_b16 a[132:133], v225 offset:57344
v_cvt_pk_bf16_f32 v37, v38, v39
v_mfma_f32_16x16x32_bf16 v[52:55], a[136:139], v[148:151], v[52:55]
s_add_u32 s71, s76, 29312
ds_read_b64_tr_b16 a[134:135], v225 offset:59392
v_cvt_pk_bf16_f32 v38, v44, v45
v_mfma_f32_16x16x32_bf16 v[56:59], a[140:143], v[144:147], v[56:59]
s_add_u32 m0, s77, 135168
ds_read_b64_tr_b16 a[136:137], v224 offset:61440
ds_read_b64_tr_b16 a[138:139], v224 offset:63488
v_mfma_f32_16x16x32_bf16 v[60:63], a[140:143], v[148:151], v[60:63]
s_waitcnt lgkmcnt(6)
buffer_load_dwordx4 v90, s[60:63], s71 offen offset:0 lds
v_mfma_f32_16x16x32_bf16 v[48:51], a[144:147], v[160:163], v[48:51]
ds_read_b64_tr_b16 a[140:141], v225 offset:61440
v_cvt_pk_bf16_f32 v39, v46, v47
v_mfma_f32_16x16x32_bf16 v[52:55], a[144:147], v[164:167], v[52:55]
ds_read_b64_tr_b16 a[142:143], v225 offset:63488
buffer_store_dwordx4 v[36:39], v253, s[64:67], s81 offen offset:1024 sc1
v_mfma_f32_16x16x32_bf16 v[56:59], a[148:151], v[160:163], v[56:59]
s_add_u32 s81, s80, 270336
ds_read_b64_tr_b16 a[144:145], v226 offset:32768
v_add_u32 v224, 0x10000, v224
v_mfma_f32_16x16x32_bf16 v[60:63], a[148:151], v[164:167], v[60:63]
ds_read_b64_tr_b16 a[146:147], v226 offset:34816
v_add_u32 v225, 0x10000, v225
v_mfma_f32_16x16x32_bf16 v[48:51], a[152:155], v[176:179], v[48:51]
ds_read_b64_tr_b16 a[148:149], v227 offset:32768
ds_read_b64_tr_b16 a[150:151], v227 offset:34816
v_mfma_f32_16x16x32_bf16 v[52:55], a[152:155], v[180:183], v[52:55]
buffer_load_dwordx4 v91, s[60:63], s71 offen offset:1024 lds
v_mfma_f32_16x16x32_bf16 v[56:59], a[156:159], v[176:179], v[56:59]
ds_read_b64_tr_b16 a[152:153], v226 offset:36864
v_mfma_f32_16x16x32_bf16 v[60:63], a[156:159], v[180:183], v[60:63]
s_waitcnt lgkmcnt(5)
ds_read_b64_tr_b16 a[154:155], v226 offset:38912
v_mfma_f32_16x16x32_bf16 v[48:51], a[128:131], v[192:195], v[48:51]
ds_read_b64_tr_b16 a[156:157], v227 offset:36864
v_mfma_f32_16x16x32_bf16 v[52:55], a[128:131], v[196:199], v[52:55]
ds_read_b64_tr_b16 a[158:159], v227 offset:38912
v_mfma_f32_16x16x32_bf16 v[56:59], a[132:135], v[192:195], v[56:59]
ds_read_b64_tr_b16 a[128:129], v226 offset:40960
ds_read_b64_tr_b16 a[130:131], v226 offset:43008
v_mfma_f32_16x16x32_bf16 v[60:63], a[132:135], v[196:199], v[60:63]
buffer_load_dwordx4 v92, s[60:63], s71 offen offset:2048 lds
v_mfma_f32_16x16x32_bf16 v[48:51], a[136:139], v[208:211], v[48:51]
ds_read_b64_tr_b16 a[132:133], v227 offset:40960
v_mfma_f32_16x16x32_bf16 v[52:55], a[136:139], v[212:215], v[52:55]
ds_read_b64_tr_b16 a[134:135], v227 offset:43008
v_mfma_f32_16x16x32_bf16 v[56:59], a[140:143], v[208:211], v[56:59]
ds_read_b64_tr_b16 a[136:137], v226 offset:45056
v_mfma_f32_16x16x32_bf16 v[60:63], a[140:143], v[212:215], v[60:63]
s_waitcnt lgkmcnt(5)
ds_read_b64_tr_b16 a[138:139], v226 offset:47104
v_mfma_f32_16x16x32_bf16 v[64:67], a[144:147], v[96:99], 0
ds_read_b64_tr_b16 a[140:141], v227 offset:45056
ds_read_b64_tr_b16 a[142:143], v227 offset:47104
v_mfma_f32_16x16x32_bf16 v[68:71], a[144:147], v[100:103], 0
buffer_load_dwordx4 v93, s[60:63], s71 offen offset:3072 lds
v_mfma_f32_16x16x32_bf16 v[72:75], a[148:151], v[96:99], 0
ds_read_b64_tr_b16 a[144:145], v226 offset:49152
v_mfma_f32_16x16x32_bf16 v[76:79], a[148:151], v[100:103], 0
ds_read_b64_tr_b16 a[146:147], v226 offset:51200
v_cvt_pk_bf16_f32 v48, v48, v49
v_mfma_f32_16x16x32_bf16 v[64:67], a[152:155], v[112:115], v[64:67]
ds_read_b64_tr_b16 a[148:149], v227 offset:49152
v_cvt_pk_bf16_f32 v49, v50, v51
v_mfma_f32_16x16x32_bf16 v[68:71], a[152:155], v[116:119], v[68:71]
ds_read_b64_tr_b16 a[150:151], v227 offset:51200
v_cvt_pk_bf16_f32 v50, v56, v57
v_mfma_f32_16x16x32_bf16 v[72:75], a[156:159], v[112:115], v[72:75]
ds_read_b64_tr_b16 a[152:153], v226 offset:53248
v_cvt_pk_bf16_f32 v51, v58, v59
v_mfma_f32_16x16x32_bf16 v[76:79], a[156:159], v[116:119], v[76:79]
s_waitcnt lgkmcnt(5)
ds_read_b64_tr_b16 a[154:155], v226 offset:55296
buffer_store_dwordx4 v[48:51], v252, s[64:67], s81 offen offset:0 sc1
v_mfma_f32_16x16x32_bf16 v[64:67], a[128:131], v[128:131], v[64:67]
ds_read_b64_tr_b16 a[156:157], v227 offset:53248
v_cvt_pk_bf16_f32 v52, v52, v53
v_mfma_f32_16x16x32_bf16 v[68:71], a[128:131], v[132:135], v[68:71]
ds_read_b64_tr_b16 a[158:159], v227 offset:55296
v_cvt_pk_bf16_f32 v53, v54, v55
v_mfma_f32_16x16x32_bf16 v[72:75], a[132:135], v[128:131], v[72:75]
ds_read_b64_tr_b16 a[128:129], v226 offset:57344
v_cvt_pk_bf16_f32 v54, v60, v61
v_mfma_f32_16x16x32_bf16 v[76:79], a[132:135], v[132:135], v[76:79]
ds_read_b64_tr_b16 a[130:131], v226 offset:59392
v_cvt_pk_bf16_f32 v55, v62, v63
v_mfma_f32_16x16x32_bf16 v[64:67], a[136:139], v[144:147], v[64:67]
ds_read_b64_tr_b16 a[132:133], v227 offset:57344
buffer_store_dwordx4 v[52:55], v253, s[64:67], s81 offen offset:0 sc1
v_mfma_f32_16x16x32_bf16 v[68:71], a[136:139], v[148:151], v[68:71]
ds_read_b64_tr_b16 a[134:135], v227 offset:59392
v_mfma_f32_16x16x32_bf16 v[72:75], a[140:143], v[144:147], v[72:75]
ds_read_b64_tr_b16 a[136:137], v226 offset:61440
v_mfma_f32_16x16x32_bf16 v[76:79], a[140:143], v[148:151], v[76:79]
s_waitcnt lgkmcnt(5)
ds_read_b64_tr_b16 a[138:139], v226 offset:63488
v_add_u32 v226, 0x10000, v226
v_mfma_f32_16x16x32_bf16 v[64:67], a[144:147], v[160:163], v[64:67]
ds_read_b64_tr_b16 a[140:141], v227 offset:61440
v_mfma_f32_16x16x32_bf16 v[68:71], a[144:147], v[164:167], v[68:71]
s_waitcnt vmcnt(26)
ds_read_b64_tr_b16 a[142:143], v227 offset:63488
v_add_u32 v227, 0x10000, v227
v_mfma_f32_16x16x32_bf16 v[72:75], a[148:151], v[160:163], v[72:75]
s_barrier
ds_read_b64_tr_b16 a[144:145], v224 offset:0
v_mfma_f32_16x16x32_bf16 v[76:79], a[148:151], v[164:167], v[76:79]
ds_read_b64_tr_b16 a[146:147], v224 offset:2048
v_mfma_f32_16x16x32_bf16 v[64:67], a[152:155], v[176:179], v[64:67]
ds_read_b64_tr_b16 a[148:149], v225 offset:0
v_mfma_f32_16x16x32_bf16 v[68:71], a[152:155], v[180:183], v[68:71]
s_add_u32 s71, s76, 768
ds_read_b64_tr_b16 a[150:151], v225 offset:2048
v_mfma_f32_16x16x32_bf16 v[72:75], a[156:159], v[176:179], v[72:75]
s_add_u32 m0, s77, 0
ds_read_b64_tr_b16 a[152:153], v224 offset:4096
ds_read_b64_tr_b16 a[154:155], v224 offset:6144
v_mfma_f32_16x16x32_bf16 v[76:79], a[156:159], v[180:183], v[76:79]
s_waitcnt lgkmcnt(6)
buffer_load_dwordx4 v90, s[60:63], s71 offen offset:0 lds
v_mfma_f32_16x16x32_bf16 v[64:67], a[128:131], v[192:195], v[64:67]
ds_read_b64_tr_b16 a[156:157], v225 offset:4096
v_mfma_f32_16x16x32_bf16 v[68:71], a[128:131], v[196:199], v[68:71]
ds_read_b64_tr_b16 a[158:159], v225 offset:6144
v_mfma_f32_16x16x32_bf16 v[72:75], a[132:135], v[192:195], v[72:75]
ds_read_b64_tr_b16 a[128:129], v224 offset:8192
v_mfma_f32_16x16x32_bf16 v[76:79], a[132:135], v[196:199], v[76:79]
ds_read_b64_tr_b16 a[130:131], v224 offset:10240
v_mfma_f32_16x16x32_bf16 v[64:67], a[136:139], v[208:211], v[64:67]
ds_read_b64_tr_b16 a[132:133], v225 offset:8192
ds_read_b64_tr_b16 a[134:135], v225 offset:10240
v_mfma_f32_16x16x32_bf16 v[68:71], a[136:139], v[212:215], v[68:71]
buffer_load_dwordx4 v91, s[60:63], s71 offen offset:1024 lds
v_mfma_f32_16x16x32_bf16 v[72:75], a[140:143], v[208:211], v[72:75]
ds_read_b64_tr_b16 a[136:137], v224 offset:12288
v_mfma_f32_16x16x32_bf16 v[76:79], a[140:143], v[212:215], v[76:79]
s_waitcnt lgkmcnt(5)
ds_read_b64_tr_b16 a[138:139], v224 offset:14336
v_mfma_f32_16x16x32_bf16 v[32:35], a[144:147], v[96:99], 0
ds_read_b64_tr_b16 a[140:141], v225 offset:12288
v_mfma_f32_16x16x32_bf16 v[36:39], a[144:147], v[100:103], 0
ds_read_b64_tr_b16 a[142:143], v225 offset:14336
v_mfma_f32_16x16x32_bf16 v[40:43], a[148:151], v[96:99], 0
ds_read_b64_tr_b16 a[144:145], v224 offset:16384
ds_read_b64_tr_b16 a[146:147], v224 offset:18432
v_mfma_f32_16x16x32_bf16 v[44:47], a[148:151], v[100:103], 0
buffer_load_dwordx4 v92, s[60:63], s71 offen offset:2048 lds
v_mfma_f32_16x16x32_bf16 v[32:35], a[152:155], v[112:115], v[32:35]
ds_read_b64_tr_b16 a[148:149], v225 offset:16384
v_cvt_pk_bf16_f32 v64, v64, v65
v_mfma_f32_16x16x32_bf16 v[36:39], a[152:155], v[116:119], v[36:39]
ds_read_b64_tr_b16 a[150:151], v225 offset:18432
v_cvt_pk_bf16_f32 v65, v66, v67
v_mfma_f32_16x16x32_bf16 v[40:43], a[156:159], v[112:115], v[40:43]
ds_read_b64_tr_b16 a[152:153], v224 offset:20480
v_cvt_pk_bf16_f32 v66, v72, v73
v_mfma_f32_16x16x32_bf16 v[44:47], a[156:159], v[116:119], v[44:47]
s_waitcnt lgkmcnt(5)
ds_read_b64_tr_b16 a[154:155], v224 offset:22528
v_cvt_pk_bf16_f32 v67, v74, v75
v_mfma_f32_16x16x32_bf16 v[32:35], a[128:131], v[128:131], v[32:35]
ds_read_b64_tr_b16 a[156:157], v225 offset:20480
ds_read_b64_tr_b16 a[158:159], v225 offset:22528
v_mfma_f32_16x16x32_bf16 v[36:39], a[128:131], v[132:135], v[36:39]
buffer_load_dwordx4 v93, s[60:63], s71 offen offset:3072 lds
v_mfma_f32_16x16x32_bf16 v[40:43], a[132:135], v[128:131], v[40:43]
ds_read_b64_tr_b16 a[128:129], v224 offset:24576
buffer_store_dwordx4 v[64:67], v252, s[64:67], s81 offen offset:1024 sc1
v_mfma_f32_16x16x32_bf16 v[44:47], a[132:135], v[132:135], v[44:47]
ds_read_b64_tr_b16 a[130:131], v224 offset:26624
v_cvt_pk_bf16_f32 v68, v68, v69
v_mfma_f32_16x16x32_bf16 v[32:35], a[136:139], v[144:147], v[32:35]
ds_read_b64_tr_b16 a[132:133], v225 offset:24576
v_cvt_pk_bf16_f32 v69, v70, v71
v_mfma_f32_16x16x32_bf16 v[36:39], a[136:139], v[148:151], v[36:39]
s_add_u32 s71, s76, 29440
ds_read_b64_tr_b16 a[134:135], v225 offset:26624
v_cvt_pk_bf16_f32 v70, v76, v77
v_mfma_f32_16x16x32_bf16 v[40:43], a[140:143], v[144:147], v[40:43]
s_add_u32 m0, s77, 4096
ds_read_b64_tr_b16 a[136:137], v224 offset:28672
ds_read_b64_tr_b16 a[138:139], v224 offset:30720
v_mfma_f32_16x16x32_bf16 v[44:47], a[140:143], v[148:151], v[44:47]
s_waitcnt lgkmcnt(6)
buffer_load_dwordx4 v90, s[60:63], s71 offen offset:0 lds
v_mfma_f32_16x16x32_bf16 v[32:35], a[144:147], v[160:163], v[32:35]
ds_read_b64_tr_b16 a[140:141], v225 offset:28672
v_cvt_pk_bf16_f32 v71, v78, v79
v_mfma_f32_16x16x32_bf16 v[36:39], a[144:147], v[164:167], v[36:39]
ds_read_b64_tr_b16 a[142:143], v225 offset:30720
buffer_store_dwordx4 v[68:71], v253, s[64:67], s81 offen offset:1024 sc1
v_mfma_f32_16x16x32_bf16 v[40:43], a[148:151], v[160:163], v[40:43]
s_add_u32 s81, s80, 405504
ds_read_b64_tr_b16 a[144:145], v226 offset:0
v_mfma_f32_16x16x32_bf16 v[44:47], a[148:151], v[164:167], v[44:47]
ds_read_b64_tr_b16 a[146:147], v226 offset:2048
v_mfma_f32_16x16x32_bf16 v[32:35], a[152:155], v[176:179], v[32:35]
ds_read_b64_tr_b16 a[148:149], v227 offset:0
ds_read_b64_tr_b16 a[150:151], v227 offset:2048
v_mfma_f32_16x16x32_bf16 v[36:39], a[152:155], v[180:183], v[36:39]
buffer_load_dwordx4 v91, s[60:63], s71 offen offset:1024 lds
v_mfma_f32_16x16x32_bf16 v[40:43], a[156:159], v[176:179], v[40:43]
ds_read_b64_tr_b16 a[152:153], v226 offset:4096
v_mfma_f32_16x16x32_bf16 v[44:47], a[156:159], v[180:183], v[44:47]
s_waitcnt lgkmcnt(5)
ds_read_b64_tr_b16 a[154:155], v226 offset:6144
v_mfma_f32_16x16x32_bf16 v[32:35], a[128:131], v[192:195], v[32:35]
ds_read_b64_tr_b16 a[156:157], v227 offset:4096
v_mfma_f32_16x16x32_bf16 v[36:39], a[128:131], v[196:199], v[36:39]
ds_read_b64_tr_b16 a[158:159], v227 offset:6144
v_mfma_f32_16x16x32_bf16 v[40:43], a[132:135], v[192:195], v[40:43]
ds_read_b64_tr_b16 a[128:129], v226 offset:8192
ds_read_b64_tr_b16 a[130:131], v226 offset:10240
v_mfma_f32_16x16x32_bf16 v[44:47], a[132:135], v[196:199], v[44:47]
buffer_load_dwordx4 v92, s[60:63], s71 offen offset:2048 lds
v_mfma_f32_16x16x32_bf16 v[32:35], a[136:139], v[208:211], v[32:35]
ds_read_b64_tr_b16 a[132:133], v227 offset:8192
v_mfma_f32_16x16x32_bf16 v[36:39], a[136:139], v[212:215], v[36:39]
ds_read_b64_tr_b16 a[134:135], v227 offset:10240
v_mfma_f32_16x16x32_bf16 v[40:43], a[140:143], v[208:211], v[40:43]
ds_read_b64_tr_b16 a[136:137], v226 offset:12288
v_mfma_f32_16x16x32_bf16 v[44:47], a[140:143], v[212:215], v[44:47]
s_waitcnt lgkmcnt(5)
ds_read_b64_tr_b16 a[138:139], v226 offset:14336
v_mfma_f32_16x16x32_bf16 v[48:51], a[144:147], v[96:99], 0
ds_read_b64_tr_b16 a[140:141], v227 offset:12288
ds_read_b64_tr_b16 a[142:143], v227 offset:14336
v_mfma_f32_16x16x32_bf16 v[52:55], a[144:147], v[100:103], 0
buffer_load_dwordx4 v93, s[60:63], s71 offen offset:3072 lds
v_mfma_f32_16x16x32_bf16 v[56:59], a[148:151], v[96:99], 0
ds_read_b64_tr_b16 a[144:145], v226 offset:16384
v_mfma_f32_16x16x32_bf16 v[60:63], a[148:151], v[100:103], 0
ds_read_b64_tr_b16 a[146:147], v226 offset:18432
v_cvt_pk_bf16_f32 v32, v32, v33
v_mfma_f32_16x16x32_bf16 v[48:51], a[152:155], v[112:115], v[48:51]
ds_read_b64_tr_b16 a[148:149], v227 offset:16384
v_cvt_pk_bf16_f32 v33, v34, v35
v_mfma_f32_16x16x32_bf16 v[52:55], a[152:155], v[116:119], v[52:55]
ds_read_b64_tr_b16 a[150:151], v227 offset:18432
v_cvt_pk_bf16_f32 v34, v40, v41
v_mfma_f32_16x16x32_bf16 v[56:59], a[156:159], v[112:115], v[56:59]
ds_read_b64_tr_b16 a[152:153], v226 offset:20480
v_cvt_pk_bf16_f32 v35, v42, v43
v_mfma_f32_16x16x32_bf16 v[60:63], a[156:159], v[116:119], v[60:63]
s_waitcnt lgkmcnt(5)
ds_read_b64_tr_b16 a[154:155], v226 offset:22528
buffer_store_dwordx4 v[32:35], v252, s[64:67], s81 offen offset:0 sc1
v_mfma_f32_16x16x32_bf16 v[48:51], a[128:131], v[128:131], v[48:51]
ds_read_b64_tr_b16 a[156:157], v227 offset:20480
v_cvt_pk_bf16_f32 v36, v36, v37
v_mfma_f32_16x16x32_bf16 v[52:55], a[128:131], v[132:135], v[52:55]
ds_read_b64_tr_b16 a[158:159], v227 offset:22528
v_cvt_pk_bf16_f32 v37, v38, v39
v_mfma_f32_16x16x32_bf16 v[56:59], a[132:135], v[128:131], v[56:59]
ds_read_b64_tr_b16 a[128:129], v226 offset:24576
v_cvt_pk_bf16_f32 v38, v44, v45
v_mfma_f32_16x16x32_bf16 v[60:63], a[132:135], v[132:135], v[60:63]
ds_read_b64_tr_b16 a[130:131], v226 offset:26624
v_cvt_pk_bf16_f32 v39, v46, v47
v_mfma_f32_16x16x32_bf16 v[48:51], a[136:139], v[144:147], v[48:51]
ds_read_b64_tr_b16 a[132:133], v227 offset:24576
buffer_store_dwordx4 v[36:39], v253, s[64:67], s81 offen offset:0 sc1
v_mfma_f32_16x16x32_bf16 v[52:55], a[136:139], v[148:151], v[52:55]
ds_read_b64_tr_b16 a[134:135], v227 offset:26624
v_mfma_f32_16x16x32_bf16 v[56:59], a[140:143], v[144:147], v[56:59]
ds_read_b64_tr_b16 a[136:137], v226 offset:28672
v_mfma_f32_16x16x32_bf16 v[60:63], a[140:143], v[148:151], v[60:63]
s_waitcnt lgkmcnt(5)
ds_read_b64_tr_b16 a[138:139], v226 offset:30720
v_mfma_f32_16x16x32_bf16 v[48:51], a[144:147], v[160:163], v[48:51]
ds_read_b64_tr_b16 a[140:141], v227 offset:28672
v_mfma_f32_16x16x32_bf16 v[52:55], a[144:147], v[164:167], v[52:55]
s_waitcnt vmcnt(26)
ds_read_b64_tr_b16 a[142:143], v227 offset:30720
v_mfma_f32_16x16x32_bf16 v[56:59], a[148:151], v[160:163], v[56:59]
s_barrier
ds_read_b64_tr_b16 a[144:145], v224 offset:32768
v_mfma_f32_16x16x32_bf16 v[60:63], a[148:151], v[164:167], v[60:63]
ds_read_b64_tr_b16 a[146:147], v224 offset:34816
v_mfma_f32_16x16x32_bf16 v[48:51], a[152:155], v[176:179], v[48:51]
ds_read_b64_tr_b16 a[148:149], v225 offset:32768
v_mfma_f32_16x16x32_bf16 v[52:55], a[152:155], v[180:183], v[52:55]
s_add_u32 s71, s74, 0
ds_read_b64_tr_b16 a[150:151], v225 offset:34816
v_mfma_f32_16x16x32_bf16 v[56:59], a[156:159], v[176:179], v[56:59]
s_add_u32 m0, s79, 32768
ds_read_b64_tr_b16 a[152:153], v224 offset:36864
ds_read_b64_tr_b16 a[154:155], v224 offset:38912
v_mfma_f32_16x16x32_bf16 v[60:63], a[156:159], v[180:183], v[60:63]
s_waitcnt lgkmcnt(6)
buffer_load_dwordx4 v90, s[48:51], s71 offen offset:0 lds
v_mfma_f32_16x16x32_bf16 v[48:51], a[128:131], v[192:195], v[48:51]
ds_read_b64_tr_b16 a[156:157], v225 offset:36864
v_mfma_f32_16x16x32_bf16 v[52:55], a[128:131], v[196:199], v[52:55]
ds_read_b64_tr_b16 a[158:159], v225 offset:38912
v_mfma_f32_16x16x32_bf16 v[56:59], a[132:135], v[192:195], v[56:59]
ds_read_b64_tr_b16 a[128:129], v224 offset:40960
v_mfma_f32_16x16x32_bf16 v[60:63], a[132:135], v[196:199], v[60:63]
ds_read_b64_tr_b16 a[130:131], v224 offset:43008
v_mfma_f32_16x16x32_bf16 v[48:51], a[136:139], v[208:211], v[48:51]
ds_read_b64_tr_b16 a[132:133], v225 offset:40960
ds_read_b64_tr_b16 a[134:135], v225 offset:43008
v_mfma_f32_16x16x32_bf16 v[52:55], a[136:139], v[212:215], v[52:55]
buffer_load_dwordx4 v91, s[48:51], s71 offen offset:1024 lds
v_mfma_f32_16x16x32_bf16 v[56:59], a[140:143], v[208:211], v[56:59]
ds_read_b64_tr_b16 a[136:137], v224 offset:45056
v_mfma_f32_16x16x32_bf16 v[60:63], a[140:143], v[212:215], v[60:63]
s_waitcnt lgkmcnt(5)
ds_read_b64_tr_b16 a[138:139], v224 offset:47104
v_mfma_f32_16x16x32_bf16 v[64:67], a[144:147], v[96:99], 0
ds_read_b64_tr_b16 a[140:141], v225 offset:45056
v_mfma_f32_16x16x32_bf16 v[68:71], a[144:147], v[100:103], 0
ds_read_b64_tr_b16 a[142:143], v225 offset:47104
v_mfma_f32_16x16x32_bf16 v[72:75], a[148:151], v[96:99], 0
ds_read_b64_tr_b16 a[144:145], v224 offset:49152
ds_read_b64_tr_b16 a[146:147], v224 offset:51200
v_mfma_f32_16x16x32_bf16 v[76:79], a[148:151], v[100:103], 0
buffer_load_dwordx4 v92, s[48:51], s71 offen offset:2048 lds
v_mfma_f32_16x16x32_bf16 v[64:67], a[152:155], v[112:115], v[64:67]
ds_read_b64_tr_b16 a[148:149], v225 offset:49152
v_cvt_pk_bf16_f32 v48, v48, v49
v_mfma_f32_16x16x32_bf16 v[68:71], a[152:155], v[116:119], v[68:71]
ds_read_b64_tr_b16 a[150:151], v225 offset:51200
v_cvt_pk_bf16_f32 v49, v50, v51
v_mfma_f32_16x16x32_bf16 v[72:75], a[156:159], v[112:115], v[72:75]
ds_read_b64_tr_b16 a[152:153], v224 offset:53248
v_cvt_pk_bf16_f32 v50, v56, v57
v_mfma_f32_16x16x32_bf16 v[76:79], a[156:159], v[116:119], v[76:79]
s_waitcnt lgkmcnt(5)
ds_read_b64_tr_b16 a[154:155], v224 offset:55296
v_cvt_pk_bf16_f32 v51, v58, v59
v_mfma_f32_16x16x32_bf16 v[64:67], a[128:131], v[128:131], v[64:67]
ds_read_b64_tr_b16 a[156:157], v225 offset:53248
ds_read_b64_tr_b16 a[158:159], v225 offset:55296
v_mfma_f32_16x16x32_bf16 v[68:71], a[128:131], v[132:135], v[68:71]
buffer_load_dwordx4 v93, s[48:51], s71 offen offset:3072 lds
v_mfma_f32_16x16x32_bf16 v[72:75], a[132:135], v[128:131], v[72:75]
ds_read_b64_tr_b16 a[128:129], v224 offset:57344
buffer_store_dwordx4 v[48:51], v252, s[64:67], s81 offen offset:1024 sc1
v_mfma_f32_16x16x32_bf16 v[76:79], a[132:135], v[132:135], v[76:79]
ds_read_b64_tr_b16 a[130:131], v224 offset:59392
v_cvt_pk_bf16_f32 v52, v52, v53
v_mfma_f32_16x16x32_bf16 v[64:67], a[136:139], v[144:147], v[64:67]
ds_read_b64_tr_b16 a[132:133], v225 offset:57344
v_cvt_pk_bf16_f32 v53, v54, v55
v_mfma_f32_16x16x32_bf16 v[68:71], a[136:139], v[148:151], v[68:71]
s_add_u32 s71, s74, 28672
ds_read_b64_tr_b16 a[134:135], v225 offset:59392
v_cvt_pk_bf16_f32 v54, v60, v61
v_mfma_f32_16x16x32_bf16 v[72:75], a[140:143], v[144:147], v[72:75]
s_add_u32 m0, s79, 36864
ds_read_b64_tr_b16 a[136:137], v224 offset:61440
ds_read_b64_tr_b16 a[138:139], v224 offset:63488
v_mfma_f32_16x16x32_bf16 v[76:79], a[140:143], v[148:151], v[76:79]
s_waitcnt lgkmcnt(6)
buffer_load_dwordx4 v90, s[48:51], s71 offen offset:0 lds
v_mfma_f32_16x16x32_bf16 v[64:67], a[144:147], v[160:163], v[64:67]
ds_read_b64_tr_b16 a[140:141], v225 offset:61440
v_cvt_pk_bf16_f32 v55, v62, v63
v_mfma_f32_16x16x32_bf16 v[68:71], a[144:147], v[164:167], v[68:71]
ds_read_b64_tr_b16 a[142:143], v225 offset:63488
buffer_store_dwordx4 v[52:55], v253, s[64:67], s81 offen offset:1024 sc1
v_mfma_f32_16x16x32_bf16 v[72:75], a[148:151], v[160:163], v[72:75]
s_add_u32 s81, s80, 540672
ds_read_b64_tr_b16 a[144:145], v226 offset:32768
v_add_u32 v224, 0x10000, v224
v_mfma_f32_16x16x32_bf16 v[76:79], a[148:151], v[164:167], v[76:79]
ds_read_b64_tr_b16 a[146:147], v226 offset:34816
v_add_u32 v225, 0x10000, v225
v_mfma_f32_16x16x32_bf16 v[64:67], a[152:155], v[176:179], v[64:67]
ds_read_b64_tr_b16 a[148:149], v227 offset:32768
ds_read_b64_tr_b16 a[150:151], v227 offset:34816
v_mfma_f32_16x16x32_bf16 v[68:71], a[152:155], v[180:183], v[68:71]
buffer_load_dwordx4 v91, s[48:51], s71 offen offset:1024 lds
v_mfma_f32_16x16x32_bf16 v[72:75], a[156:159], v[176:179], v[72:75]
ds_read_b64_tr_b16 a[152:153], v226 offset:36864
v_mfma_f32_16x16x32_bf16 v[76:79], a[156:159], v[180:183], v[76:79]
s_waitcnt lgkmcnt(5)
ds_read_b64_tr_b16 a[154:155], v226 offset:38912
v_mfma_f32_16x16x32_bf16 v[64:67], a[128:131], v[192:195], v[64:67]
ds_read_b64_tr_b16 a[156:157], v227 offset:36864
v_mfma_f32_16x16x32_bf16 v[68:71], a[128:131], v[196:199], v[68:71]
ds_read_b64_tr_b16 a[158:159], v227 offset:38912
v_mfma_f32_16x16x32_bf16 v[72:75], a[132:135], v[192:195], v[72:75]
ds_read_b64_tr_b16 a[128:129], v226 offset:40960
ds_read_b64_tr_b16 a[130:131], v226 offset:43008
v_mfma_f32_16x16x32_bf16 v[76:79], a[132:135], v[196:199], v[76:79]
buffer_load_dwordx4 v92, s[48:51], s71 offen offset:2048 lds
v_mfma_f32_16x16x32_bf16 v[64:67], a[136:139], v[208:211], v[64:67]
ds_read_b64_tr_b16 a[132:133], v227 offset:40960
v_mfma_f32_16x16x32_bf16 v[68:71], a[136:139], v[212:215], v[68:71]
ds_read_b64_tr_b16 a[134:135], v227 offset:43008
v_mfma_f32_16x16x32_bf16 v[72:75], a[140:143], v[208:211], v[72:75]
ds_read_b64_tr_b16 a[136:137], v226 offset:45056
v_mfma_f32_16x16x32_bf16 v[76:79], a[140:143], v[212:215], v[76:79]
s_waitcnt lgkmcnt(5)
ds_read_b64_tr_b16 a[138:139], v226 offset:47104
v_mfma_f32_16x16x32_bf16 v[32:35], a[144:147], v[96:99], 0
ds_read_b64_tr_b16 a[140:141], v227 offset:45056
v_mfma_f32_16x16x32_bf16 v[36:39], a[144:147], v[100:103], 0
ds_read_b64_tr_b16 a[142:143], v227 offset:47104
v_mfma_f32_16x16x32_bf16 v[40:43], a[148:151], v[96:99], 0
ds_read_b64_tr_b16 a[144:145], v226 offset:49152
v_mfma_f32_16x16x32_bf16 v[44:47], a[148:151], v[100:103], 0
ds_read_b64_tr_b16 a[146:147], v226 offset:51200
v_cvt_pk_bf16_f32 v64, v64, v65
v_mfma_f32_16x16x32_bf16 v[32:35], a[152:155], v[112:115], v[32:35]
ds_read_b64_tr_b16 a[148:149], v227 offset:49152
v_cvt_pk_bf16_f32 v65, v66, v67
v_mfma_f32_16x16x32_bf16 v[36:39], a[152:155], v[116:119], v[36:39]
ds_read_b64_tr_b16 a[150:151], v227 offset:51200
v_cvt_pk_bf16_f32 v66, v72, v73
v_mfma_f32_16x16x32_bf16 v[40:43], a[156:159], v[112:115], v[40:43]
ds_read_b64_tr_b16 a[152:153], v226 offset:53248
v_cvt_pk_bf16_f32 v67, v74, v75
v_mfma_f32_16x16x32_bf16 v[44:47], a[156:159], v[116:119], v[44:47]
s_waitcnt lgkmcnt(5)
ds_read_b64_tr_b16 a[154:155], v226 offset:55296
buffer_store_dwordx4 v[64:67], v252, s[64:67], s81 offen offset:0 sc1
v_mfma_f32_16x16x32_bf16 v[32:35], a[128:131], v[128:131], v[32:35]
ds_read_b64_tr_b16 a[156:157], v227 offset:53248
v_cvt_pk_bf16_f32 v68, v68, v69
v_mfma_f32_16x16x32_bf16 v[36:39], a[128:131], v[132:135], v[36:39]
ds_read_b64_tr_b16 a[158:159], v227 offset:55296
v_cvt_pk_bf16_f32 v69, v70, v71
v_mfma_f32_16x16x32_bf16 v[40:43], a[132:135], v[128:131], v[40:43]
ds_read_b64_tr_b16 a[128:129], v226 offset:57344
v_cvt_pk_bf16_f32 v70, v76, v77
v_mfma_f32_16x16x32_bf16 v[44:47], a[132:135], v[132:135], v[44:47]
ds_read_b64_tr_b16 a[130:131], v226 offset:59392
v_cvt_pk_bf16_f32 v71, v78, v79
v_mfma_f32_16x16x32_bf16 v[32:35], a[136:139], v[144:147], v[32:35]
ds_read_b64_tr_b16 a[132:133], v227 offset:57344
buffer_store_dwordx4 v[68:71], v253, s[64:67], s81 offen offset:0 sc1
v_mfma_f32_16x16x32_bf16 v[36:39], a[136:139], v[148:151], v[36:39]
ds_read_b64_tr_b16 a[134:135], v227 offset:59392
v_mfma_f32_16x16x32_bf16 v[40:43], a[140:143], v[144:147], v[40:43]
ds_read_b64_tr_b16 a[136:137], v226 offset:61440
v_mfma_f32_16x16x32_bf16 v[44:47], a[140:143], v[148:151], v[44:47]
s_waitcnt lgkmcnt(5)
ds_read_b64_tr_b16 a[138:139], v226 offset:63488
v_add_u32 v226, 0x10000, v226
v_mfma_f32_16x16x32_bf16 v[32:35], a[144:147], v[160:163], v[32:35]
ds_read_b64_tr_b16 a[140:141], v227 offset:61440
v_mfma_f32_16x16x32_bf16 v[36:39], a[144:147], v[164:167], v[36:39]
s_waitcnt vmcnt(25)
ds_read_b64_tr_b16 a[142:143], v227 offset:63488
v_add_u32 v227, 0x10000, v227
v_mfma_f32_16x16x32_bf16 v[40:43], a[148:151], v[160:163], v[40:43]
s_barrier
ds_read_b64_tr_b16 a[144:145], v224 offset:0
v_mfma_f32_16x16x32_bf16 v[44:47], a[148:151], v[164:167], v[44:47]
ds_read_b64_tr_b16 a[146:147], v224 offset:2048
v_mfma_f32_16x16x32_bf16 v[32:35], a[152:155], v[176:179], v[32:35]
ds_read_b64_tr_b16 a[148:149], v225 offset:0
v_mfma_f32_16x16x32_bf16 v[36:39], a[152:155], v[180:183], v[36:39]
s_add_u32 s71, s74, 200704
ds_read_b64_tr_b16 a[150:151], v225 offset:2048
v_mfma_f32_16x16x32_bf16 v[40:43], a[156:159], v[176:179], v[40:43]
s_add_u32 m0, s79, 65536
ds_read_b64_tr_b16 a[152:153], v224 offset:4096
ds_read_b64_tr_b16 a[154:155], v224 offset:6144
v_mfma_f32_16x16x32_bf16 v[44:47], a[156:159], v[180:183], v[44:47]
s_waitcnt lgkmcnt(6)
buffer_load_dwordx4 v90, s[48:51], s71 offen offset:0 lds
v_mfma_f32_16x16x32_bf16 v[32:35], a[128:131], v[192:195], v[32:35]
ds_read_b64_tr_b16 a[156:157], v225 offset:4096
v_mfma_f32_16x16x32_bf16 v[36:39], a[128:131], v[196:199], v[36:39]
ds_read_b64_tr_b16 a[158:159], v225 offset:6144
v_mfma_f32_16x16x32_bf16 v[40:43], a[132:135], v[192:195], v[40:43]
ds_read_b64_tr_b16 a[128:129], v224 offset:8192
v_mfma_f32_16x16x32_bf16 v[44:47], a[132:135], v[196:199], v[44:47]
ds_read_b64_tr_b16 a[130:131], v224 offset:10240
v_mfma_f32_16x16x32_bf16 v[32:35], a[136:139], v[208:211], v[32:35]
ds_read_b64_tr_b16 a[132:133], v225 offset:8192
ds_read_b64_tr_b16 a[134:135], v225 offset:10240
v_mfma_f32_16x16x32_bf16 v[36:39], a[136:139], v[212:215], v[36:39]
buffer_load_dwordx4 v91, s[48:51], s71 offen offset:1024 lds
v_mfma_f32_16x16x32_bf16 v[40:43], a[140:143], v[208:211], v[40:43]
ds_read_b64_tr_b16 a[136:137], v224 offset:12288
v_mfma_f32_16x16x32_bf16 v[44:47], a[140:143], v[212:215], v[44:47]
s_waitcnt lgkmcnt(5)
ds_read_b64_tr_b16 a[138:139], v224 offset:14336
v_mfma_f32_16x16x32_bf16 v[48:51], a[144:147], v[96:99], 0
ds_read_b64_tr_b16 a[140:141], v225 offset:12288
v_mfma_f32_16x16x32_bf16 v[52:55], a[144:147], v[100:103], 0
ds_read_b64_tr_b16 a[142:143], v225 offset:14336
v_mfma_f32_16x16x32_bf16 v[56:59], a[148:151], v[96:99], 0
ds_read_b64_tr_b16 a[144:145], v224 offset:16384
ds_read_b64_tr_b16 a[146:147], v224 offset:18432
v_mfma_f32_16x16x32_bf16 v[60:63], a[148:151], v[100:103], 0
buffer_load_dwordx4 v92, s[48:51], s71 offen offset:2048 lds
v_mfma_f32_16x16x32_bf16 v[48:51], a[152:155], v[112:115], v[48:51]
ds_read_b64_tr_b16 a[148:149], v225 offset:16384
v_cvt_pk_bf16_f32 v32, v32, v33
v_mfma_f32_16x16x32_bf16 v[52:55], a[152:155], v[116:119], v[52:55]
ds_read_b64_tr_b16 a[150:151], v225 offset:18432
v_cvt_pk_bf16_f32 v33, v34, v35
v_mfma_f32_16x16x32_bf16 v[56:59], a[156:159], v[112:115], v[56:59]
ds_read_b64_tr_b16 a[152:153], v224 offset:20480
v_cvt_pk_bf16_f32 v34, v40, v41
v_mfma_f32_16x16x32_bf16 v[60:63], a[156:159], v[116:119], v[60:63]
s_waitcnt lgkmcnt(5)
ds_read_b64_tr_b16 a[154:155], v224 offset:22528
v_cvt_pk_bf16_f32 v35, v42, v43
v_mfma_f32_16x16x32_bf16 v[48:51], a[128:131], v[128:131], v[48:51]
ds_read_b64_tr_b16 a[156:157], v225 offset:20480
ds_read_b64_tr_b16 a[158:159], v225 offset:22528
v_mfma_f32_16x16x32_bf16 v[52:55], a[128:131], v[132:135], v[52:55]
buffer_load_dwordx4 v93, s[48:51], s71 offen offset:3072 lds
v_mfma_f32_16x16x32_bf16 v[56:59], a[132:135], v[128:131], v[56:59]
ds_read_b64_tr_b16 a[128:129], v224 offset:24576
buffer_store_dwordx4 v[32:35], v252, s[64:67], s81 offen offset:1024 sc1
v_mfma_f32_16x16x32_bf16 v[60:63], a[132:135], v[132:135], v[60:63]
ds_read_b64_tr_b16 a[130:131], v224 offset:26624
v_cvt_pk_bf16_f32 v36, v36, v37
v_mfma_f32_16x16x32_bf16 v[48:51], a[136:139], v[144:147], v[48:51]
ds_read_b64_tr_b16 a[132:133], v225 offset:24576
v_cvt_pk_bf16_f32 v37, v38, v39
v_mfma_f32_16x16x32_bf16 v[52:55], a[136:139], v[148:151], v[52:55]
s_add_u32 s71, s74, 229376
ds_read_b64_tr_b16 a[134:135], v225 offset:26624
v_cvt_pk_bf16_f32 v38, v44, v45
v_mfma_f32_16x16x32_bf16 v[56:59], a[140:143], v[144:147], v[56:59]
s_add_u32 m0, s79, 69632
ds_read_b64_tr_b16 a[136:137], v224 offset:28672
ds_read_b64_tr_b16 a[138:139], v224 offset:30720
v_mfma_f32_16x16x32_bf16 v[60:63], a[140:143], v[148:151], v[60:63]
s_waitcnt lgkmcnt(6)
buffer_load_dwordx4 v90, s[48:51], s71 offen offset:0 lds
v_mfma_f32_16x16x32_bf16 v[48:51], a[144:147], v[160:163], v[48:51]
ds_read_b64_tr_b16 a[140:141], v225 offset:28672
v_cvt_pk_bf16_f32 v39, v46, v47
v_mfma_f32_16x16x32_bf16 v[52:55], a[144:147], v[164:167], v[52:55]
ds_read_b64_tr_b16 a[142:143], v225 offset:30720
buffer_store_dwordx4 v[36:39], v253, s[64:67], s81 offen offset:1024 sc1
v_mfma_f32_16x16x32_bf16 v[56:59], a[148:151], v[160:163], v[56:59]
s_add_u32 s81, s80, 675840
ds_read_b64_tr_b16 a[144:145], v226 offset:0
v_add_u32 v224, 0xfffe0000, v224
v_mfma_f32_16x16x32_bf16 v[60:63], a[148:151], v[164:167], v[60:63]
ds_read_b64_tr_b16 a[146:147], v226 offset:2048
v_add_u32 v225, 0xfffe0000, v225
v_mfma_f32_16x16x32_bf16 v[48:51], a[152:155], v[176:179], v[48:51]
ds_read_b64_tr_b16 a[148:149], v227 offset:0
ds_read_b64_tr_b16 a[150:151], v227 offset:2048
v_mfma_f32_16x16x32_bf16 v[52:55], a[152:155], v[180:183], v[52:55]
buffer_load_dwordx4 v91, s[48:51], s71 offen offset:1024 lds
v_mfma_f32_16x16x32_bf16 v[56:59], a[156:159], v[176:179], v[56:59]
ds_read_b64_tr_b16 a[152:153], v226 offset:4096
v_mfma_f32_16x16x32_bf16 v[60:63], a[156:159], v[180:183], v[60:63]
s_waitcnt lgkmcnt(5)
ds_read_b64_tr_b16 a[154:155], v226 offset:6144
v_mfma_f32_16x16x32_bf16 v[48:51], a[128:131], v[192:195], v[48:51]
ds_read_b64_tr_b16 a[156:157], v227 offset:4096
v_mfma_f32_16x16x32_bf16 v[52:55], a[128:131], v[196:199], v[52:55]
ds_read_b64_tr_b16 a[158:159], v227 offset:6144
v_mfma_f32_16x16x32_bf16 v[56:59], a[132:135], v[192:195], v[56:59]
ds_read_b64_tr_b16 a[128:129], v226 offset:8192
ds_read_b64_tr_b16 a[130:131], v226 offset:10240
v_mfma_f32_16x16x32_bf16 v[60:63], a[132:135], v[196:199], v[60:63]
buffer_load_dwordx4 v92, s[48:51], s71 offen offset:2048 lds
v_mfma_f32_16x16x32_bf16 v[48:51], a[136:139], v[208:211], v[48:51]
ds_read_b64_tr_b16 a[132:133], v227 offset:8192
v_mfma_f32_16x16x32_bf16 v[52:55], a[136:139], v[212:215], v[52:55]
ds_read_b64_tr_b16 a[134:135], v227 offset:10240
v_mfma_f32_16x16x32_bf16 v[56:59], a[140:143], v[208:211], v[56:59]
s_add_u32 s71, s25, 0
ds_read_b64_tr_b16 a[136:137], v226 offset:12288
v_mfma_f32_16x16x32_bf16 v[60:63], a[140:143], v[212:215], v[60:63]
s_waitcnt lgkmcnt(5)
ds_read_b64_tr_b16 a[138:139], v226 offset:14336
v_mfma_f32_16x16x32_bf16 v[64:67], a[144:147], v[96:99], 0
s_add_u32 m0, s24, 0
ds_read_b64_tr_b16 a[140:141], v227 offset:12288
ds_read_b64_tr_b16 a[142:143], v227 offset:14336
v_mfma_f32_16x16x32_bf16 v[68:71], a[144:147], v[100:103], 0
buffer_load_dwordx4 v90, s[36:39], s71 offen offset:0 lds
v_mfma_f32_16x16x32_bf16 v[72:75], a[148:151], v[96:99], 0
ds_read_b64_tr_b16 a[144:145], v226 offset:16384
v_mfma_f32_16x16x32_bf16 v[76:79], a[148:151], v[100:103], 0
ds_read_b64_tr_b16 a[146:147], v226 offset:18432
v_cvt_pk_bf16_f32 v48, v48, v49
v_mfma_f32_16x16x32_bf16 v[64:67], a[152:155], v[112:115], v[64:67]
ds_read_b64_tr_b16 a[148:149], v227 offset:16384
v_cvt_pk_bf16_f32 v49, v50, v51
v_mfma_f32_16x16x32_bf16 v[68:71], a[152:155], v[116:119], v[68:71]
ds_read_b64_tr_b16 a[150:151], v227 offset:18432
v_cvt_pk_bf16_f32 v50, v56, v57
v_mfma_f32_16x16x32_bf16 v[72:75], a[156:159], v[112:115], v[72:75]
ds_read_b64_tr_b16 a[152:153], v226 offset:20480
ds_read_b64_tr_b16 a[154:155], v226 offset:22528
v_mfma_f32_16x16x32_bf16 v[76:79], a[156:159], v[116:119], v[76:79]
s_waitcnt lgkmcnt(6)
buffer_load_dwordx4 v91, s[36:39], s71 offen offset:1024 lds
v_mfma_f32_16x16x32_bf16 v[64:67], a[128:131], v[128:131], v[64:67]
ds_read_b64_tr_b16 a[156:157], v227 offset:20480
v_cvt_pk_bf16_f32 v51, v58, v59
v_mfma_f32_16x16x32_bf16 v[68:71], a[128:131], v[132:135], v[68:71]
ds_read_b64_tr_b16 a[158:159], v227 offset:22528
buffer_store_dwordx4 v[48:51], v252, s[64:67], s81 offen offset:0 sc1
v_mfma_f32_16x16x32_bf16 v[72:75], a[132:135], v[128:131], v[72:75]
ds_read_b64_tr_b16 a[128:129], v226 offset:24576
v_cvt_pk_bf16_f32 v52, v52, v53
v_mfma_f32_16x16x32_bf16 v[76:79], a[132:135], v[132:135], v[76:79]
ds_read_b64_tr_b16 a[130:131], v226 offset:26624
v_cvt_pk_bf16_f32 v53, v54, v55
v_mfma_f32_16x16x32_bf16 v[64:67], a[136:139], v[144:147], v[64:67]
ds_read_b64_tr_b16 a[132:133], v227 offset:24576
v_cvt_pk_bf16_f32 v54, v60, v61
v_mfma_f32_16x16x32_bf16 v[68:71], a[136:139], v[148:151], v[68:71]
ds_read_b64_tr_b16 a[134:135], v227 offset:26624
v_cvt_pk_bf16_f32 v55, v62, v63
v_mfma_f32_16x16x32_bf16 v[72:75], a[140:143], v[144:147], v[72:75]
ds_read_b64_tr_b16 a[136:137], v226 offset:28672
buffer_store_dwordx4 v[52:55], v253, s[64:67], s81 offen offset:0 sc1
v_mfma_f32_16x16x32_bf16 v[76:79], a[140:143], v[148:151], v[76:79]
s_waitcnt lgkmcnt(5)
ds_read_b64_tr_b16 a[138:139], v226 offset:30720
v_add_u32 v226, 0xfffe0000, v226
v_mfma_f32_16x16x32_bf16 v[64:67], a[144:147], v[160:163], v[64:67]
ds_read_b64_tr_b16 a[140:141], v227 offset:28672
v_mfma_f32_16x16x32_bf16 v[68:71], a[144:147], v[164:167], v[68:71]
s_waitcnt vmcnt(26)
ds_read_b64_tr_b16 a[142:143], v227 offset:30720
v_add_u32 v227, 0xfffe0000, v227
v_mfma_f32_16x16x32_bf16 v[72:75], a[148:151], v[160:163], v[72:75]
s_barrier
ds_read_b64_tr_b16 a[144:145], v224 offset:0
v_mfma_f32_16x16x32_bf16 v[76:79], a[148:151], v[164:167], v[76:79]
ds_read_b64_tr_b16 a[146:147], v224 offset:2048
v_mfma_f32_16x16x32_bf16 v[64:67], a[152:155], v[176:179], v[64:67]
ds_read_b64_tr_b16 a[148:149], v225 offset:0
v_mfma_f32_16x16x32_bf16 v[68:71], a[152:155], v[180:183], v[68:71]
s_add_u32 s71, s74, 401408
ds_read_b64_tr_b16 a[150:151], v225 offset:2048
v_mfma_f32_16x16x32_bf16 v[72:75], a[156:159], v[176:179], v[72:75]
s_add_u32 m0, s79, 98304
ds_read_b64_tr_b16 a[152:153], v224 offset:4096
ds_read_b64_tr_b16 a[154:155], v224 offset:6144
v_mfma_f32_16x16x32_bf16 v[76:79], a[156:159], v[180:183], v[76:79]
s_waitcnt lgkmcnt(6)
buffer_load_dwordx4 v90, s[48:51], s71 offen offset:0 lds
v_mfma_f32_16x16x32_bf16 v[64:67], a[128:131], v[192:195], v[64:67]
ds_read_b64_tr_b16 a[156:157], v225 offset:4096
v_mfma_f32_16x16x32_bf16 v[68:71], a[128:131], v[196:199], v[68:71]
ds_read_b64_tr_b16 a[158:159], v225 offset:6144
v_mfma_f32_16x16x32_bf16 v[72:75], a[132:135], v[192:195], v[72:75]
ds_read_b64_tr_b16 a[128:129], v224 offset:8192
v_mfma_f32_16x16x32_bf16 v[76:79], a[132:135], v[196:199], v[76:79]
ds_read_b64_tr_b16 a[130:131], v224 offset:10240
v_mfma_f32_16x16x32_bf16 v[64:67], a[136:139], v[208:211], v[64:67]
ds_read_b64_tr_b16 a[132:133], v225 offset:8192
ds_read_b64_tr_b16 a[134:135], v225 offset:10240
v_mfma_f32_16x16x32_bf16 v[68:71], a[136:139], v[212:215], v[68:71]
buffer_load_dwordx4 v91, s[48:51], s71 offen offset:1024 lds
v_mfma_f32_16x16x32_bf16 v[72:75], a[140:143], v[208:211], v[72:75]
ds_read_b64_tr_b16 a[136:137], v224 offset:12288
v_mfma_f32_16x16x32_bf16 v[76:79], a[140:143], v[212:215], v[76:79]
s_waitcnt lgkmcnt(5)
ds_read_b64_tr_b16 a[138:139], v224 offset:14336
v_mfma_f32_16x16x32_bf16 v[32:35], a[144:147], v[96:99], 0
ds_read_b64_tr_b16 a[140:141], v225 offset:12288
v_mfma_f32_16x16x32_bf16 v[36:39], a[144:147], v[100:103], 0
ds_read_b64_tr_b16 a[142:143], v225 offset:14336
v_mfma_f32_16x16x32_bf16 v[40:43], a[148:151], v[96:99], 0
ds_read_b64_tr_b16 a[144:145], v224 offset:16384
ds_read_b64_tr_b16 a[146:147], v224 offset:18432
v_mfma_f32_16x16x32_bf16 v[44:47], a[148:151], v[100:103], 0
buffer_load_dwordx4 v92, s[48:51], s71 offen offset:2048 lds
v_mfma_f32_16x16x32_bf16 v[32:35], a[152:155], v[112:115], v[32:35]
ds_read_b64_tr_b16 a[148:149], v225 offset:16384
v_cvt_pk_bf16_f32 v64, v64, v65
v_mfma_f32_16x16x32_bf16 v[36:39], a[152:155], v[116:119], v[36:39]
ds_read_b64_tr_b16 a[150:151], v225 offset:18432
v_cvt_pk_bf16_f32 v65, v66, v67
v_mfma_f32_16x16x32_bf16 v[40:43], a[156:159], v[112:115], v[40:43]
ds_read_b64_tr_b16 a[152:153], v224 offset:20480
v_cvt_pk_bf16_f32 v66, v72, v73
v_mfma_f32_16x16x32_bf16 v[44:47], a[156:159], v[116:119], v[44:47]
s_waitcnt lgkmcnt(5)
ds_read_b64_tr_b16 a[154:155], v224 offset:22528
v_cvt_pk_bf16_f32 v67, v74, v75
v_mfma_f32_16x16x32_bf16 v[32:35], a[128:131], v[128:131], v[32:35]
ds_read_b64_tr_b16 a[156:157], v225 offset:20480
ds_read_b64_tr_b16 a[158:159], v225 offset:22528
v_mfma_f32_16x16x32_bf16 v[36:39], a[128:131], v[132:135], v[36:39]
buffer_load_dwordx4 v93, s[48:51], s71 offen offset:3072 lds
v_mfma_f32_16x16x32_bf16 v[40:43], a[132:135], v[128:131], v[40:43]
ds_read_b64_tr_b16 a[128:129], v224 offset:24576
buffer_store_dwordx4 v[64:67], v252, s[64:67], s81 offen offset:1024 sc1
v_mfma_f32_16x16x32_bf16 v[44:47], a[132:135], v[132:135], v[44:47]
ds_read_b64_tr_b16 a[130:131], v224 offset:26624
v_cvt_pk_bf16_f32 v68, v68, v69
v_mfma_f32_16x16x32_bf16 v[32:35], a[136:139], v[144:147], v[32:35]
ds_read_b64_tr_b16 a[132:133], v225 offset:24576
v_cvt_pk_bf16_f32 v69, v70, v71
v_mfma_f32_16x16x32_bf16 v[36:39], a[136:139], v[148:151], v[36:39]
s_add_u32 s71, s74, 430080
ds_read_b64_tr_b16 a[134:135], v225 offset:26624
v_cvt_pk_bf16_f32 v70, v76, v77
v_mfma_f32_16x16x32_bf16 v[40:43], a[140:143], v[144:147], v[40:43]
s_add_u32 m0, s79, 102400
ds_read_b64_tr_b16 a[136:137], v224 offset:28672
ds_read_b64_tr_b16 a[138:139], v224 offset:30720
v_mfma_f32_16x16x32_bf16 v[44:47], a[140:143], v[148:151], v[44:47]
s_waitcnt lgkmcnt(6)
buffer_load_dwordx4 v90, s[48:51], s71 offen offset:0 lds
v_mfma_f32_16x16x32_bf16 v[32:35], a[144:147], v[160:163], v[32:35]
ds_read_b64_tr_b16 a[140:141], v225 offset:28672
v_cvt_pk_bf16_f32 v71, v78, v79
v_mfma_f32_16x16x32_bf16 v[36:39], a[144:147], v[164:167], v[36:39]
ds_read_b64_tr_b16 a[142:143], v225 offset:30720
buffer_store_dwordx4 v[68:71], v253, s[64:67], s81 offen offset:1024 sc1
v_mfma_f32_16x16x32_bf16 v[40:43], a[148:151], v[160:163], v[40:43]
s_add_u32 s81, s80, 811008
ds_read_b64_tr_b16 a[144:145], v226 offset:0
v_mfma_f32_16x16x32_bf16 v[44:47], a[148:151], v[164:167], v[44:47]
ds_read_b64_tr_b16 a[146:147], v226 offset:2048
v_mfma_f32_16x16x32_bf16 v[32:35], a[152:155], v[176:179], v[32:35]
ds_read_b64_tr_b16 a[148:149], v227 offset:0
ds_read_b64_tr_b16 a[150:151], v227 offset:2048
v_mfma_f32_16x16x32_bf16 v[36:39], a[152:155], v[180:183], v[36:39]
buffer_load_dwordx4 v91, s[48:51], s71 offen offset:1024 lds
v_mfma_f32_16x16x32_bf16 v[40:43], a[156:159], v[176:179], v[40:43]
ds_read_b64_tr_b16 a[152:153], v226 offset:4096
v_mfma_f32_16x16x32_bf16 v[44:47], a[156:159], v[180:183], v[44:47]
s_waitcnt lgkmcnt(5)
ds_read_b64_tr_b16 a[154:155], v226 offset:6144
v_mfma_f32_16x16x32_bf16 v[32:35], a[128:131], v[192:195], v[32:35]
ds_read_b64_tr_b16 a[156:157], v227 offset:4096
v_mfma_f32_16x16x32_bf16 v[36:39], a[128:131], v[196:199], v[36:39]
ds_read_b64_tr_b16 a[158:159], v227 offset:6144
v_mfma_f32_16x16x32_bf16 v[40:43], a[132:135], v[192:195], v[40:43]
ds_read_b64_tr_b16 a[128:129], v226 offset:8192
ds_read_b64_tr_b16 a[130:131], v226 offset:10240
v_mfma_f32_16x16x32_bf16 v[44:47], a[132:135], v[196:199], v[44:47]
buffer_load_dwordx4 v92, s[48:51], s71 offen offset:2048 lds
v_mfma_f32_16x16x32_bf16 v[32:35], a[136:139], v[208:211], v[32:35]
ds_read_b64_tr_b16 a[132:133], v227 offset:8192
v_mfma_f32_16x16x32_bf16 v[36:39], a[136:139], v[212:215], v[36:39]
ds_read_b64_tr_b16 a[134:135], v227 offset:10240
v_mfma_f32_16x16x32_bf16 v[40:43], a[140:143], v[208:211], v[40:43]
ds_read_b64_tr_b16 a[136:137], v226 offset:12288
v_mfma_f32_16x16x32_bf16 v[44:47], a[140:143], v[212:215], v[44:47]
s_waitcnt lgkmcnt(5)
ds_read_b64_tr_b16 a[138:139], v226 offset:14336
v_mfma_f32_16x16x32_bf16 v[48:51], a[144:147], v[96:99], 0
ds_read_b64_tr_b16 a[140:141], v227 offset:12288
v_mfma_f32_16x16x32_bf16 v[52:55], a[144:147], v[100:103], 0
ds_read_b64_tr_b16 a[142:143], v227 offset:14336
v_mfma_f32_16x16x32_bf16 v[56:59], a[148:151], v[96:99], 0
ds_read_b64_tr_b16 a[144:145], v226 offset:16384
v_mfma_f32_16x16x32_bf16 v[60:63], a[148:151], v[100:103], 0
ds_read_b64_tr_b16 a[146:147], v226 offset:18432
v_cvt_pk_bf16_f32 v32, v32, v33
v_mfma_f32_16x16x32_bf16 v[48:51], a[152:155], v[112:115], v[48:51]
ds_read_b64_tr_b16 a[148:149], v227 offset:16384
v_cvt_pk_bf16_f32 v33, v34, v35
v_mfma_f32_16x16x32_bf16 v[52:55], a[152:155], v[116:119], v[52:55]
ds_read_b64_tr_b16 a[150:151], v227 offset:18432
v_cvt_pk_bf16_f32 v34, v40, v41
v_mfma_f32_16x16x32_bf16 v[56:59], a[156:159], v[112:115], v[56:59]
ds_read_b64_tr_b16 a[152:153], v226 offset:20480
v_cvt_pk_bf16_f32 v35, v42, v43
v_mfma_f32_16x16x32_bf16 v[60:63], a[156:159], v[116:119], v[60:63]
s_waitcnt lgkmcnt(5)
ds_read_b64_tr_b16 a[154:155], v226 offset:22528
buffer_store_dwordx4 v[32:35], v252, s[64:67], s81 offen offset:0 sc1
v_mfma_f32_16x16x32_bf16 v[48:51], a[128:131], v[128:131], v[48:51]
ds_read_b64_tr_b16 a[156:157], v227 offset:20480
v_cvt_pk_bf16_f32 v36, v36, v37
v_mfma_f32_16x16x32_bf16 v[52:55], a[128:131], v[132:135], v[52:55]
ds_read_b64_tr_b16 a[158:159], v227 offset:22528
v_cvt_pk_bf16_f32 v37, v38, v39
v_mfma_f32_16x16x32_bf16 v[56:59], a[132:135], v[128:131], v[56:59]
ds_read_b64_tr_b16 a[128:129], v226 offset:24576
v_cvt_pk_bf16_f32 v38, v44, v45
v_mfma_f32_16x16x32_bf16 v[60:63], a[132:135], v[132:135], v[60:63]
ds_read_b64_tr_b16 a[130:131], v226 offset:26624
v_cvt_pk_bf16_f32 v39, v46, v47
v_mfma_f32_16x16x32_bf16 v[48:51], a[136:139], v[144:147], v[48:51]
ds_read_b64_tr_b16 a[132:133], v227 offset:24576
buffer_store_dwordx4 v[36:39], v253, s[64:67], s81 offen offset:0 sc1
v_mfma_f32_16x16x32_bf16 v[52:55], a[136:139], v[148:151], v[52:55]
ds_read_b64_tr_b16 a[134:135], v227 offset:26624
v_mfma_f32_16x16x32_bf16 v[56:59], a[140:143], v[144:147], v[56:59]
ds_read_b64_tr_b16 a[136:137], v226 offset:28672
v_mfma_f32_16x16x32_bf16 v[60:63], a[140:143], v[148:151], v[60:63]
s_waitcnt lgkmcnt(5)
ds_read_b64_tr_b16 a[138:139], v226 offset:30720
v_mfma_f32_16x16x32_bf16 v[48:51], a[144:147], v[160:163], v[48:51]
ds_read_b64_tr_b16 a[140:141], v227 offset:28672
v_mfma_f32_16x16x32_bf16 v[52:55], a[144:147], v[164:167], v[52:55]
s_waitcnt vmcnt(26)
ds_read_b64_tr_b16 a[142:143], v227 offset:30720
v_mfma_f32_16x16x32_bf16 v[56:59], a[148:151], v[160:163], v[56:59]
s_barrier
ds_read_b64_tr_b16 a[144:145], v224 offset:32768
v_mfma_f32_16x16x32_bf16 v[60:63], a[148:151], v[164:167], v[60:63]
ds_read_b64_tr_b16 a[146:147], v224 offset:34816
v_mfma_f32_16x16x32_bf16 v[48:51], a[152:155], v[176:179], v[48:51]
ds_read_b64_tr_b16 a[148:149], v225 offset:32768
v_mfma_f32_16x16x32_bf16 v[52:55], a[152:155], v[180:183], v[52:55]
s_add_u32 s71, s74, 602112
ds_read_b64_tr_b16 a[150:151], v225 offset:34816
v_mfma_f32_16x16x32_bf16 v[56:59], a[156:159], v[176:179], v[56:59]
s_add_u32 m0, s79, 131072
ds_read_b64_tr_b16 a[152:153], v226 offset:32768
v_mfma_f32_16x16x32_bf16 v[60:63], a[156:159], v[180:183], v[60:63]
s_waitcnt lgkmcnt(5)
ds_read_b64_tr_b16 a[154:155], v226 offset:34816
buffer_load_dwordx4 v90, s[48:51], s71 offen offset:0 lds
v_mfma_f32_16x16x32_bf16 v[48:51], a[128:131], v[192:195], v[48:51]
ds_read_b64_tr_b16 a[156:157], v227 offset:32768
v_mfma_f32_16x16x32_bf16 v[52:55], a[128:131], v[196:199], v[52:55]
ds_read_b64_tr_b16 a[158:159], v227 offset:34816
v_mfma_f32_16x16x32_bf16 v[56:59], a[132:135], v[192:195], v[56:59]
ds_read_b64_tr_b16 a[128:129], v224 offset:36864
v_mfma_f32_16x16x32_bf16 v[60:63], a[132:135], v[196:199], v[60:63]
ds_read_b64_tr_b16 a[130:131], v224 offset:38912
buffer_load_dwordx4 v91, s[48:51], s71 offen offset:1024 lds
v_mfma_f32_16x16x32_bf16 v[48:51], a[136:139], v[208:211], v[48:51]
ds_read_b64_tr_b16 a[132:133], v225 offset:36864
v_mfma_f32_16x16x32_bf16 v[52:55], a[136:139], v[212:215], v[52:55]
ds_read_b64_tr_b16 a[134:135], v225 offset:38912
v_mfma_f32_16x16x32_bf16 v[56:59], a[140:143], v[208:211], v[56:59]
ds_read_b64_tr_b16 a[136:137], v226 offset:36864
v_mfma_f32_16x16x32_bf16 v[60:63], a[140:143], v[212:215], v[60:63]
s_waitcnt lgkmcnt(5)
ds_read_b64_tr_b16 a[138:139], v226 offset:38912
buffer_load_dwordx4 v92, s[48:51], s71 offen offset:2048 lds
v_mfma_f32_16x16x32_bf16 v[64:67], a[144:147], a[8:11], 0
ds_read_b64_tr_b16 a[140:141], v227 offset:36864
ds_read_b64_tr_b16 a[142:143], v227 offset:38912
v_mfma_f32_16x16x32_bf16 v[68:71], a[148:151], a[8:11], 0
s_barrier
ds_read_b64_tr_b16 a[144:145], v224 offset:40960
ds_read_b64_tr_b16 a[146:147], v224 offset:43008
v_mfma_f32_16x16x32_bf16 v[72:75], a[152:155], a[8:11], 0
ds_read_b64_tr_b16 a[148:149], v225 offset:40960
ds_read_b64_tr_b16 a[150:151], v225 offset:43008
v_mfma_f32_16x16x32_bf16 v[76:79], a[156:159], a[8:11], 0
s_waitcnt lgkmcnt(4)
ds_read_b64_tr_b16 a[152:153], v226 offset:40960
ds_read_b64_tr_b16 a[154:155], v226 offset:43008
buffer_load_dwordx4 v93, s[48:51], s71 offen offset:3072 lds
v_mfma_f32_16x16x32_bf16 v[64:67], a[128:131], a[12:15], v[64:67]
ds_read_b64_tr_b16 a[156:157], v227 offset:40960
ds_read_b64_tr_b16 a[158:159], v227 offset:43008
v_mfma_f32_16x16x32_bf16 v[68:71], a[132:135], a[12:15], v[68:71]
s_add_u32 s71, s74, 630784
ds_read_b64_tr_b16 a[128:129], v224 offset:47104
ds_read_b64_tr_b16 a[130:131], v224 offset:49152
v_mfma_f32_16x16x32_bf16 v[72:75], a[136:139], a[12:15], v[72:75]
s_add_u32 m0, s79, 135168
ds_read_b64_tr_b16 a[132:133], v225 offset:47104
ds_read_b64_tr_b16 a[134:135], v225 offset:49152
v_mfma_f32_16x16x32_bf16 v[76:79], a[140:143], a[12:15], v[76:79]
s_waitcnt lgkmcnt(4)
ds_read_b64_tr_b16 a[136:137], v226 offset:47104
ds_read_b64_tr_b16 a[138:139], v226 offset:49152
buffer_load_dwordx4 v90, s[48:51], s71 offen offset:0 lds
v_mfma_f32_16x16x32_bf16 v[64:67], a[144:147], a[24:27], v[64:67]
ds_read_b64_tr_b16 a[140:141], v227 offset:47104
ds_read_b64_tr_b16 a[142:143], v227 offset:49152
v_mfma_f32_16x16x32_bf16 v[68:71], a[148:151], a[24:27], v[68:71]
ds_read_b64_tr_b16 a[144:145], v224 offset:51200
ds_read_b64_tr_b16 a[146:147], v224 offset:53248
v_cvt_pk_bf16_f32 v48, v48, v49
v_cvt_pk_bf16_f32 v49, v50, v51
v_cvt_pk_bf16_f32 v50, v56, v57
v_cvt_pk_bf16_f32 v51, v58, v59
buffer_store_dwordx4 v[48:51], v252, s[64:67], s81 offen offset:1024 sc1
v_cvt_pk_bf16_f32 v52, v52, v53
v_cvt_pk_bf16_f32 v53, v54, v55
v_cvt_pk_bf16_f32 v54, v60, v61
v_cvt_pk_bf16_f32 v55, v62, v63
buffer_store_dwordx4 v[52:55], v253, s[64:67], s81 offen offset:1024 sc1
v_mfma_f32_16x16x32_bf16 v[72:75], a[152:155], a[24:27], v[72:75]
ds_read_b64_tr_b16 a[148:149], v225 offset:51200
ds_read_b64_tr_b16 a[150:151], v225 offset:53248
v_mfma_f32_16x16x32_bf16 v[76:79], a[156:159], a[24:27], v[76:79]
s_waitcnt lgkmcnt(4)
ds_read_b64_tr_b16 a[152:153], v226 offset:51200
ds_read_b64_tr_b16 a[154:155], v226 offset:53248
buffer_load_dwordx4 v91, s[48:51], s71 offen offset:1024 lds
v_mfma_f32_16x16x32_bf16 v[64:67], a[128:131], a[28:31], v[64:67]
ds_read_b64_tr_b16 a[156:157], v227 offset:51200
ds_read_b64_tr_b16 a[158:159], v227 offset:53248
v_mfma_f32_16x16x32_bf16 v[68:71], a[132:135], a[28:31], v[68:71]
ds_read_b64_tr_b16 a[128:129], v224 offset:55296
ds_read_b64_tr_b16 a[130:131], v224 offset:57344
v_mfma_f32_16x16x32_bf16 v[72:75], a[136:139], a[28:31], v[72:75]
ds_read_b64_tr_b16 a[132:133], v225 offset:55296
ds_read_b64_tr_b16 a[134:135], v225 offset:57344
v_mfma_f32_16x16x32_bf16 v[76:79], a[140:143], a[28:31], v[76:79]
s_waitcnt lgkmcnt(4)
ds_read_b64_tr_b16 a[136:137], v226 offset:55296
ds_read_b64_tr_b16 a[138:139], v226 offset:57344
buffer_load_dwordx4 v92, s[48:51], s71 offen offset:2048 lds
v_mfma_f32_16x16x32_bf16 v[64:67], a[144:147], a[40:43], v[64:67]
ds_read_b64_tr_b16 a[140:141], v227 offset:55296
ds_read_b64_tr_b16 a[142:143], v227 offset:57344
v_mfma_f32_16x16x32_bf16 v[68:71], a[148:151], a[40:43], v[68:71]
s_add_u32 s71, s74, 802816
ds_read_b64_tr_b16 a[144:145], v224 offset:45056
ds_read_b64_tr_b16 a[146:147], v224 offset:59392
v_mfma_f32_16x16x32_bf16 v[72:75], a[152:155], a[40:43], v[72:75]
s_add_u32 m0, s79, 0
ds_read_b64_tr_b16 a[148:149], v225 offset:45056
ds_read_b64_tr_b16 a[150:151], v225 offset:59392
v_mfma_f32_16x16x32_bf16 v[76:79], a[156:159], a[40:43], v[76:79]
s_waitcnt lgkmcnt(4)
ds_read_b64_tr_b16 a[152:153], v226 offset:45056
ds_read_b64_tr_b16 a[154:155], v226 offset:59392
v_mfma_f32_16x16x32_bf16 v[64:67], a[128:131], a[44:47], v[64:67]
s_waitcnt vmcnt(22)
ds_read_b64_tr_b16 a[156:157], v227 offset:45056
ds_read_b64_tr_b16 a[158:159], v227 offset:59392
v_add_u32 v224, 0x10000, v224
v_mfma_f32_16x16x32_bf16 v[68:71], a[132:135], a[44:47], v[68:71]
s_barrier
ds_read_b64_tr_b16 a[128:129], v224 offset:0
ds_read_b64_tr_b16 a[130:131], v224 offset:2048
buffer_load_dwordx4 v90, s[48:51], s71 offen offset:0 lds
v_add_u32 v225, 0x10000, v225
v_mfma_f32_16x16x32_bf16 v[72:75], a[136:139], a[44:47], v[72:75]
ds_read_b64_tr_b16 a[132:133], v225 offset:0
ds_read_b64_tr_b16 a[134:135], v225 offset:2048
v_add_u32 v226, 0x10000, v226
v_mfma_f32_16x16x32_bf16 v[76:79], a[140:143], a[44:47], v[76:79]
s_waitcnt lgkmcnt(4)
ds_read_b64_tr_b16 a[136:137], v226 offset:0
ds_read_b64_tr_b16 a[138:139], v226 offset:2048
buffer_load_dwordx4 v91, s[48:51], s71 offen offset:1024 lds
v_add_u32 v227, 0x10000, v227
v_mfma_f32_16x16x32_bf16 v[64:67], a[144:147], a[56:59], v[64:67]
ds_read_b64_tr_b16 a[140:141], v227 offset:0
ds_read_b64_tr_b16 a[142:143], v227 offset:2048
v_mfma_f32_16x16x32_bf16 v[68:71], a[148:151], a[56:59], v[68:71]
ds_read_b64_tr_b16 a[144:145], v224 offset:4096
ds_read_b64_tr_b16 a[146:147], v224 offset:6144
buffer_load_dwordx4 v92, s[48:51], s71 offen offset:2048 lds
v_mfma_f32_16x16x32_bf16 v[72:75], a[152:155], a[56:59], v[72:75]
ds_read_b64_tr_b16 a[148:149], v225 offset:4096
ds_read_b64_tr_b16 a[150:151], v225 offset:6144
v_mfma_f32_16x16x32_bf16 v[76:79], a[156:159], a[56:59], v[76:79]
s_waitcnt lgkmcnt(4)
ds_read_b64_tr_b16 a[152:153], v226 offset:4096
ds_read_b64_tr_b16 a[154:155], v226 offset:6144
buffer_load_dwordx4 v93, s[48:51], s71 offen offset:3072 lds
v_mfma_f32_16x16x32_bf16 v[64:67], a[128:131], a[60:63], v[64:67]
s_add_u32 m0, s79, 4096
s_add_u32 s71, s74, 831488
ds_read_b64_tr_b16 a[156:157], v227 offset:4096
ds_read_b64_tr_b16 a[158:159], v227 offset:6144
v_mfma_f32_16x16x32_bf16 v[68:71], a[132:135], a[60:63], v[68:71]
s_barrier
ds_read_b64_tr_b16 a[128:129], v224 offset:8192
ds_read_b64_tr_b16 a[130:131], v224 offset:10240
buffer_load_dwordx4 v90, s[48:51], s71 offen offset:0 lds
v_mfma_f32_16x16x32_bf16 v[72:75], a[136:139], a[60:63], v[72:75]
ds_read_b64_tr_b16 a[132:133], v225 offset:8192
ds_read_b64_tr_b16 a[134:135], v225 offset:10240
v_mfma_f32_16x16x32_bf16 v[76:79], a[140:143], a[60:63], v[76:79]
s_waitcnt lgkmcnt(4)
ds_read_b64_tr_b16 a[136:137], v226 offset:8192
ds_read_b64_tr_b16 a[138:139], v226 offset:10240
buffer_load_dwordx4 v91, s[48:51], s71 offen offset:1024 lds
v_mfma_f32_16x16x32_bf16 v[64:67], a[144:147], a[72:75], v[64:67]
ds_read_b64_tr_b16 a[140:141], v227 offset:8192
ds_read_b64_tr_b16 a[142:143], v227 offset:10240
v_mfma_f32_16x16x32_bf16 v[68:71], a[148:151], a[72:75], v[68:71]
ds_read_b64_tr_b16 a[144:145], v224 offset:14336
ds_read_b64_tr_b16 a[146:147], v224 offset:16384
buffer_load_dwordx4 v92, s[48:51], s71 offen offset:2048 lds
v_mfma_f32_16x16x32_bf16 v[72:75], a[152:155], a[72:75], v[72:75]
ds_read_b64_tr_b16 a[148:149], v225 offset:14336
ds_read_b64_tr_b16 a[150:151], v225 offset:16384
v_mfma_f32_16x16x32_bf16 v[76:79], a[156:159], a[72:75], v[76:79]
s_waitcnt lgkmcnt(4)
ds_read_b64_tr_b16 a[152:153], v226 offset:14336
ds_read_b64_tr_b16 a[154:155], v226 offset:16384
v_mfma_f32_16x16x32_bf16 v[64:67], a[128:131], a[76:79], v[64:67]
ds_read_b64_tr_b16 a[156:157], v227 offset:14336
ds_read_b64_tr_b16 a[158:159], v227 offset:16384
v_mfma_f32_16x16x32_bf16 v[68:71], a[132:135], a[76:79], v[68:71]
s_add_u32 s71, s74, 1003520
ds_read_b64_tr_b16 a[128:129], v224 offset:18432
ds_read_b64_tr_b16 a[130:131], v224 offset:20480
v_mfma_f32_16x16x32_bf16 v[72:75], a[136:139], a[76:79], v[72:75]
s_add_u32 m0, s79, 32768
ds_read_b64_tr_b16 a[132:133], v225 offset:18432
ds_read_b64_tr_b16 a[134:135], v225 offset:20480
v_mfma_f32_16x16x32_bf16 v[76:79], a[140:143], a[76:79], v[76:79]
s_waitcnt vmcnt(9) lgkmcnt(4)
ds_read_b64_tr_b16 a[136:137], v226 offset:18432
ds_read_b64_tr_b16 a[138:139], v226 offset:20480
buffer_load_dwordx4 v90, s[48:51], s71 offen offset:0 lds
buffer_store_dword v233, v235, s[64:67], s83 offen offset:4 sc1
s_cmp_ge_u32 s98, 14
s_cbranch_scc1 L_idle
v_mfma_f32_16x16x32_bf16 v[64:67], a[144:147], a[88:91], v[64:67]
ds_read_b64_tr_b16 a[140:141], v227 offset:18432
ds_read_b64_tr_b16 a[142:143], v227 offset:20480
v_mfma_f32_16x16x32_bf16 v[68:71], a[148:151], a[88:91], v[68:71]
ds_read_b64_tr_b16 a[144:145], v224 offset:22528
ds_read_b64_tr_b16 a[146:147], v224 offset:24576
buffer_load_dwordx4 v91, s[48:51], s71 offen offset:1024 lds
v_mfma_f32_16x16x32_bf16 v[72:75], a[152:155], a[88:91], v[72:75]
ds_read_b64_tr_b16 a[148:149], v225 offset:22528
ds_read_b64_tr_b16 a[150:151], v225 offset:24576
v_mfma_f32_16x16x32_bf16 v[76:79], a[156:159], a[88:91], v[76:79]
s_waitcnt lgkmcnt(4)
ds_read_b64_tr_b16 a[152:153], v226 offset:22528
ds_read_b64_tr_b16 a[154:155], v226 offset:24576
buffer_load_dwordx4 v92, s[48:51], s71 offen offset:2048 lds
v_mfma_f32_16x16x32_bf16 v[64:67], a[128:131], a[92:95], v[64:67]
ds_read_b64_tr_b16 a[156:157], v227 offset:22528
ds_read_b64_tr_b16 a[158:159], v227 offset:24576
v_mfma_f32_16x16x32_bf16 v[68:71], a[132:135], a[92:95], v[68:71]
ds_read_b64_tr_b16 a[128:129], v224 offset:12288
ds_read_b64_tr_b16 a[130:131], v224 offset:26624
buffer_load_dwordx4 v93, s[48:51], s71 offen offset:3072 lds
v_mfma_f32_16x16x32_bf16 v[72:75], a[136:139], a[92:95], v[72:75]
s_add_u32 m0, s79, 36864
s_add_u32 s71, s74, 1032192
ds_read_b64_tr_b16 a[132:133], v225 offset:12288
ds_read_b64_tr_b16 a[134:135], v225 offset:26624
v_mfma_f32_16x16x32_bf16 v[76:79], a[140:143], a[92:95], v[76:79]
s_waitcnt lgkmcnt(4)
ds_read_b64_tr_b16 a[136:137], v226 offset:12288
ds_read_b64_tr_b16 a[138:139], v226 offset:26624
buffer_load_dwordx4 v90, s[48:51], s71 offen offset:0 lds
v_mfma_f32_16x16x32_bf16 v[64:67], a[144:147], a[104:107], v[64:67]
ds_read_b64_tr_b16 a[140:141], v227 offset:12288
ds_read_b64_tr_b16 a[142:143], v227 offset:26624
v_mfma_f32_16x16x32_bf16 v[68:71], a[148:151], a[104:107], v[68:71]
s_barrier
ds_read_b64_tr_b16 a[144:145], v224 offset:32768
ds_read_b64_tr_b16 a[146:147], v224 offset:34816
buffer_load_dwordx4 v91, s[48:51], s71 offen offset:1024 lds
v_mfma_f32_16x16x32_bf16 v[72:75], a[152:155], a[104:107], v[72:75]
ds_read_b64_tr_b16 a[148:149], v225 offset:32768
ds_read_b64_tr_b16 a[150:151], v225 offset:34816
v_mfma_f32_16x16x32_bf16 v[76:79], a[156:159], a[104:107], v[76:79]
s_waitcnt lgkmcnt(4)
ds_read_b64_tr_b16 a[152:153], v226 offset:32768
ds_read_b64_tr_b16 a[154:155], v226 offset:34816
buffer_load_dwordx4 v92, s[48:51], s71 offen offset:2048 lds
v_mfma_f32_16x16x32_bf16 v[64:67], a[128:131], a[108:111], v[64:67]
ds_read_b64_tr_b16 a[156:157], v227 offset:32768
ds_read_b64_tr_b16 a[158:159], v227 offset:34816
v_mfma_f32_16x16x32_bf16 v[68:71], a[132:135], a[108:111], v[68:71]
ds_read_b64_tr_b16 a[128:129], v224 offset:36864
ds_read_b64_tr_b16 a[130:131], v224 offset:38912
v_mfma_f32_16x16x32_bf16 v[72:75], a[136:139], a[108:111], v[72:75]
ds_read_b64_tr_b16 a[132:133], v225 offset:36864
ds_read_b64_tr_b16 a[134:135], v225 offset:38912
v_mfma_f32_16x16x32_bf16 v[76:79], a[140:143], a[108:111], v[76:79]
s_waitcnt lgkmcnt(4)
ds_read_b64_tr_b16 a[136:137], v226 offset:36864
ds_read_b64_tr_b16 a[138:139], v226 offset:38912
v_mfma_f32_16x16x32_bf16 v[64:67], a[144:147], a[120:123], v[64:67]
ds_read_b64_tr_b16 a[140:141], v227 offset:36864
ds_read_b64_tr_b16 a[142:143], v227 offset:38912
v_mfma_f32_16x16x32_bf16 v[68:71], a[148:151], a[120:123], v[68:71]
s_barrier
ds_read_b64_tr_b16 a[144:145], v224 offset:40960
ds_read_b64_tr_b16 a[146:147], v224 offset:43008
v_mfma_f32_16x16x32_bf16 v[72:75], a[152:155], a[120:123], v[72:75]
s_add_u32 s71, s74, 1204224
ds_read_b64_tr_b16 a[148:149], v225 offset:40960
ds_read_b64_tr_b16 a[150:151], v225 offset:43008
v_mfma_f32_16x16x32_bf16 v[76:79], a[156:159], a[120:123], v[76:79]
s_waitcnt lgkmcnt(4)
ds_read_b64_tr_b16 a[152:153], v226 offset:40960
ds_read_b64_tr_b16 a[154:155], v226 offset:43008
v_mfma_f32_16x16x32_bf16 v[64:67], a[128:131], a[124:127], v[64:67]
s_add_u32 m0, s79, 65536
ds_read_b64_tr_b16 a[156:157], v227 offset:40960
ds_read_b64_tr_b16 a[158:159], v227 offset:43008
v_mfma_f32_16x16x32_bf16 v[68:71], a[132:135], a[124:127], v[68:71]
ds_read_b64_tr_b16 a[128:129], v224 offset:47104
ds_read_b64_tr_b16 a[130:131], v224 offset:49152
buffer_load_dwordx4 v90, s[48:51], s71 offen offset:0 lds
v_mfma_f32_16x16x32_bf16 v[72:75], a[136:139], a[124:127], v[72:75]
ds_read_b64_tr_b16 a[132:133], v225 offset:47104
ds_read_b64_tr_b16 a[134:135], v225 offset:49152
v_mfma_f32_16x16x32_bf16 v[76:79], a[140:143], a[124:127], v[76:79]
s_waitcnt lgkmcnt(4)
ds_read_b64_tr_b16 a[136:137], v226 offset:47104
ds_read_b64_tr_b16 a[138:139], v226 offset:49152
buffer_load_dwordx4 v91, s[48:51], s71 offen offset:1024 lds
v_mfma_f32_16x16x32_bf16 v[64:67], a[144:147], a[208:211], v[64:67]
ds_read_b64_tr_b16 a[140:141], v227 offset:47104
ds_read_b64_tr_b16 a[142:143], v227 offset:49152
v_mfma_f32_16x16x32_bf16 v[68:71], a[148:151], a[208:211], v[68:71]
ds_read_b64_tr_b16 a[144:145], v224 offset:51200
ds_read_b64_tr_b16 a[146:147], v224 offset:53248
buffer_load_dwordx4 v92, s[48:51], s71 offen offset:2048 lds
v_mfma_f32_16x16x32_bf16 v[72:75], a[152:155], a[208:211], v[72:75]
ds_read_b64_tr_b16 a[148:149], v225 offset:51200
ds_read_b64_tr_b16 a[150:151], v225 offset:53248
v_mfma_f32_16x16x32_bf16 v[76:79], a[156:159], a[208:211], v[76:79]
s_waitcnt lgkmcnt(4)
ds_read_b64_tr_b16 a[152:153], v226 offset:51200
ds_read_b64_tr_b16 a[154:155], v226 offset:53248
buffer_load_dwordx4 v93, s[48:51], s71 offen offset:3072 lds
v_mfma_f32_16x16x32_bf16 v[64:67], a[128:131], a[212:215], v[64:67]
s_add_u32 m0, s79, 69632
s_add_u32 s71, s74, 1232896
ds_read_b64_tr_b16 a[156:157], v227 offset:51200
ds_read_b64_tr_b16 a[158:159], v227 offset:53248
v_mfma_f32_16x16x32_bf16 v[68:71], a[132:135], a[212:215], v[68:71]
ds_read_b64_tr_b16 a[128:129], v224 offset:55296
ds_read_b64_tr_b16 a[130:131], v224 offset:57344
buffer_load_dwordx4 v90, s[48:51], s71 offen offset:0 lds
v_mfma_f32_16x16x32_bf16 v[72:75], a[136:139], a[212:215], v[72:75]
ds_read_b64_tr_b16 a[132:133], v225 offset:55296
ds_read_b64_tr_b16 a[134:135], v225 offset:57344
v_mfma_f32_16x16x32_bf16 v[76:79], a[140:143], a[212:215], v[76:79]
s_waitcnt lgkmcnt(4)
ds_read_b64_tr_b16 a[136:137], v226 offset:55296
ds_read_b64_tr_b16 a[138:139], v226 offset:57344
buffer_load_dwordx4 v91, s[48:51], s71 offen offset:1024 lds
v_mfma_f32_16x16x32_bf16 v[64:67], a[144:147], a[216:219], v[64:67]
ds_read_b64_tr_b16 a[140:141], v227 offset:55296
ds_read_b64_tr_b16 a[142:143], v227 offset:57344
v_mfma_f32_16x16x32_bf16 v[68:71], a[148:151], a[216:219], v[68:71]
ds_read_b64_tr_b16 a[144:145], v224 offset:45056
ds_read_b64_tr_b16 a[146:147], v224 offset:59392
buffer_load_dwordx4 v92, s[48:51], s71 offen offset:2048 lds
v_mfma_f32_16x16x32_bf16 v[72:75], a[152:155], a[216:219], v[72:75]
ds_read_b64_tr_b16 a[148:149], v225 offset:45056
ds_read_b64_tr_b16 a[150:151], v225 offset:59392
v_mfma_f32_16x16x32_bf16 v[76:79], a[156:159], a[216:219], v[76:79]
s_waitcnt lgkmcnt(4)
ds_read_b64_tr_b16 a[152:153], v226 offset:45056
ds_read_b64_tr_b16 a[154:155], v226 offset:59392
v_mfma_f32_16x16x32_bf16 v[64:67], a[128:131], a[220:223], v[64:67]
s_waitcnt vmcnt(22)
ds_read_b64_tr_b16 a[156:157], v227 offset:45056
ds_read_b64_tr_b16 a[158:159], v227 offset:59392
v_add_u32 v224, 0x10000, v224
v_mfma_f32_16x16x32_bf16 v[68:71], a[132:135], a[220:223], v[68:71]
s_barrier
ds_read_b64_tr_b16 a[128:129], v224 offset:0
ds_read_b64_tr_b16 a[130:131], v224 offset:2048
v_add_u32 v225, 0x10000, v225
v_mfma_f32_16x16x32_bf16 v[72:75], a[136:139], a[220:223], v[72:75]
ds_read_b64_tr_b16 a[132:133], v225 offset:0
ds_read_b64_tr_b16 a[134:135], v225 offset:2048
v_add_u32 v226, 0x10000, v226
v_mfma_f32_16x16x32_bf16 v[76:79], a[140:143], a[220:223], v[76:79]
s_waitcnt lgkmcnt(4)
ds_read_b64_tr_b16 a[136:137], v226 offset:0
ds_read_b64_tr_b16 a[138:139], v226 offset:2048
v_add_u32 v227, 0x10000, v227
v_mfma_f32_16x16x32_bf16 v[64:67], a[144:147], a[224:227], v[64:67]
ds_read_b64_tr_b16 a[140:141], v227 offset:0
ds_read_b64_tr_b16 a[142:143], v227 offset:2048
v_mfma_f32_16x16x32_bf16 v[68:71], a[148:151], a[224:227], v[68:71]
ds_read_b64_tr_b16 a[144:145], v224 offset:4096
ds_read_b64_tr_b16 a[146:147], v224 offset:6144
v_mfma_f32_16x16x32_bf16 v[72:75], a[152:155], a[224:227], v[72:75]
ds_read_b64_tr_b16 a[148:149], v225 offset:4096
ds_read_b64_tr_b16 a[150:151], v225 offset:6144
v_mfma_f32_16x16x32_bf16 v[76:79], a[156:159], a[224:227], v[76:79]
s_waitcnt lgkmcnt(4)
ds_read_b64_tr_b16 a[152:153], v226 offset:4096
ds_read_b64_tr_b16 a[154:155], v226 offset:6144
v_mfma_f32_16x16x32_bf16 v[64:67], a[128:131], a[228:231], v[64:67]
ds_read_b64_tr_b16 a[156:157], v227 offset:4096
ds_read_b64_tr_b16 a[158:159], v227 offset:6144
buffer_load_dword v236, v234, s[64:67], s82 offen offset:4 sc1
v_mfma_f32_16x16x32_bf16 v[68:71], a[132:135], a[228:231], v[68:71]
s_barrier
ds_read_b64_tr_b16 a[128:129], v224 offset:8192
ds_read_b64_tr_b16 a[130:131], v224 offset:10240
v_mfma_f32_16x16x32_bf16 v[72:75], a[136:139], a[228:231], v[72:75]
s_add_u32 s71, s74, 1404928
ds_read_b64_tr_b16 a[132:133], v225 offset:8192
ds_read_b64_tr_b16 a[134:135], v225 offset:10240
v_mfma_f32_16x16x32_bf16 v[76:79], a[140:143], a[228:231], v[76:79]
s_waitcnt lgkmcnt(4)
ds_read_b64_tr_b16 a[136:137], v226 offset:8192
ds_read_b64_tr_b16 a[138:139], v226 offset:10240
v_mfma_f32_16x16x32_bf16 v[64:67], a[144:147], a[232:235], v[64:67]
s_add_u32 m0, s79, 98304
ds_read_b64_tr_b16 a[140:141], v227 offset:8192
ds_read_b64_tr_b16 a[142:143], v227 offset:10240
v_mfma_f32_16x16x32_bf16 v[68:71], a[148:151], a[232:235], v[68:71]
ds_read_b64_tr_b16 a[144:145], v224 offset:14336
ds_read_b64_tr_b16 a[146:147], v224 offset:16384
buffer_load_dwordx4 v90, s[48:51], s71 offen offset:0 lds
v_mfma_f32_16x16x32_bf16 v[72:75], a[152:155], a[232:235], v[72:75]
ds_read_b64_tr_b16 a[148:149], v225 offset:14336
ds_read_b64_tr_b16 a[150:151], v225 offset:16384
v_mfma_f32_16x16x32_bf16 v[76:79], a[156:159], a[232:235], v[76:79]
s_waitcnt lgkmcnt(4)
ds_read_b64_tr_b16 a[152:153], v226 offset:14336
ds_read_b64_tr_b16 a[154:155], v226 offset:16384
buffer_load_dwordx4 v91, s[48:51], s71 offen offset:1024 lds
v_mfma_f32_16x16x32_bf16 v[64:67], a[128:131], a[236:239], v[64:67]
ds_read_b64_tr_b16 a[156:157], v227 offset:14336
ds_read_b64_tr_b16 a[158:159], v227 offset:16384
v_mfma_f32_16x16x32_bf16 v[68:71], a[132:135], a[236:239], v[68:71]
ds_read_b64_tr_b16 a[128:129], v224 offset:18432
ds_read_b64_tr_b16 a[130:131], v224 offset:20480
buffer_load_dwordx4 v92, s[48:51], s71 offen offset:2048 lds
v_mfma_f32_16x16x32_bf16 v[72:75], a[136:139], a[236:239], v[72:75]
ds_read_b64_tr_b16 a[132:133], v225 offset:18432
ds_read_b64_tr_b16 a[134:135], v225 offset:20480
v_mfma_f32_16x16x32_bf16 v[76:79], a[140:143], a[236:239], v[76:79]
s_waitcnt lgkmcnt(4)
ds_read_b64_tr_b16 a[136:137], v226 offset:18432
ds_read_b64_tr_b16 a[138:139], v226 offset:20480
buffer_load_dwordx4 v93, s[48:51], s71 offen offset:3072 lds
v_mfma_f32_16x16x32_bf16 v[64:67], a[144:147], a[240:243], v[64:67]
s_add_u32 m0, s79, 102400
s_add_u32 s71, s74, 1433600
ds_read_b64_tr_b16 a[140:141], v227 offset:18432
ds_read_b64_tr_b16 a[142:143], v227 offset:20480
v_mfma_f32_16x16x32_bf16 v[68:71], a[148:151], a[240:243], v[68:71]
ds_read_b64_tr_b16 a[144:145], v224 offset:22528
ds_read_b64_tr_b16 a[146:147], v224 offset:24576
buffer_load_dwordx4 v90, s[48:51], s71 offen offset:0 lds
v_mfma_f32_16x16x32_bf16 v[72:75], a[152:155], a[240:243], v[72:75]
ds_read_b64_tr_b16 a[148:149], v225 offset:22528
ds_read_b64_tr_b16 a[150:151], v225 offset:24576
v_mfma_f32_16x16x32_bf16 v[76:79], a[156:159], a[240:243], v[76:79]
s_waitcnt lgkmcnt(4)
ds_read_b64_tr_b16 a[152:153], v226 offset:22528
ds_read_b64_tr_b16 a[154:155], v226 offset:24576
buffer_load_dwordx4 v91, s[48:51], s71 offen offset:1024 lds
v_mfma_f32_16x16x32_bf16 v[64:67], a[128:131], a[244:247], v[64:67]
ds_read_b64_tr_b16 a[156:157], v227 offset:22528
ds_read_b64_tr_b16 a[158:159], v227 offset:24576
v_mfma_f32_16x16x32_bf16 v[68:71], a[132:135], a[244:247], v[68:71]
ds_read_b64_tr_b16 a[128:129], v224 offset:12288
ds_read_b64_tr_b16 a[130:131], v224 offset:26624
buffer_load_dwordx4 v92, s[48:51], s71 offen offset:2048 lds
v_mfma_f32_16x16x32_bf16 v[72:75], a[136:139], a[244:247], v[72:75]
ds_read_b64_tr_b16 a[132:133], v225 offset:12288
ds_read_b64_tr_b16 a[134:135], v225 offset:26624
v_mfma_f32_16x16x32_bf16 v[76:79], a[140:143], a[244:247], v[76:79]
s_waitcnt lgkmcnt(4)
ds_read_b64_tr_b16 a[136:137], v226 offset:12288
ds_read_b64_tr_b16 a[138:139], v226 offset:26624
v_mfma_f32_16x16x32_bf16 v[64:67], a[144:147], a[248:251], v[64:67]
s_waitcnt vmcnt(23)
ds_read_b64_tr_b16 a[140:141], v227 offset:12288
ds_read_b64_tr_b16 a[142:143], v227 offset:26624
v_add_u32 v224, 0xfffe0000, v224
v_mfma_f32_16x16x32_bf16 v[68:71], a[148:151], a[248:251], v[68:71]
s_barrier
ds_read_b64_tr_b16 a[144:145], v224 offset:0
ds_read_b64_tr_b16 a[146:147], v224 offset:2048
v_add_u32 v225, 0xfffe0000, v225
v_mfma_f32_16x16x32_bf16 v[72:75], a[152:155], a[248:251], v[72:75]
ds_read_b64_tr_b16 a[148:149], v225 offset:0
ds_read_b64_tr_b16 a[150:151], v225 offset:2048
v_add_u32 v226, 0xfffe0000, v226
v_mfma_f32_16x16x32_bf16 v[76:79], a[156:159], a[248:251], v[76:79]
s_waitcnt lgkmcnt(4)
ds_read_b64_tr_b16 a[152:153], v226 offset:0
ds_read_b64_tr_b16 a[154:155], v226 offset:2048
v_add_u32 v227, 0xfffe0000, v227
v_mfma_f32_16x16x32_bf16 v[64:67], a[128:131], a[252:255], v[64:67]
ds_read_b64_tr_b16 a[156:157], v227 offset:0
ds_read_b64_tr_b16 a[158:159], v227 offset:2048
v_mfma_f32_16x16x32_bf16 v[68:71], a[132:135], a[252:255], v[68:71]
ds_read_b64_tr_b16 a[128:129], v224 offset:4096
ds_read_b64_tr_b16 a[130:131], v224 offset:6144
v_mfma_f32_16x16x32_bf16 v[72:75], a[136:139], a[252:255], v[72:75]
ds_read_b64_tr_b16 a[132:133], v225 offset:4096
ds_read_b64_tr_b16 a[134:135], v225 offset:6144
v_mfma_f32_16x16x32_bf16 v[76:79], a[140:143], a[252:255], v[76:79]
s_waitcnt lgkmcnt(4)
ds_read_b64_tr_b16 a[136:137], v226 offset:4096
ds_read_b64_tr_b16 a[138:139], v226 offset:6144
v_mfma_f32_16x16x32_bf16 v[64:67], a[144:147], a[176:179], v[64:67]
ds_read_b64_tr_b16 a[140:141], v227 offset:4096
ds_read_b64_tr_b16 a[142:143], v227 offset:6144
v_mfma_f32_16x16x32_bf16 v[68:71], a[148:151], a[176:179], v[68:71]
ds_read_b64_tr_b16 a[144:145], v224 offset:8192
ds_read_b64_tr_b16 a[146:147], v224 offset:10240
v_mfma_f32_16x16x32_bf16 v[72:75], a[152:155], a[176:179], v[72:75]
ds_read_b64_tr_b16 a[148:149], v225 offset:8192
ds_read_b64_tr_b16 a[150:151], v225 offset:10240
v_mfma_f32_16x16x32_bf16 v[76:79], a[156:159], a[176:179], v[76:79]
s_waitcnt lgkmcnt(4)
ds_read_b64_tr_b16 a[152:153], v226 offset:8192
ds_read_b64_tr_b16 a[154:155], v226 offset:10240
v_mfma_f32_16x16x32_bf16 v[64:67], a[128:131], a[180:183], v[64:67]
ds_read_b64_tr_b16 a[156:157], v227 offset:8192
ds_read_b64_tr_b16 a[158:159], v227 offset:10240
s_waitcnt vmcnt(7)
v_cmp_ne_u32 vcc, v236, v232
v_mfma_f32_16x16x32_bf16 v[68:71], a[132:135], a[180:183], v[68:71]
ds_read_b64_tr_b16 a[128:129], v224 offset:14336
ds_read_b64_tr_b16 a[130:131], v224 offset:16384
v_mfma_f32_16x16x32_bf16 v[72:75], a[136:139], a[180:183], v[72:75]
s_cbranch_vccnz L_sps
L_bks:
ds_read_b64_tr_b16 a[132:133], v225 offset:14336
ds_read_b64_tr_b16 a[134:135], v225 offset:16384
buffer_load_dwordx4 v[96:99], v252, s[64:67], s95 offen offset:0 sc1
v_mfma_f32_16x16x32_bf16 v[76:79], a[140:143], a[180:183], v[76:79]
s_waitcnt lgkmcnt(4)
ds_read_b64_tr_b16 a[136:137], v226 offset:14336
ds_read_b64_tr_b16 a[138:139], v226 offset:16384
buffer_load_dwordx4 v[100:103], v252, s[64:67], s95 offen offset:1024 sc1
v_mfma_f32_16x16x32_bf16 v[64:67], a[144:147], a[160:163], v[64:67]
ds_read_b64_tr_b16 a[140:141], v227 offset:14336
ds_read_b64_tr_b16 a[142:143], v227 offset:16384
buffer_load_dwordx4 v[104:107], v252, s[64:67], s95 offen offset:2048 sc1
v_mfma_f32_16x16x32_bf16 v[68:71], a[148:151], a[160:163], v[68:71]
ds_read_b64_tr_b16 a[144:145], v224 offset:18432
ds_read_b64_tr_b16 a[146:147], v224 offset:20480
buffer_load_dwordx4 v[108:111], v252, s[64:67], s95 offen offset:3072 sc1
v_mfma_f32_16x16x32_bf16 v[72:75], a[152:155], a[160:163], v[72:75]
s_add_u32 s95, s95, 4096
ds_read_b64_tr_b16 a[148:149], v225 offset:18432
ds_read_b64_tr_b16 a[150:151], v225 offset:20480
buffer_load_dwordx4 v[112:115], v252, s[64:67], s95 offen offset:0 sc1
v_mfma_f32_16x16x32_bf16 v[76:79], a[156:159], a[160:163], v[76:79]
s_waitcnt lgkmcnt(4)
ds_read_b64_tr_b16 a[152:153], v226 offset:18432
ds_read_b64_tr_b16 a[154:155], v226 offset:20480
buffer_load_dwordx4 v[116:119], v252, s[64:67], s95 offen offset:1024 sc1
v_mfma_f32_16x16x32_bf16 v[64:67], a[128:131], a[164:167], v[64:67]
ds_read_b64_tr_b16 a[156:157], v227 offset:18432
ds_read_b64_tr_b16 a[158:159], v227 offset:20480
buffer_load_dwordx4 v[120:123], v252, s[64:67], s95 offen offset:2048 sc1
v_mfma_f32_16x16x32_bf16 v[68:71], a[132:135], a[164:167], v[68:71]
ds_read_b64_tr_b16 a[128:129], v224 offset:22528
ds_read_b64_tr_b16 a[130:131], v224 offset:24576
buffer_load_dwordx4 v[124:127], v252, s[64:67], s95 offen offset:3072 sc1
v_mfma_f32_16x16x32_bf16 v[72:75], a[136:139], a[164:167], v[72:75]
ds_read_b64_tr_b16 a[132:133], v225 offset:22528
ds_read_b64_tr_b16 a[134:135], v225 offset:24576
v_mfma_f32_16x16x32_bf16 v[76:79], a[140:143], a[164:167], v[76:79]
s_waitcnt lgkmcnt(4)
ds_read_b64_tr_b16 a[136:137], v226 offset:22528
ds_read_b64_tr_b16 a[138:139], v226 offset:24576
v_mfma_f32_16x16x32_bf16 v[64:67], a[144:147], a[204:207], v[64:67]
ds_read_b64_tr_b16 a[140:141], v227 offset:22528
ds_read_b64_tr_b16 a[142:143], v227 offset:24576
v_mfma_f32_16x16x32_bf16 v[68:71], a[148:151], a[204:207], v[68:71]
ds_read_b64_tr_b16 a[144:145], v224 offset:12288
ds_read_b64_tr_b16 a[146:147], v224 offset:26624
v_mfma_f32_16x16x32_bf16 v[72:75], a[152:155], a[204:207], v[72:75]
ds_read_b64_tr_b16 a[148:149], v225 offset:12288
ds_read_b64_tr_b16 a[150:151], v225 offset:26624
v_mfma_f32_16x16x32_bf16 v[76:79], a[156:159], a[204:207], v[76:79]
s_waitcnt lgkmcnt(4)
ds_read_b64_tr_b16 a[152:153], v226 offset:12288
ds_read_b64_tr_b16 a[154:155], v226 offset:26624
v_mfma_f32_16x16x32_bf16 v[64:67], a[128:131], a[0:3], v[64:67]
s_barrier
ds_read_b64_tr_b16 a[156:157], v227 offset:12288
ds_read_b64_tr_b16 a[158:159], v227 offset:26624
v_mfma_f32_16x16x32_bf16 v[68:71], a[132:135], a[0:3], v[68:71]
s_add_u32 s95, s95, 4096
ds_read_b64_tr_b16 a[128:129], v224 offset:32768
ds_read_b64_tr_b16 a[130:131], v224 offset:34816
buffer_load_dwordx4 v[128:131], v252, s[64:67], s95 offen offset:0 sc1
v_mfma_f32_16x16x32_bf16 v[72:75], a[136:139], a[0:3], v[72:75]
ds_read_b64_tr_b16 a[132:133], v225 offset:32768
ds_read_b64_tr_b16 a[134:135], v225 offset:34816
v_mfma_f32_16x16x32_bf16 v[76:79], a[140:143], a[0:3], v[76:79]
s_waitcnt lgkmcnt(4)
ds_read_b64_tr_b16 a[136:137], v226 offset:32768
ds_read_b64_tr_b16 a[138:139], v226 offset:34816
v_mfma_f32_16x16x32_bf16 v[64:67], a[144:147], a[4:7], v[64:67]
ds_read_b64_tr_b16 a[140:141], v227 offset:32768
ds_read_b64_tr_b16 a[142:143], v227 offset:34816
v_mfma_f32_16x16x32_bf16 v[68:71], a[148:151], a[4:7], v[68:71]
ds_read_b64_tr_b16 a[144:145], v224 offset:36864
ds_read_b64_tr_b16 a[146:147], v224 offset:38912
v_mfma_f32_16x16x32_bf16 v[72:75], a[152:155], a[4:7], v[72:75]
ds_read_b64_tr_b16 a[148:149], v225 offset:36864
ds_read_b64_tr_b16 a[150:151], v225 offset:38912
v_mfma_f32_16x16x32_bf16 v[76:79], a[156:159], a[4:7], v[76:79]
s_waitcnt lgkmcnt(4)
ds_read_b64_tr_b16 a[152:153], v226 offset:36864
ds_read_b64_tr_b16 a[154:155], v226 offset:38912
v_mfma_f32_16x16x32_bf16 v[64:67], a[128:131], a[16:19], v[64:67]
ds_read_b64_tr_b16 a[156:157], v227 offset:36864
ds_read_b64_tr_b16 a[158:159], v227 offset:38912
v_mfma_f32_16x16x32_bf16 v[68:71], a[132:135], a[16:19], v[68:71]
ds_read_b64_tr_b16 a[128:129], v224 offset:40960
ds_read_b64_tr_b16 a[130:131], v224 offset:43008
buffer_load_dwordx4 v[132:135], v252, s[64:67], s95 offen offset:1024 sc1
v_mfma_f32_16x16x32_bf16 v[72:75], a[136:139], a[16:19], v[72:75]
ds_read_b64_tr_b16 a[132:133], v225 offset:40960
ds_read_b64_tr_b16 a[134:135], v225 offset:43008
v_mfma_f32_16x16x32_bf16 v[76:79], a[140:143], a[16:19], v[76:79]
s_waitcnt lgkmcnt(4)
ds_read_b64_tr_b16 a[136:137], v226 offset:40960
ds_read_b64_tr_b16 a[138:139], v226 offset:43008
v_mfma_f32_16x16x32_bf16 v[64:67], a[144:147], a[20:23], v[64:67]
ds_read_b64_tr_b16 a[140:141], v227 offset:40960
ds_read_b64_tr_b16 a[142:143], v227 offset:43008
v_mfma_f32_16x16x32_bf16 v[68:71], a[148:151], a[20:23], v[68:71]
ds_read_b64_tr_b16 a[144:145], v224 offset:47104
ds_read_b64_tr_b16 a[146:147], v224 offset:49152
v_mfma_f32_16x16x32_bf16 v[72:75], a[152:155], a[20:23], v[72:75]
ds_read_b64_tr_b16 a[148:149], v225 offset:47104
ds_read_b64_tr_b16 a[150:151], v225 offset:49152
v_mfma_f32_16x16x32_bf16 v[76:79], a[156:159], a[20:23], v[76:79]
s_waitcnt lgkmcnt(4)
ds_read_b64_tr_b16 a[152:153], v226 offset:47104
ds_read_b64_tr_b16 a[154:155], v226 offset:49152
v_mfma_f32_16x16x32_bf16 v[64:67], a[128:131], a[32:35], v[64:67]
ds_read_b64_tr_b16 a[156:157], v227 offset:47104
ds_read_b64_tr_b16 a[158:159], v227 offset:49152
v_mfma_f32_16x16x32_bf16 v[68:71], a[132:135], a[32:35], v[68:71]
ds_read_b64_tr_b16 a[128:129], v224 offset:51200
ds_read_b64_tr_b16 a[130:131], v224 offset:53248
buffer_load_dwordx4 v[136:139], v252, s[64:67], s95 offen offset:2048 sc1
v_mfma_f32_16x16x32_bf16 v[72:75], a[136:139], a[32:35], v[72:75]
ds_read_b64_tr_b16 a[132:133], v225 offset:51200
ds_read_b64_tr_b16 a[134:135], v225 offset:53248
v_mfma_f32_16x16x32_bf16 v[76:79], a[140:143], a[32:35], v[76:79]
s_waitcnt lgkmcnt(4)
ds_read_b64_tr_b16 a[136:137], v226 offset:51200
ds_read_b64_tr_b16 a[138:139], v226 offset:53248
v_mfma_f32_16x16x32_bf16 v[64:67], a[144:147], a[36:39], v[64:67]
ds_read_b64_tr_b16 a[140:141], v227 offset:51200
ds_read_b64_tr_b16 a[142:143], v227 offset:53248
v_mfma_f32_16x16x32_bf16 v[68:71], a[148:151], a[36:39], v[68:71]
ds_read_b64_tr_b16 a[144:145], v224 offset:55296
ds_read_b64_tr_b16 a[146:147], v224 offset:57344
v_mfma_f32_16x16x32_bf16 v[72:75], a[152:155], a[36:39], v[72:75]
ds_read_b64_tr_b16 a[148:149], v225 offset:55296
ds_read_b64_tr_b16 a[150:151], v225 offset:57344
v_mfma_f32_16x16x32_bf16 v[76:79], a[156:159], a[36:39], v[76:79]
s_waitcnt lgkmcnt(4)
ds_read_b64_tr_b16 a[152:153], v226 offset:55296
ds_read_b64_tr_b16 a[154:155], v226 offset:57344
v_mfma_f32_16x16x32_bf16 v[64:67], a[128:131], a[48:51], v[64:67]
ds_read_b64_tr_b16 a[156:157], v227 offset:55296
ds_read_b64_tr_b16 a[158:159], v227 offset:57344
v_mfma_f32_16x16x32_bf16 v[68:71], a[132:135], a[48:51], v[68:71]
ds_read_b64_tr_b16 a[128:129], v224 offset:45056
ds_read_b64_tr_b16 a[130:131], v224 offset:59392
buffer_load_dwordx4 v[140:143], v252, s[64:67], s95 offen offset:3072 sc1
v_mfma_f32_16x16x32_bf16 v[72:75], a[136:139], a[48:51], v[72:75]
ds_read_b64_tr_b16 a[132:133], v225 offset:45056
ds_read_b64_tr_b16 a[134:135], v225 offset:59392
v_mfma_f32_16x16x32_bf16 v[76:79], a[140:143], a[48:51], v[76:79]
s_waitcnt lgkmcnt(4)
ds_read_b64_tr_b16 a[136:137], v226 offset:45056
ds_read_b64_tr_b16 a[138:139], v226 offset:59392
v_mfma_f32_16x16x32_bf16 v[64:67], a[144:147], a[52:55], v[64:67]
ds_read_b64_tr_b16 a[140:141], v227 offset:45056
ds_read_b64_tr_b16 a[142:143], v227 offset:59392
v_add_u32 v224, 0x10000, v224
v_mfma_f32_16x16x32_bf16 v[68:71], a[148:151], a[52:55], v[68:71]
s_barrier
ds_read_b64_tr_b16 a[144:145], v224 offset:0
ds_read_b64_tr_b16 a[146:147], v224 offset:2048
v_add_u32 v225, 0x10000, v225
v_mfma_f32_16x16x32_bf16 v[72:75], a[152:155], a[52:55], v[72:75]
ds_read_b64_tr_b16 a[148:149], v225 offset:0
ds_read_b64_tr_b16 a[150:151], v225 offset:2048
v_add_u32 v226, 0x10000, v226
v_mfma_f32_16x16x32_bf16 v[76:79], a[156:159], a[52:55], v[76:79]
s_waitcnt lgkmcnt(4)
ds_read_b64_tr_b16 a[152:153], v226 offset:0
ds_read_b64_tr_b16 a[154:155], v226 offset:2048
v_add_u32 v227, 0x10000, v227
v_mfma_f32_16x16x32_bf16 v[64:67], a[128:131], a[64:67], v[64:67]
ds_read_b64_tr_b16 a[156:157], v227 offset:0
ds_read_b64_tr_b16 a[158:159], v227 offset:2048
v_mfma_f32_16x16x32_bf16 v[68:71], a[132:135], a[64:67], v[68:71]
s_add_u32 s95, s95, 4096
ds_read_b64_tr_b16 a[128:129], v224 offset:4096
ds_read_b64_tr_b16 a[130:131], v224 offset:6144
buffer_load_dwordx4 v[144:147], v252, s[64:67], s95 offen offset:0 sc1
v_mfma_f32_16x16x32_bf16 v[72:75], a[136:139], a[64:67], v[72:75]
ds_read_b64_tr_b16 a[132:133], v225 offset:4096
ds_read_b64_tr_b16 a[134:135], v225 offset:6144
v_mfma_f32_16x16x32_bf16 v[76:79], a[140:143], a[64:67], v[76:79]
s_waitcnt lgkmcnt(4)
ds_read_b64_tr_b16 a[136:137], v226 offset:4096
ds_read_b64_tr_b16 a[138:139], v226 offset:6144
v_mfma_f32_16x16x32_bf16 v[64:67], a[144:147], a[68:71], v[64:67]
ds_read_b64_tr_b16 a[140:141], v227 offset:4096
ds_read_b64_tr_b16 a[142:143], v227 offset:6144
v_mfma_f32_16x16x32_bf16 v[68:71], a[148:151], a[68:71], v[68:71]
ds_read_b64_tr_b16 a[144:145], v224 offset:8192
ds_read_b64_tr_b16 a[146:147], v224 offset:10240
v_mfma_f32_16x16x32_bf16 v[72:75], a[152:155], a[68:71], v[72:75]
ds_read_b64_tr_b16 a[148:149], v225 offset:8192
ds_read_b64_tr_b16 a[150:151], v225 offset:10240
v_mfma_f32_16x16x32_bf16 v[76:79], a[156:159], a[68:71], v[76:79]
s_waitcnt lgkmcnt(4)
ds_read_b64_tr_b16 a[152:153], v226 offset:8192
ds_read_b64_tr_b16 a[154:155], v226 offset:10240
v_mfma_f32_16x16x32_bf16 v[64:67], a[128:131], a[80:83], v[64:67]
ds_read_b64_tr_b16 a[156:157], v227 offset:8192
ds_read_b64_tr_b16 a[158:159], v227 offset:10240
v_mfma_f32_16x16x32_bf16 v[68:71], a[132:135], a[80:83], v[68:71]
ds_read_b64_tr_b16 a[128:129], v224 offset:14336
ds_read_b64_tr_b16 a[130:131], v224 offset:16384
buffer_load_dwordx4 v[148:151], v252, s[64:67], s95 offen offset:1024 sc1
v_mfma_f32_16x16x32_bf16 v[72:75], a[136:139], a[80:83], v[72:75]
ds_read_b64_tr_b16 a[132:133], v225 offset:14336
ds_read_b64_tr_b16 a[134:135], v225 offset:16384
v_mfma_f32_16x16x32_bf16 v[76:79], a[140:143], a[80:83], v[76:79]
s_waitcnt lgkmcnt(4)
ds_read_b64_tr_b16 a[136:137], v226 offset:14336
ds_read_b64_tr_b16 a[138:139], v226 offset:16384
v_mfma_f32_16x16x32_bf16 v[64:67], a[144:147], a[84:87], v[64:67]
ds_read_b64_tr_b16 a[140:141], v227 offset:14336
ds_read_b64_tr_b16 a[142:143], v227 offset:16384
v_mfma_f32_16x16x32_bf16 v[68:71], a[148:151], a[84:87], v[68:71]
ds_read_b64_tr_b16 a[144:145], v224 offset:18432
ds_read_b64_tr_b16 a[146:147], v224 offset:20480
v_mfma_f32_16x16x32_bf16 v[72:75], a[152:155], a[84:87], v[72:75]
ds_read_b64_tr_b16 a[148:149], v225 offset:18432
ds_read_b64_tr_b16 a[150:151], v225 offset:20480
v_mfma_f32_16x16x32_bf16 v[76:79], a[156:159], a[84:87], v[76:79]
s_waitcnt lgkmcnt(4)
ds_read_b64_tr_b16 a[152:153], v226 offset:18432
ds_read_b64_tr_b16 a[154:155], v226 offset:20480
v_mfma_f32_16x16x32_bf16 v[64:67], a[128:131], a[96:99], v[64:67]
ds_read_b64_tr_b16 a[156:157], v227 offset:18432
ds_read_b64_tr_b16 a[158:159], v227 offset:20480
v_mfma_f32_16x16x32_bf16 v[68:71], a[132:135], a[96:99], v[68:71]
ds_read_b64_tr_b16 a[128:129], v224 offset:22528
ds_read_b64_tr_b16 a[130:131], v224 offset:24576
buffer_load_dwordx4 v[152:155], v252, s[64:67], s95 offen offset:2048 sc1
v_mfma_f32_16x16x32_bf16 v[72:75], a[136:139], a[96:99], v[72:75]
ds_read_b64_tr_b16 a[132:133], v225 offset:22528
ds_read_b64_tr_b16 a[134:135], v225 offset:24576
v_mfma_f32_16x16x32_bf16 v[76:79], a[140:143], a[96:99], v[76:79]
s_waitcnt lgkmcnt(4)
ds_read_b64_tr_b16 a[136:137], v226 offset:22528
ds_read_b64_tr_b16 a[138:139], v226 offset:24576
v_mfma_f32_16x16x32_bf16 v[64:67], a[144:147], a[100:103], v[64:67]
ds_read_b64_tr_b16 a[140:141], v227 offset:22528
ds_read_b64_tr_b16 a[142:143], v227 offset:24576
v_mfma_f32_16x16x32_bf16 v[68:71], a[148:151], a[100:103], v[68:71]
ds_read_b64_tr_b16 a[144:145], v224 offset:12288
ds_read_b64_tr_b16 a[146:147], v224 offset:26624
v_mfma_f32_16x16x32_bf16 v[72:75], a[152:155], a[100:103], v[72:75]
ds_read_b64_tr_b16 a[148:149], v225 offset:12288
ds_read_b64_tr_b16 a[150:151], v225 offset:26624
v_mfma_f32_16x16x32_bf16 v[76:79], a[156:159], a[100:103], v[76:79]
s_waitcnt lgkmcnt(4)
ds_read_b64_tr_b16 a[152:153], v226 offset:12288
ds_read_b64_tr_b16 a[154:155], v226 offset:26624
v_mfma_f32_16x16x32_bf16 v[64:67], a[128:131], a[112:115], v[64:67]
s_waitcnt vmcnt(15)
ds_read_b64_tr_b16 a[156:157], v227 offset:12288
ds_read_b64_tr_b16 a[158:159], v227 offset:26624
v_mfma_f32_16x16x32_bf16 v[68:71], a[132:135], a[112:115], v[68:71]
s_barrier
ds_read_b64_tr_b16 a[128:129], v224 offset:32768
ds_read_b64_tr_b16 a[130:131], v224 offset:34816
buffer_load_dwordx4 v[156:159], v252, s[64:67], s95 offen offset:3072 sc1
v_mfma_f32_16x16x32_bf16 v[72:75], a[136:139], a[112:115], v[72:75]
ds_read_b64_tr_b16 a[132:133], v225 offset:32768
ds_read_b64_tr_b16 a[134:135], v225 offset:34816
v_mfma_f32_16x16x32_bf16 v[76:79], a[140:143], a[112:115], v[76:79]
s_waitcnt lgkmcnt(4)
ds_read_b64_tr_b16 a[136:137], v226 offset:32768
ds_read_b64_tr_b16 a[138:139], v226 offset:34816
v_mfma_f32_16x16x32_bf16 v[64:67], a[144:147], a[116:119], v[64:67]
ds_read_b64_tr_b16 a[140:141], v227 offset:32768
ds_read_b64_tr_b16 a[142:143], v227 offset:34816
v_mfma_f32_16x16x32_bf16 v[68:71], a[148:151], a[116:119], v[68:71]
ds_read_b64_tr_b16 a[144:145], v224 offset:36864
ds_read_b64_tr_b16 a[146:147], v224 offset:38912
v_mfma_f32_16x16x32_bf16 v[72:75], a[152:155], a[116:119], v[72:75]
ds_read_b64_tr_b16 a[148:149], v225 offset:36864
ds_read_b64_tr_b16 a[150:151], v225 offset:38912
v_mfma_f32_16x16x32_bf16 v[76:79], a[156:159], a[116:119], v[76:79]
s_waitcnt lgkmcnt(4)
ds_read_b64_tr_b16 a[152:153], v226 offset:36864
ds_read_b64_tr_b16 a[154:155], v226 offset:38912
v_mfma_f32_16x16x32_bf16 v[64:67], a[128:131], v[0:3], v[64:67]
ds_read_b64_tr_b16 a[156:157], v227 offset:36864
ds_read_b64_tr_b16 a[158:159], v227 offset:38912
v_mfma_f32_16x16x32_bf16 v[68:71], a[132:135], v[0:3], v[68:71]
s_add_u32 s95, s95, 4096
ds_read_b64_tr_b16 a[128:129], v224 offset:40960
ds_read_b64_tr_b16 a[130:131], v224 offset:43008
buffer_load_dwordx4 v[160:163], v252, s[64:67], s95 offen offset:0 sc1
v_mfma_f32_16x16x32_bf16 v[72:75], a[136:139], v[0:3], v[72:75]
ds_read_b64_tr_b16 a[132:133], v225 offset:40960
ds_read_b64_tr_b16 a[134:135], v225 offset:43008
v_mfma_f32_16x16x32_bf16 v[76:79], a[140:143], v[0:3], v[76:79]
s_waitcnt lgkmcnt(4)
ds_read_b64_tr_b16 a[136:137], v226 offset:40960
ds_read_b64_tr_b16 a[138:139], v226 offset:43008
v_mfma_f32_16x16x32_bf16 v[64:67], a[144:147], v[4:7], v[64:67]
ds_read_b64_tr_b16 a[140:141], v227 offset:40960
ds_read_b64_tr_b16 a[142:143], v227 offset:43008
v_mfma_f32_16x16x32_bf16 v[68:71], a[148:151], v[4:7], v[68:71]
ds_read_b64_tr_b16 a[144:145], v224 offset:47104
ds_read_b64_tr_b16 a[146:147], v224 offset:49152
v_mfma_f32_16x16x32_bf16 v[72:75], a[152:155], v[4:7], v[72:75]
ds_read_b64_tr_b16 a[148:149], v225 offset:47104
ds_read_b64_tr_b16 a[150:151], v225 offset:49152
v_mfma_f32_16x16x32_bf16 v[76:79], a[156:159], v[4:7], v[76:79]
s_waitcnt lgkmcnt(4)
ds_read_b64_tr_b16 a[152:153], v226 offset:47104
ds_read_b64_tr_b16 a[154:155], v226 offset:49152
v_mfma_f32_16x16x32_bf16 v[64:67], a[128:131], v[8:11], v[64:67]
ds_read_b64_tr_b16 a[156:157], v227 offset:47104
ds_read_b64_tr_b16 a[158:159], v227 offset:49152
v_mfma_f32_16x16x32_bf16 v[68:71], a[132:135], v[8:11], v[68:71]
ds_read_b64_tr_b16 a[128:129], v224 offset:51200
ds_read_b64_tr_b16 a[130:131], v224 offset:53248
buffer_load_dwordx4 v[164:167], v252, s[64:67], s95 offen offset:1024 sc1
v_mfma_f32_16x16x32_bf16 v[72:75], a[136:139], v[8:11], v[72:75]
ds_read_b64_tr_b16 a[132:133], v225 offset:51200
ds_read_b64_tr_b16 a[134:135], v225 offset:53248
v_mfma_f32_16x16x32_bf16 v[76:79], a[140:143], v[8:11], v[76:79]
s_waitcnt lgkmcnt(4)
ds_read_b64_tr_b16 a[136:137], v226 offset:51200
ds_read_b64_tr_b16 a[138:139], v226 offset:53248
v_mfma_f32_16x16x32_bf16 v[64:67], a[144:147], v[12:15], v[64:67]
ds_read_b64_tr_b16 a[140:141], v227 offset:51200
ds_read_b64_tr_b16 a[142:143], v227 offset:53248
v_mfma_f32_16x16x32_bf16 v[68:71], a[148:151], v[12:15], v[68:71]
ds_read_b64_tr_b16 a[144:145], v224 offset:55296
ds_read_b64_tr_b16 a[146:147], v224 offset:57344
v_mfma_f32_16x16x32_bf16 v[72:75], a[152:155], v[12:15], v[72:75]
ds_read_b64_tr_b16 a[148:149], v225 offset:55296
ds_read_b64_tr_b16 a[150:151], v225 offset:57344
v_mfma_f32_16x16x32_bf16 v[76:79], a[156:159], v[12:15], v[76:79]
s_waitcnt lgkmcnt(4)
ds_read_b64_tr_b16 a[152:153], v226 offset:55296
ds_read_b64_tr_b16 a[154:155], v226 offset:57344
v_mfma_f32_16x16x32_bf16 v[64:67], a[128:131], v[16:19], v[64:67]
ds_read_b64_tr_b16 a[156:157], v227 offset:55296
ds_read_b64_tr_b16 a[158:159], v227 offset:57344
v_mfma_f32_16x16x32_bf16 v[68:71], a[132:135], v[16:19], v[68:71]
ds_read_b64_tr_b16 a[128:129], v224 offset:45056
ds_read_b64_tr_b16 a[130:131], v224 offset:59392
buffer_load_dwordx4 v[168:171], v252, s[64:67], s95 offen offset:2048 sc1
v_mfma_f32_16x16x32_bf16 v[72:75], a[136:139], v[16:19], v[72:75]
ds_read_b64_tr_b16 a[132:133], v225 offset:45056
ds_read_b64_tr_b16 a[134:135], v225 offset:59392
v_mfma_f32_16x16x32_bf16 v[76:79], a[140:143], v[16:19], v[76:79]
s_waitcnt lgkmcnt(4)
ds_read_b64_tr_b16 a[136:137], v226 offset:45056
ds_read_b64_tr_b16 a[138:139], v226 offset:59392
v_mfma_f32_16x16x32_bf16 v[64:67], a[144:147], v[20:23], v[64:67]
ds_read_b64_tr_b16 a[140:141], v227 offset:45056
ds_read_b64_tr_b16 a[142:143], v227 offset:59392
v_mfma_f32_16x16x32_bf16 v[68:71], a[148:151], v[20:23], v[68:71]
v_mfma_f32_16x16x32_bf16 v[72:75], a[152:155], v[20:23], v[72:75]
v_mfma_f32_16x16x32_bf16 v[76:79], a[156:159], v[20:23], v[76:79]
s_waitcnt lgkmcnt(0)
v_mfma_f32_16x16x32_bf16 v[64:67], a[128:131], v[24:27], v[64:67]
v_mfma_f32_16x16x32_bf16 v[68:71], a[132:135], v[24:27], v[68:71]
buffer_load_dwordx4 v[172:175], v252, s[64:67], s95 offen offset:3072 sc1
v_mfma_f32_16x16x32_bf16 v[72:75], a[136:139], v[24:27], v[72:75]
v_mfma_f32_16x16x32_bf16 v[76:79], a[140:143], v[24:27], v[76:79]
v_mbcnt_lo_u32_b32 v236, -1, 0
v_mbcnt_hi_u32_b32 v236, -1, v236
v_and_b32 v237, 15, v236
v_lshl_add_u32 v238, v237, 7, s24
v_and_b32 v237, 6, v236
v_lshrrev_b32 v243, 5, v236
v_xor_b32 v237, v237, v243
v_lshl_add_u32 v238, v237, 4, v238
v_bfe_u32 v237, v236, 4, 1
v_lshl_add_u32 v238, v237, 3, v238
ds_read_b64 v[228:229], v238 offset:0
v_xor_b32 v236, 32, v238
ds_read_b64 v[230:231], v236 offset:0
v_xor_b32 v236, 64, v238
ds_read_b64 v[244:245], v236 offset:0
v_xor_b32 v236, 96, v238
ds_read_b64 v[246:247], v236 offset:0
s_add_u32 s95, s95, 4096
buffer_load_dwordx4 v[176:179], v252, s[64:67], s95 offen offset:0 sc1
v_mul_f32 v48, 0xc3317218, v64
v_mul_f32 v49, 0xc3317218, v65
v_mul_f32 v50, 0xc3317218, v66
v_mul_f32 v51, 0xc3317218, v67
v_exp_f32 v48, v48
v_exp_f32 v49, v49
v_exp_f32 v50, v50
v_exp_f32 v51, v51
v_mul_f32 v64, 0x42f5fdf0, v64
v_mul_f32 v65, 0x42f5fdf0, v65
v_mul_f32 v66, 0x42f5fdf0, v66
v_mul_f32 v67, 0x42f5fdf0, v67
v_add_f32 v48, 1.0, v48
v_add_f32 v49, 1.0, v49
v_add_f32 v50, 1.0, v50
v_add_f32 v51, 1.0, v51
v_rcp_f32 v48, v48
v_rcp_f32 v49, v49
v_rcp_f32 v50, v50
v_rcp_f32 v51, v51
s_nop 0
v_mul_f32 v64, v64, v48
v_mul_f32 v65, v65, v49
v_mul_f32 v66, v66, v50
v_mul_f32 v67, v67, v51
s_waitcnt lgkmcnt(3)
v_lshlrev_b32 v48, 16, v228
v_and_b32 v49, s86, v228
v_lshlrev_b32 v50, 16, v229
v_and_b32 v51, s86, v229
buffer_load_dwordx4 v[180:183], v252, s[64:67], s95 offen offset:1024 sc1
v_mul_f32 v52, 0xc3317218, v68
v_mul_f32 v53, 0xc3317218, v69
v_mul_f32 v54, 0xc3317218, v70
v_mul_f32 v55, 0xc3317218, v71
v_exp_f32 v52, v52
v_exp_f32 v53, v53
v_exp_f32 v54, v54
v_exp_f32 v55, v55
v_mul_f32 v68, 0x42f5fdf0, v68
v_mul_f32 v69, 0x42f5fdf0, v69
v_mul_f32 v70, 0x42f5fdf0, v70
v_mul_f32 v71, 0x42f5fdf0, v71
v_add_f32 v52, 1.0, v52
v_add_f32 v53, 1.0, v53
v_add_f32 v54, 1.0, v54
v_add_f32 v55, 1.0, v55
v_rcp_f32 v52, v52
v_rcp_f32 v53, v53
v_rcp_f32 v54, v54
v_rcp_f32 v55, v55
s_nop 0
v_mul_f32 v68, v68, v52
v_mul_f32 v69, v69, v53
v_mul_f32 v70, v70, v54
v_mul_f32 v71, v71, v55
s_waitcnt lgkmcnt(2)
v_lshlrev_b32 v52, 16, v230
v_and_b32 v53, s86, v230
v_lshlrev_b32 v54, 16, v231
v_and_b32 v55, s86, v231
buffer_load_dwordx4 v[184:187], v252, s[64:67], s95 offen offset:2048 sc1
v_mul_f32 v56, 0xc3317218, v72
v_mul_f32 v57, 0xc3317218, v73
v_mul_f32 v58, 0xc3317218, v74
v_mul_f32 v59, 0xc3317218, v75
v_exp_f32 v56, v56
v_exp_f32 v57, v57
v_exp_f32 v58, v58
v_exp_f32 v59, v59
v_mul_f32 v72, 0x42f5fdf0, v72
v_mul_f32 v73, 0x42f5fdf0, v73
v_mul_f32 v74, 0x42f5fdf0, v74
v_mul_f32 v75, 0x42f5fdf0, v75
v_add_f32 v56, 1.0, v56
v_add_f32 v57, 1.0, v57
v_add_f32 v58, 1.0, v58
v_add_f32 v59, 1.0, v59
v_rcp_f32 v56, v56
v_rcp_f32 v57, v57
v_rcp_f32 v58, v58
v_rcp_f32 v59, v59
s_nop 0
v_mul_f32 v72, v72, v56
v_mul_f32 v73, v73, v57
v_mul_f32 v74, v74, v58
v_mul_f32 v75, v75, v59
s_waitcnt lgkmcnt(1)
v_lshlrev_b32 v56, 16, v244
v_and_b32 v57, s86, v244
v_lshlrev_b32 v58, 16, v245
v_and_b32 v59, s86, v245
buffer_load_dwordx4 v[188:191], v252, s[64:67], s95 offen offset:3072 sc1
v_mul_f32 v60, 0xc3317218, v76
v_mul_f32 v61, 0xc3317218, v77
v_mul_f32 v62, 0xc3317218, v78
v_mul_f32 v63, 0xc3317218, v79
v_exp_f32 v60, v60
v_exp_f32 v61, v61
v_exp_f32 v62, v62
v_exp_f32 v63, v63
v_mul_f32 v76, 0x42f5fdf0, v76
v_mul_f32 v77, 0x42f5fdf0, v77
v_mul_f32 v78, 0x42f5fdf0, v78
v_mul_f32 v79, 0x42f5fdf0, v79
v_add_f32 v60, 1.0, v60
v_add_f32 v61, 1.0, v61
v_add_f32 v62, 1.0, v62
v_add_f32 v63, 1.0, v63
v_rcp_f32 v60, v60
v_rcp_f32 v61, v61
v_rcp_f32 v62, v62
v_rcp_f32 v63, v63
s_nop 0
v_mul_f32 v76, v76, v60
v_mul_f32 v77, v77, v61
v_mul_f32 v78, v78, v62
v_mul_f32 v79, v79, v63
s_waitcnt lgkmcnt(0)
v_lshlrev_b32 v60, 16, v246
v_and_b32 v61, s86, v246
v_lshlrev_b32 v62, 16, v247
v_and_b32 v63, s86, v247
s_add_u32 s95, s95, 4096
buffer_load_dwordx4 v[192:195], v252, s[64:67], s95 offen offset:0 sc1
s_waitcnt vmcnt(24)
v_mfma_f32_16x16x32_bf16 v[32:35], a[170:173], v[96:99], 0
v_mfma_f32_16x16x32_bf16 v[36:39], a[168:171], v[96:99], 0
buffer_load_dwordx4 v[196:199], v252, s[64:67], s95 offen offset:1024 sc1
s_waitcnt vmcnt(24)
v_mfma_f32_16x16x32_bf16 v[40:43], a[170:173], v[100:103], 0
v_mfma_f32_16x16x32_bf16 v[44:47], a[168:171], v[100:103], 0
buffer_load_dwordx4 v[200:203], v252, s[64:67], s95 offen offset:2048 sc1
s_waitcnt vmcnt(24)
v_mfma_f32_16x16x32_bf16 v[32:35], a[170:173], v[104:107], v[32:35]
v_mfma_f32_16x16x32_bf16 v[36:39], a[168:171], v[104:107], v[36:39]
buffer_load_dwordx4 v[204:207], v252, s[64:67], s95 offen offset:3072 sc1
s_waitcnt vmcnt(24)
v_mfma_f32_16x16x32_bf16 v[40:43], a[170:173], v[108:111], v[40:43]
v_mfma_f32_16x16x32_bf16 v[44:47], a[168:171], v[108:111], v[44:47]
s_add_u32 s95, s95, 4096
buffer_load_dwordx4 v[208:211], v252, s[64:67], s95 offen offset:0 sc1
s_waitcnt vmcnt(24)
v_mfma_f32_16x16x32_bf16 v[32:35], a[170:173], v[112:115], v[32:35]
v_mfma_f32_16x16x32_bf16 v[36:39], a[168:171], v[112:115], v[36:39]
buffer_load_dwordx4 v[212:215], v252, s[64:67], s95 offen offset:1024 sc1
s_waitcnt vmcnt(24)
v_mfma_f32_16x16x32_bf16 v[40:43], a[170:173], v[116:119], v[40:43]
v_mfma_f32_16x16x32_bf16 v[44:47], a[168:171], v[116:119], v[44:47]
buffer_load_dwordx4 v[216:219], v252, s[64:67], s95 offen offset:2048 sc1
s_waitcnt vmcnt(24)
v_mfma_f32_16x16x32_bf16 v[32:35], a[170:173], v[120:123], v[32:35]
v_mfma_f32_16x16x32_bf16 v[36:39], a[168:171], v[120:123], v[36:39]
buffer_load_dwordx4 v[220:223], v252, s[64:67], s95 offen offset:3072 sc1
s_waitcnt vmcnt(24)
v_mfma_f32_16x16x32_bf16 v[40:43], a[170:173], v[124:127], v[40:43]
v_mfma_f32_16x16x32_bf16 v[44:47], a[168:171], v[124:127], v[44:47]
s_waitcnt vmcnt(23)
v_mfma_f32_16x16x32_bf16 v[32:35], a[170:173], v[128:131], v[32:35]
v_mfma_f32_16x16x32_bf16 v[36:39], a[168:171], v[128:131], v[36:39]
s_waitcnt vmcnt(22)
v_mfma_f32_16x16x32_bf16 v[40:43], a[170:173], v[132:135], v[40:43]
v_mfma_f32_16x16x32_bf16 v[44:47], a[168:171], v[132:135], v[44:47]
s_waitcnt vmcnt(21)
v_mfma_f32_16x16x32_bf16 v[32:35], a[170:173], v[136:139], v[32:35]
v_mfma_f32_16x16x32_bf16 v[36:39], a[168:171], v[136:139], v[36:39]
s_waitcnt vmcnt(20)
v_mfma_f32_16x16x32_bf16 v[40:43], a[170:173], v[140:143], v[40:43]
v_mfma_f32_16x16x32_bf16 v[44:47], a[168:171], v[140:143], v[44:47]
s_waitcnt vmcnt(19)
v_mfma_f32_16x16x32_bf16 v[32:35], a[170:173], v[144:147], v[32:35]
v_mfma_f32_16x16x32_bf16 v[36:39], a[168:171], v[144:147], v[36:39]
s_waitcnt vmcnt(18)
v_mfma_f32_16x16x32_bf16 v[40:43], a[170:173], v[148:151], v[40:43]
v_mfma_f32_16x16x32_bf16 v[44:47], a[168:171], v[148:151], v[44:47]
s_waitcnt vmcnt(17)
v_mfma_f32_16x16x32_bf16 v[32:35], a[170:173], v[152:155], v[32:35]
v_mfma_f32_16x16x32_bf16 v[36:39], a[168:171], v[152:155], v[36:39]
s_waitcnt vmcnt(16)
v_mfma_f32_16x16x32_bf16 v[40:43], a[170:173], v[156:159], v[40:43]
v_mfma_f32_16x16x32_bf16 v[44:47], a[168:171], v[156:159], v[44:47]
s_waitcnt vmcnt(15)
v_mfma_f32_16x16x32_bf16 v[32:35], a[170:173], v[160:163], v[32:35]
v_mfma_f32_16x16x32_bf16 v[36:39], a[168:171], v[160:163], v[36:39]
s_waitcnt vmcnt(14)
v_mfma_f32_16x16x32_bf16 v[40:43], a[170:173], v[164:167], v[40:43]
v_mfma_f32_16x16x32_bf16 v[44:47], a[168:171], v[164:167], v[44:47]
s_waitcnt vmcnt(13)
v_mfma_f32_16x16x32_bf16 v[32:35], a[170:173], v[168:171], v[32:35]
v_mfma_f32_16x16x32_bf16 v[36:39], a[168:171], v[168:171], v[36:39]
s_waitcnt vmcnt(12)
v_mfma_f32_16x16x32_bf16 v[40:43], a[170:173], v[172:175], v[40:43]
v_mfma_f32_16x16x32_bf16 v[44:47], a[168:171], v[172:175], v[44:47]
s_waitcnt vmcnt(11)
v_mfma_f32_16x16x32_bf16 v[32:35], a[170:173], v[176:179], v[32:35]
v_mfma_f32_16x16x32_bf16 v[36:39], a[168:171], v[176:179], v[36:39]
s_waitcnt vmcnt(10)
v_mfma_f32_16x16x32_bf16 v[40:43], a[170:173], v[180:183], v[40:43]
v_mfma_f32_16x16x32_bf16 v[44:47], a[168:171], v[180:183], v[44:47]
s_waitcnt vmcnt(9)
v_mfma_f32_16x16x32_bf16 v[32:35], a[170:173], v[184:187], v[32:35]
v_mfma_f32_16x16x32_bf16 v[36:39], a[168:171], v[184:187], v[36:39]
s_waitcnt vmcnt(8)
v_mfma_f32_16x16x32_bf16 v[40:43], a[170:173], v[188:191], v[40:43]
v_mfma_f32_16x16x32_bf16 v[44:47], a[168:171], v[188:191], v[44:47]
s_waitcnt vmcnt(7)
v_mfma_f32_16x16x32_bf16 v[32:35], a[170:173], v[192:195], v[32:35]
v_mfma_f32_16x16x32_bf16 v[36:39], a[168:171], v[192:195], v[36:39]
s_waitcnt vmcnt(6)
v_mfma_f32_16x16x32_bf16 v[40:43], a[170:173], v[196:199], v[40:43]
v_mfma_f32_16x16x32_bf16 v[44:47], a[168:171], v[196:199], v[44:47]
s_waitcnt vmcnt(5)
v_mfma_f32_16x16x32_bf16 v[32:35], a[170:173], v[200:203], v[32:35]
v_mfma_f32_16x16x32_bf16 v[36:39], a[168:171], v[200:203], v[36:39]
s_waitcnt vmcnt(4)
v_mfma_f32_16x16x32_bf16 v[40:43], a[170:173], v[204:207], v[40:43]
v_mfma_f32_16x16x32_bf16 v[44:47], a[168:171], v[204:207], v[44:47]
s_waitcnt vmcnt(3)
v_mfma_f32_16x16x32_bf16 v[32:35], a[170:173], v[208:211], v[32:35]
v_mfma_f32_16x16x32_bf16 v[36:39], a[168:171], v[208:211], v[36:39]
s_waitcnt vmcnt(2)
v_mfma_f32_16x16x32_bf16 v[40:43], a[170:173], v[212:215], v[40:43]
v_mfma_f32_16x16x32_bf16 v[44:47], a[168:171], v[212:215], v[44:47]
s_waitcnt vmcnt(1)
v_mfma_f32_16x16x32_bf16 v[32:35], a[170:173], v[216:219], v[32:35]
v_mfma_f32_16x16x32_bf16 v[36:39], a[168:171], v[216:219], v[36:39]
s_waitcnt vmcnt(0)
v_mfma_f32_16x16x32_bf16 v[40:43], a[170:173], v[220:223], v[40:43]
v_mfma_f32_16x16x32_bf16 v[44:47], a[168:171], v[220:223], v[44:47]
s_nop 3
v_fmac_f32 v48, v32, v64
v_fmac_f32 v49, v33, v65
v_fmac_f32 v50, v34, v66
v_fmac_f32 v51, v35, v67
v_cvt_pk_bf16_f32 v48, v48, v49
v_cvt_pk_bf16_f32 v49, v50, v51
v_fmac_f32 v52, v36, v68
v_fmac_f32 v53, v37, v69
v_fmac_f32 v54, v38, v70
v_fmac_f32 v55, v39, v71
v_cvt_pk_bf16_f32 v50, v52, v53
v_cvt_pk_bf16_f32 v51, v54, v55
s_nop 0
v_permlane16_swap_b32 v48, v50
v_permlane16_swap_b32 v49, v51
buffer_store_dwordx4 v[48:51], v239, s[40:43], 0 offen offset:0 sc1
v_fmac_f32 v56, v40, v72
v_fmac_f32 v57, v41, v73
v_fmac_f32 v58, v42, v74
v_fmac_f32 v59, v43, v75
v_cvt_pk_bf16_f32 v56, v56, v57
v_cvt_pk_bf16_f32 v57, v58, v59
v_fmac_f32 v60, v44, v76
v_fmac_f32 v61, v45, v77
v_fmac_f32 v62, v46, v78
v_fmac_f32 v63, v47, v79
v_cvt_pk_bf16_f32 v58, v60, v61
v_cvt_pk_bf16_f32 v59, v62, v63
s_nop 0
v_permlane16_swap_b32 v56, v58
v_permlane16_swap_b32 v57, v59
buffer_store_dwordx4 v[56:59], v239, s[40:43], 0 offen offset:64 sc1
s_waitcnt vmcnt(0)
s_branch L_end
L_spm:
s_mov_b32 s97, 200000
L_spm_l:
s_sleep 1
buffer_load_dword v236, v234, s[64:67], s82 offen offset:0 sc1
s_waitcnt vmcnt(0)
v_cmp_ne_u32 vcc, v236, v232
s_sub_u32 s97, s97, 1
s_cbranch_scc1 L_bkmx
s_cbranch_vccnz L_spm_l
L_bkmx:
s_branch L_bkm
L_re:
v_mov_b32 v233, v237
buffer_store_dword v233, v235, s[64:67], s83 offen sc1
s_waitcnt vmcnt(0)
s_branch L_reb
L_rareA0:
s_nop 7
v_mov_b32 v250, v248
s_nop 1
v_permlane16_swap_b32 v248, v250
v_max_f32 v248, v248, v250
v_mov_b32 v250, v248
s_nop 1
v_permlane32_swap_b32 v248, v250
v_max_f32 v248, v248, v250
v_mov_b32 v76, v248
v_mov_b32 v80, v248
v_sub_f32 v0, v0, v248
v_sub_f32 v1, v1, v248
v_sub_f32 v2, v2, v248
v_sub_f32 v3, v3, v248
v_sub_f32 v8, v8, v248
v_sub_f32 v9, v9, v248
v_sub_f32 v10, v10, v248
v_sub_f32 v11, v11, v248
v_sub_f32 v16, v16, v248
v_sub_f32 v17, v17, v248
v_sub_f32 v18, v18, v248
v_sub_f32 v19, v19, v248
v_sub_f32 v24, v24, v248
v_sub_f32 v25, v25, v248
v_sub_f32 v26, v26, v248
v_sub_f32 v27, v27, v248
v_sub_f32 v64, 0, v76
v_sub_f32 v65, 0, v76
v_sub_f32 v66, 0, v76
v_sub_f32 v67, 0, v76
v_mov_b32 v250, v249
s_nop 1
v_permlane16_swap_b32 v249, v250
v_max_f32 v249, v249, v250
v_mov_b32 v250, v249
s_nop 1
v_permlane32_swap_b32 v249, v250
v_max_f32 v249, v249, v250
v_mov_b32 v77, v249
v_mov_b32 v81, v249
v_sub_f32 v4, v4, v249
v_sub_f32 v5, v5, v249
v_sub_f32 v6, v6, v249
v_sub_f32 v7, v7, v249
v_sub_f32 v12, v12, v249
v_sub_f32 v13, v13, v249
v_sub_f32 v14, v14, v249
v_sub_f32 v15, v15, v249
v_sub_f32 v20, v20, v249
v_sub_f32 v21, v21, v249
v_sub_f32 v22, v22, v249
v_sub_f32 v23, v23, v249
v_sub_f32 v28, v28, v249
v_sub_f32 v29, v29, v249
v_sub_f32 v30, v30, v249
v_sub_f32 v31, v31, v249
v_sub_f32 v68, 0, v77
v_sub_f32 v69, 0, v77
v_sub_f32 v70, 0, v77
v_sub_f32 v71, 0, v77
s_mov_b32 s84, 1
s_nop 4
s_branch L_backA0
L_rareB0:
s_nop 15
v_sub_f32 v32, v32, v80
v_sub_f32 v33, v33, v80
v_sub_f32 v34, v34, v80
v_sub_f32 v35, v35, v80
v_sub_f32 v36, v36, v81
v_sub_f32 v37, v37, v81
v_sub_f32 v38, v38, v81
v_sub_f32 v39, v39, v81
s_mov_b32 s84, 0
s_nop 4
s_branch L_backB0
L_rareA1:
s_nop 7
v_mov_b32 v250, v248
s_nop 1
v_permlane16_swap_b32 v248, v250
v_max_f32 v248, v248, v250
v_mov_b32 v250, v248
s_nop 1
v_permlane32_swap_b32 v248, v250
v_max_f32 v248, v248, v250
v_max_f32 v248, 0, v248
v_add_f32 v76, v76, v248
v_sub_f32 v80, 0, v248
v_exp_f32 v80, v80
v_sub_f32 v32, v32, v248
v_sub_f32 v33, v33, v248
v_sub_f32 v34, v34, v248
v_sub_f32 v35, v35, v248
v_sub_f32 v40, v40, v248
v_sub_f32 v41, v41, v248
v_sub_f32 v42, v42, v248
v_sub_f32 v43, v43, v248
v_sub_f32 v48, v48, v248
v_sub_f32 v49, v49, v248
v_sub_f32 v50, v50, v248
v_sub_f32 v51, v51, v248
v_sub_f32 v56, v56, v248
v_sub_f32 v57, v57, v248
v_sub_f32 v58, v58, v248
v_sub_f32 v59, v59, v248
v_sub_f32 v64, 0, v76
v_sub_f32 v65, 0, v76
v_sub_f32 v66, 0, v76
v_sub_f32 v67, 0, v76
v_mov_b32 v250, v249
s_nop 1
v_permlane16_swap_b32 v249, v250
v_max_f32 v249, v249, v250
v_mov_b32 v250, v249
s_nop 1
v_permlane32_swap_b32 v249, v250
v_max_f32 v249, v249, v250
v_max_f32 v249, 0, v249
v_add_f32 v77, v77, v249
v_sub_f32 v81, 0, v249
v_exp_f32 v81, v81
v_sub_f32 v36, v36, v249
v_sub_f32 v37, v37, v249
v_sub_f32 v38, v38, v249
v_sub_f32 v39, v39, v249
v_sub_f32 v44, v44, v249
v_sub_f32 v45, v45, v249
v_sub_f32 v46, v46, v249
v_sub_f32 v47, v47, v249
v_sub_f32 v52, v52, v249
v_sub_f32 v53, v53, v249
v_sub_f32 v54, v54, v249
v_sub_f32 v55, v55, v249
v_sub_f32 v60, v60, v249
v_sub_f32 v61, v61, v249
v_sub_f32 v62, v62, v249
v_sub_f32 v63, v63, v249
v_sub_f32 v68, 0, v77
v_sub_f32 v69, 0, v77
v_sub_f32 v70, 0, v77
v_sub_f32 v71, 0, v77
s_mov_b32 s84, 1
s_nop 4
s_branch L_backA1
L_rareB1:
s_nop 15
v_mul_f32 v96, v96, v80
v_mul_f32 v97, v97, v80
v_mul_f32 v98, v98, v80
v_mul_f32 v99, v99, v80
v_mul_f32 v100, v100, v81
v_mul_f32 v101, v101, v81
v_mul_f32 v102, v102, v81
v_mul_f32 v103, v103, v81
v_mul_f32 v104, v104, v80
v_mul_f32 v105, v105, v80
v_mul_f32 v106, v106, v80
v_mul_f32 v107, v107, v80
v_mul_f32 v108, v108, v81
v_mul_f32 v109, v109, v81
v_mul_f32 v110, v110, v81
v_mul_f32 v111, v111, v81
v_mul_f32 v112, v112, v80
v_mul_f32 v113, v113, v80
v_mul_f32 v114, v114, v80
v_mul_f32 v115, v115, v80
v_mul_f32 v116, v116, v81
v_mul_f32 v117, v117, v81
v_mul_f32 v118, v118, v81
v_mul_f32 v119, v119, v81
v_mul_f32 v120, v120, v80
v_mul_f32 v121, v121, v80
v_mul_f32 v122, v122, v80
v_mul_f32 v123, v123, v80
v_mul_f32 v124, v124, v81
v_mul_f32 v125, v125, v81
v_mul_f32 v126, v126, v81
v_mul_f32 v127, v127, v81
v_mul_f32 v128, v128, v80
v_mul_f32 v129, v129, v80
v_mul_f32 v130, v130, v80
v_mul_f32 v131, v131, v80
v_mul_f32 v132, v132, v81
v_mul_f32 v133, v133, v81
v_mul_f32 v134, v134, v81
v_mul_f32 v135, v135, v81
v_mul_f32 v136, v136, v80
v_mul_f32 v137, v137, v80
v_mul_f32 v138, v138, v80
v_mul_f32 v139, v139, v80
v_mul_f32 v140, v140, v81
v_mul_f32 v141, v141, v81
v_mul_f32 v142, v142, v81
v_mul_f32 v143, v143, v81
v_mul_f32 v144, v144, v80
v_mul_f32 v145, v145, v80
v_mul_f32 v146, v146, v80
v_mul_f32 v147, v147, v80
v_mul_f32 v148, v148, v81
v_mul_f32 v149, v149, v81
v_mul_f32 v150, v150, v81
v_mul_f32 v151, v151, v81
v_mul_f32 v152, v152, v80
v_mul_f32 v153, v153, v80
v_mul_f32 v154, v154, v80
v_mul_f32 v155, v155, v80
v_mul_f32 v156, v156, v81
v_mul_f32 v157, v157, v81
v_mul_f32 v158, v158, v81
v_mul_f32 v159, v159, v81
v_mul_f32 v160, v160, v80
v_mul_f32 v161, v161, v80
v_mul_f32 v162, v162, v80
v_mul_f32 v163, v163, v80
v_mul_f32 v164, v164, v81
v_mul_f32 v165, v165, v81
v_mul_f32 v166, v166, v81
v_mul_f32 v167, v167, v81
v_mul_f32 v168, v168, v80
v_mul_f32 v169, v169, v80
v_mul_f32 v170, v170, v80
v_mul_f32 v171, v171, v80
v_mul_f32 v172, v172, v81
v_mul_f32 v173, v173, v81
v_mul_f32 v174, v174, v81
v_mul_f32 v175, v175, v81
v_mul_f32 v176, v176, v80
v_mul_f32 v177, v177, v80
v_mul_f32 v178, v178, v80
v_mul_f32 v179, v179, v80
v_mul_f32 v180, v180, v81
v_mul_f32 v181, v181, v81
v_mul_f32 v182, v182, v81
v_mul_f32 v183, v183, v81
v_mul_f32 v184, v184, v80
v_mul_f32 v185, v185, v80
v_mul_f32 v186, v186, v80
v_mul_f32 v187, v187, v80
v_mul_f32 v188, v188, v81
v_mul_f32 v189, v189, v81
v_mul_f32 v190, v190, v81
v_mul_f32 v191, v191, v81
v_mul_f32 v192, v192, v80
v_mul_f32 v193, v193, v80
v_mul_f32 v194, v194, v80
v_mul_f32 v195, v195, v80
v_mul_f32 v196, v196, v81
v_mul_f32 v197, v197, v81
v_mul_f32 v198, v198, v81
v_mul_f32 v199, v199, v81
v_mul_f32 v200, v200, v80
v_mul_f32 v201, v201, v80
v_mul_f32 v202, v202, v80
v_mul_f32 v203, v203, v80
v_mul_f32 v204, v204, v81
v_mul_f32 v205, v205, v81
v_mul_f32 v206, v206, v81
v_mul_f32 v207, v207, v81
v_mul_f32 v208, v208, v80
v_mul_f32 v209, v209, v80
v_mul_f32 v210, v210, v80
v_mul_f32 v211, v211, v80
v_mul_f32 v212, v212, v81
v_mul_f32 v213, v213, v81
v_mul_f32 v214, v214, v81
v_mul_f32 v215, v215, v81
v_mul_f32 v216, v216, v80
v_mul_f32 v217, v217, v80
v_mul_f32 v218, v218, v80
v_mul_f32 v219, v219, v80
v_mul_f32 v220, v220, v81
v_mul_f32 v221, v221, v81
v_mul_f32 v222, v222, v81
v_mul_f32 v223, v223, v81
v_accvgpr_read_b32 v78, a192
v_mul_f32 v78, v78, v80
v_accvgpr_write_b32 a192, v78
v_accvgpr_read_b32 v79, a196
v_mul_f32 v79, v79, v81
v_accvgpr_write_b32 a196, v79
s_mov_b32 s84, 0
s_nop 4
s_branch L_backB1
L_rareA2:
s_nop 7
v_mov_b32 v250, v248
s_nop 1
v_permlane16_swap_b32 v248, v250
v_max_f32 v248, v248, v250
v_mov_b32 v250, v248
s_nop 1
v_permlane32_swap_b32 v248, v250
v_max_f32 v248, v248, v250
v_max_f32 v248, 0, v248
v_add_f32 v76, v76, v248
v_sub_f32 v80, 0, v248
v_exp_f32 v80, v80
v_sub_f32 v0, v0, v248
v_sub_f32 v1, v1, v248
v_sub_f32 v2, v2, v248
v_sub_f32 v3, v3, v248
v_sub_f32 v8, v8, v248
v_sub_f32 v9, v9, v248
v_sub_f32 v10, v10, v248
v_sub_f32 v11, v11, v248
v_sub_f32 v16, v16, v248
v_sub_f32 v17, v17, v248
v_sub_f32 v18, v18, v248
v_sub_f32 v19, v19, v248
v_sub_f32 v24, v24, v248
v_sub_f32 v25, v25, v248
v_sub_f32 v26, v26, v248
v_sub_f32 v27, v27, v248
v_sub_f32 v64, 0, v76
v_sub_f32 v65, 0, v76
v_sub_f32 v66, 0, v76
v_sub_f32 v67, 0, v76
v_mov_b32 v250, v249
s_nop 1
v_permlane16_swap_b32 v249, v250
v_max_f32 v249, v249, v250
v_mov_b32 v250, v249
s_nop 1
v_permlane32_swap_b32 v249, v250
v_max_f32 v249, v249, v250
v_max_f32 v249, 0, v249
v_add_f32 v77, v77, v249
v_sub_f32 v81, 0, v249
v_exp_f32 v81, v81
v_sub_f32 v4, v4, v249
v_sub_f32 v5, v5, v249
v_sub_f32 v6, v6, v249
v_sub_f32 v7, v7, v249
v_sub_f32 v12, v12, v249
v_sub_f32 v13, v13, v249
v_sub_f32 v14, v14, v249
v_sub_f32 v15, v15, v249
v_sub_f32 v20, v20, v249
v_sub_f32 v21, v21, v249
v_sub_f32 v22, v22, v249
v_sub_f32 v23, v23, v249
v_sub_f32 v28, v28, v249
v_sub_f32 v29, v29, v249
v_sub_f32 v30, v30, v249
v_sub_f32 v31, v31, v249
v_sub_f32 v68, 0, v77
v_sub_f32 v69, 0, v77
v_sub_f32 v70, 0, v77
v_sub_f32 v71, 0, v77
s_mov_b32 s84, 1
s_nop 4
s_branch L_backA2
L_rareB2:
s_nop 15
v_mul_f32 v96, v96, v80
v_mul_f32 v97, v97, v80
v_mul_f32 v98, v98, v80
v_mul_f32 v99, v99, v80
v_mul_f32 v100, v100, v81
v_mul_f32 v101, v101, v81
v_mul_f32 v102, v102, v81
v_mul_f32 v103, v103, v81
v_mul_f32 v104, v104, v80
v_mul_f32 v105, v105, v80
v_mul_f32 v106, v106, v80
v_mul_f32 v107, v107, v80
v_mul_f32 v108, v108, v81
v_mul_f32 v109, v109, v81
v_mul_f32 v110, v110, v81
v_mul_f32 v111, v111, v81
v_mul_f32 v112, v112, v80
v_mul_f32 v113, v113, v80
v_mul_f32 v114, v114, v80
v_mul_f32 v115, v115, v80
v_mul_f32 v116, v116, v81
v_mul_f32 v117, v117, v81
v_mul_f32 v118, v118, v81
v_mul_f32 v119, v119, v81
v_mul_f32 v120, v120, v80
v_mul_f32 v121, v121, v80
v_mul_f32 v122, v122, v80
v_mul_f32 v123, v123, v80
v_mul_f32 v124, v124, v81
v_mul_f32 v125, v125, v81
v_mul_f32 v126, v126, v81
v_mul_f32 v127, v127, v81
v_mul_f32 v128, v128, v80
v_mul_f32 v129, v129, v80
v_mul_f32 v130, v130, v80
v_mul_f32 v131, v131, v80
v_mul_f32 v132, v132, v81
v_mul_f32 v133, v133, v81
v_mul_f32 v134, v134, v81
v_mul_f32 v135, v135, v81
v_mul_f32 v136, v136, v80
v_mul_f32 v137, v137, v80
v_mul_f32 v138, v138, v80
v_mul_f32 v139, v139, v80
v_mul_f32 v140, v140, v81
v_mul_f32 v141, v141, v81
v_mul_f32 v142, v142, v81
v_mul_f32 v143, v143, v81
v_mul_f32 v144, v144, v80
v_mul_f32 v145, v145, v80
v_mul_f32 v146, v146, v80
v_mul_f32 v147, v147, v80
v_mul_f32 v148, v148, v81
v_mul_f32 v149, v149, v81
v_mul_f32 v150, v150, v81
v_mul_f32 v151, v151, v81
v_mul_f32 v152, v152, v80
v_mul_f32 v153, v153, v80
v_mul_f32 v154, v154, v80
v_mul_f32 v155, v155, v80
v_mul_f32 v156, v156, v81
v_mul_f32 v157, v157, v81
v_mul_f32 v158, v158, v81
v_mul_f32 v159, v159, v81
v_mul_f32 v160, v160, v80
v_mul_f32 v161, v161, v80
v_mul_f32 v162, v162, v80
v_mul_f32 v163, v163, v80
v_mul_f32 v164, v164, v81
v_mul_f32 v165, v165, v81
v_mul_f32 v166, v166, v81
v_mul_f32 v167, v167, v81
v_mul_f32 v168, v168, v80
v_mul_f32 v169, v169, v80
v_mul_f32 v170, v170, v80
v_mul_f32 v171, v171, v80
v_mul_f32 v172, v172, v81
v_mul_f32 v173, v173, v81
v_mul_f32 v174, v174, v81
v_mul_f32 v175, v175, v81
v_mul_f32 v176, v176, v80
v_mul_f32 v177, v177, v80
v_mul_f32 v178, v178, v80
v_mul_f32 v179, v179, v80
v_mul_f32 v180, v180, v81
v_mul_f32 v181, v181, v81
v_mul_f32 v182, v182, v81
v_mul_f32 v183, v183, v81
v_mul_f32 v184, v184, v80
v_mul_f32 v185, v185, v80
v_mul_f32 v186, v186, v80
v_mul_f32 v187, v187, v80
v_mul_f32 v188, v188, v81
v_mul_f32 v189, v189, v81
v_mul_f32 v190, v190, v81
v_mul_f32 v191, v191, v81
v_mul_f32 v192, v192, v80
v_mul_f32 v193, v193, v80
v_mul_f32 v194, v194, v80
v_mul_f32 v195, v195, v80
v_mul_f32 v196, v196, v81
v_mul_f32 v197, v197, v81
v_mul_f32 v198, v198, v81
v_mul_f32 v199, v199, v81
v_mul_f32 v200, v200, v80
v_mul_f32 v201, v201, v80
v_mul_f32 v202, v202, v80
v_mul_f32 v203, v203, v80
v_mul_f32 v204, v204, v81
v_mul_f32 v205, v205, v81
v_mul_f32 v206, v206, v81
v_mul_f32 v207, v207, v81
v_mul_f32 v208, v208, v80
v_mul_f32 v209, v209, v80
v_mul_f32 v210, v210, v80
v_mul_f32 v211, v211, v80
v_mul_f32 v212, v212, v81
v_mul_f32 v213, v213, v81
v_mul_f32 v214, v214, v81
v_mul_f32 v215, v215, v81
v_mul_f32 v216, v216, v80
v_mul_f32 v217, v217, v80
v_mul_f32 v218, v218, v80
v_mul_f32 v219, v219, v80
v_mul_f32 v220, v220, v81
v_mul_f32 v221, v221, v81
v_mul_f32 v222, v222, v81
v_mul_f32 v223, v223, v81
v_accvgpr_read_b32 v78, a192
v_mul_f32 v78, v78, v80
v_accvgpr_write_b32 a192, v78
v_accvgpr_read_b32 v79, a196
v_mul_f32 v79, v79, v81
v_accvgpr_write_b32 a196, v79
s_mov_b32 s84, 0
s_nop 4
s_branch L_backB2
L_rareA3:
s_nop 7
v_mov_b32 v250, v248
s_nop 1
v_permlane16_swap_b32 v248, v250
v_max_f32 v248, v248, v250
v_mov_b32 v250, v248
s_nop 1
v_permlane32_swap_b32 v248, v250
v_max_f32 v248, v248, v250
v_max_f32 v248, 0, v248
v_add_f32 v76, v76, v248
v_sub_f32 v80, 0, v248
v_exp_f32 v80, v80
v_sub_f32 v32, v32, v248
v_sub_f32 v33, v33, v248
v_sub_f32 v34, v34, v248
v_sub_f32 v35, v35, v248
v_sub_f32 v40, v40, v248
v_sub_f32 v41, v41, v248
v_sub_f32 v42, v42, v248
v_sub_f32 v43, v43, v248
v_sub_f32 v48, v48, v248
v_sub_f32 v49, v49, v248
v_sub_f32 v50, v50, v248
v_sub_f32 v51, v51, v248
v_sub_f32 v56, v56, v248
v_sub_f32 v57, v57, v248
v_sub_f32 v58, v58, v248
v_sub_f32 v59, v59, v248
v_sub_f32 v64, 0, v76
v_sub_f32 v65, 0, v76
v_sub_f32 v66, 0, v76
v_sub_f32 v67, 0, v76
v_mov_b32 v250, v249
s_nop 1
v_permlane16_swap_b32 v249, v250
v_max_f32 v249, v249, v250
v_mov_b32 v250, v249
s_nop 1
v_permlane32_swap_b32 v249, v250
v_max_f32 v249, v249, v250
v_max_f32 v249, 0, v249
v_add_f32 v77, v77, v249
v_sub_f32 v81, 0, v249
v_exp_f32 v81, v81
v_sub_f32 v36, v36, v249
v_sub_f32 v37, v37, v249
v_sub_f32 v38, v38, v249
v_sub_f32 v39, v39, v249
v_sub_f32 v44, v44, v249
v_sub_f32 v45, v45, v249
v_sub_f32 v46, v46, v249
v_sub_f32 v47, v47, v249
v_sub_f32 v52, v52, v249
v_sub_f32 v53, v53, v249
v_sub_f32 v54, v54, v249
v_sub_f32 v55, v55, v249
v_sub_f32 v60, v60, v249
v_sub_f32 v61, v61, v249
v_sub_f32 v62, v62, v249
v_sub_f32 v63, v63, v249
v_sub_f32 v68, 0, v77
v_sub_f32 v69, 0, v77
v_sub_f32 v70, 0, v77
v_sub_f32 v71, 0, v77
s_mov_b32 s84, 1
s_nop 4
s_branch L_backA3
L_rareB3:
s_nop 15
v_mul_f32 v96, v96, v80
v_mul_f32 v97, v97, v80
v_mul_f32 v98, v98, v80
v_mul_f32 v99, v99, v80
v_mul_f32 v100, v100, v81
v_mul_f32 v101, v101, v81
v_mul_f32 v102, v102, v81
v_mul_f32 v103, v103, v81
v_mul_f32 v104, v104, v80
v_mul_f32 v105, v105, v80
v_mul_f32 v106, v106, v80
v_mul_f32 v107, v107, v80
v_mul_f32 v108, v108, v81
v_mul_f32 v109, v109, v81
v_mul_f32 v110, v110, v81
v_mul_f32 v111, v111, v81
v_mul_f32 v112, v112, v80
v_mul_f32 v113, v113, v80
v_mul_f32 v114, v114, v80
v_mul_f32 v115, v115, v80
v_mul_f32 v116, v116, v81
v_mul_f32 v117, v117, v81
v_mul_f32 v118, v118, v81
v_mul_f32 v119, v119, v81
v_mul_f32 v120, v120, v80
v_mul_f32 v121, v121, v80
v_mul_f32 v122, v122, v80
v_mul_f32 v123, v123, v80
v_mul_f32 v124, v124, v81
v_mul_f32 v125, v125, v81
v_mul_f32 v126, v126, v81
v_mul_f32 v127, v127, v81
v_mul_f32 v128, v128, v80
v_mul_f32 v129, v129, v80
v_mul_f32 v130, v130, v80
v_mul_f32 v131, v131, v80
v_mul_f32 v132, v132, v81
v_mul_f32 v133, v133, v81
v_mul_f32 v134, v134, v81
v_mul_f32 v135, v135, v81
v_mul_f32 v136, v136, v80
v_mul_f32 v137, v137, v80
v_mul_f32 v138, v138, v80
v_mul_f32 v139, v139, v80
v_mul_f32 v140, v140, v81
v_mul_f32 v141, v141, v81
v_mul_f32 v142, v142, v81
v_mul_f32 v143, v143, v81
v_mul_f32 v144, v144, v80
v_mul_f32 v145, v145, v80
v_mul_f32 v146, v146, v80
v_mul_f32 v147, v147, v80
v_mul_f32 v148, v148, v81
v_mul_f32 v149, v149, v81
v_mul_f32 v150, v150, v81
v_mul_f32 v151, v151, v81
v_mul_f32 v152, v152, v80
v_mul_f32 v153, v153, v80
v_mul_f32 v154, v154, v80
v_mul_f32 v155, v155, v80
v_mul_f32 v156, v156, v81
v_mul_f32 v157, v157, v81
v_mul_f32 v158, v158, v81
v_mul_f32 v159, v159, v81
v_mul_f32 v160, v160, v80
v_mul_f32 v161, v161, v80
v_mul_f32 v162, v162, v80
v_mul_f32 v163, v163, v80
v_mul_f32 v164, v164, v81
v_mul_f32 v165, v165, v81
v_mul_f32 v166, v166, v81
v_mul_f32 v167, v167, v81
v_mul_f32 v168, v168, v80
v_mul_f32 v169, v169, v80
v_mul_f32 v170, v170, v80
v_mul_f32 v171, v171, v80
v_mul_f32 v172, v172, v81
v_mul_f32 v173, v173, v81
v_mul_f32 v174, v174, v81
v_mul_f32 v175, v175, v81
v_mul_f32 v176, v176, v80
v_mul_f32 v177, v177, v80
v_mul_f32 v178, v178, v80
v_mul_f32 v179, v179, v80
v_mul_f32 v180, v180, v81
v_mul_f32 v181, v181, v81
v_mul_f32 v182, v182, v81
v_mul_f32 v183, v183, v81
v_mul_f32 v184, v184, v80
v_mul_f32 v185, v185, v80
v_mul_f32 v186, v186, v80
v_mul_f32 v187, v187, v80
v_mul_f32 v188, v188, v81
v_mul_f32 v189, v189, v81
v_mul_f32 v190, v190, v81
v_mul_f32 v191, v191, v81
v_mul_f32 v192, v192, v80
v_mul_f32 v193, v193, v80
v_mul_f32 v194, v194, v80
v_mul_f32 v195, v195, v80
v_mul_f32 v196, v196, v81
v_mul_f32 v197, v197, v81
v_mul_f32 v198, v198, v81
v_mul_f32 v199, v199, v81
v_mul_f32 v200, v200, v80
v_mul_f32 v201, v201, v80
v_mul_f32 v202, v202, v80
v_mul_f32 v203, v203, v80
v_mul_f32 v204, v204, v81
v_mul_f32 v205, v205, v81
v_mul_f32 v206, v206, v81
v_mul_f32 v207, v207, v81
v_mul_f32 v208, v208, v80
v_mul_f32 v209, v209, v80
v_mul_f32 v210, v210, v80
v_mul_f32 v211, v211, v80
v_mul_f32 v212, v212, v81
v_mul_f32 v213, v213, v81
v_mul_f32 v214, v214, v81
v_mul_f32 v215, v215, v81
v_mul_f32 v216, v216, v80
v_mul_f32 v217, v217, v80
v_mul_f32 v218, v218, v80
v_mul_f32 v219, v219, v80
v_mul_f32 v220, v220, v81
v_mul_f32 v221, v221, v81
v_mul_f32 v222, v222, v81
v_mul_f32 v223, v223, v81
v_accvgpr_read_b32 v78, a192
v_mul_f32 v78, v78, v80
v_accvgpr_write_b32 a192, v78
v_accvgpr_read_b32 v79, a196
v_mul_f32 v79, v79, v81
v_accvgpr_write_b32 a196, v79
s_mov_b32 s84, 0
s_nop 4
s_branch L_backB3
L_rareA4:
s_nop 7
v_mov_b32 v250, v248
s_nop 1
v_permlane16_swap_b32 v248, v250
v_max_f32 v248, v248, v250
v_mov_b32 v250, v248
s_nop 1
v_permlane32_swap_b32 v248, v250
v_max_f32 v248, v248, v250
v_max_f32 v248, 0, v248
v_add_f32 v76, v76, v248
v_sub_f32 v80, 0, v248
v_exp_f32 v80, v80
v_sub_f32 v0, v0, v248
v_sub_f32 v1, v1, v248
v_sub_f32 v2, v2, v248
v_sub_f32 v3, v3, v248
v_sub_f32 v8, v8, v248
v_sub_f32 v9, v9, v248
v_sub_f32 v10, v10, v248
v_sub_f32 v11, v11, v248
v_sub_f32 v16, v16, v248
v_sub_f32 v17, v17, v248
v_sub_f32 v18, v18, v248
v_sub_f32 v19, v19, v248
v_sub_f32 v24, v24, v248
v_sub_f32 v25, v25, v248
v_sub_f32 v26, v26, v248
v_sub_f32 v27, v27, v248
v_sub_f32 v64, 0, v76
v_sub_f32 v65, 0, v76
v_sub_f32 v66, 0, v76
v_sub_f32 v67, 0, v76
v_mov_b32 v250, v249
s_nop 1
v_permlane16_swap_b32 v249, v250
v_max_f32 v249, v249, v250
v_mov_b32 v250, v249
s_nop 1
v_permlane32_swap_b32 v249, v250
v_max_f32 v249, v249, v250
v_max_f32 v249, 0, v249
v_add_f32 v77, v77, v249
v_sub_f32 v81, 0, v249
v_exp_f32 v81, v81
v_sub_f32 v4, v4, v249
v_sub_f32 v5, v5, v249
v_sub_f32 v6, v6, v249
v_sub_f32 v7, v7, v249
v_sub_f32 v12, v12, v249
v_sub_f32 v13, v13, v249
v_sub_f32 v14, v14, v249
v_sub_f32 v15, v15, v249
v_sub_f32 v20, v20, v249
v_sub_f32 v21, v21, v249
v_sub_f32 v22, v22, v249
v_sub_f32 v23, v23, v249
v_sub_f32 v28, v28, v249
v_sub_f32 v29, v29, v249
v_sub_f32 v30, v30, v249
v_sub_f32 v31, v31, v249
v_sub_f32 v68, 0, v77
v_sub_f32 v69, 0, v77
v_sub_f32 v70, 0, v77
v_sub_f32 v71, 0, v77
s_mov_b32 s84, 1
s_nop 4
s_branch L_backA4
L_rareB4:
s_nop 15
v_mul_f32 v96, v96, v80
v_mul_f32 v97, v97, v80
v_mul_f32 v98, v98, v80
v_mul_f32 v99, v99, v80
v_mul_f32 v100, v100, v81
v_mul_f32 v101, v101, v81
v_mul_f32 v102, v102, v81
v_mul_f32 v103, v103, v81
v_mul_f32 v104, v104, v80
v_mul_f32 v105, v105, v80
v_mul_f32 v106, v106, v80
v_mul_f32 v107, v107, v80
v_mul_f32 v108, v108, v81
v_mul_f32 v109, v109, v81
v_mul_f32 v110, v110, v81
v_mul_f32 v111, v111, v81
v_mul_f32 v112, v112, v80
v_mul_f32 v113, v113, v80
v_mul_f32 v114, v114, v80
v_mul_f32 v115, v115, v80
v_mul_f32 v116, v116, v81
v_mul_f32 v117, v117, v81
v_mul_f32 v118, v118, v81
v_mul_f32 v119, v119, v81
v_mul_f32 v120, v120, v80
v_mul_f32 v121, v121, v80
v_mul_f32 v122, v122, v80
v_mul_f32 v123, v123, v80
v_mul_f32 v124, v124, v81
v_mul_f32 v125, v125, v81
v_mul_f32 v126, v126, v81
v_mul_f32 v127, v127, v81
v_mul_f32 v128, v128, v80
v_mul_f32 v129, v129, v80
v_mul_f32 v130, v130, v80
v_mul_f32 v131, v131, v80
v_mul_f32 v132, v132, v81
v_mul_f32 v133, v133, v81
v_mul_f32 v134, v134, v81
v_mul_f32 v135, v135, v81
v_mul_f32 v136, v136, v80
v_mul_f32 v137, v137, v80
v_mul_f32 v138, v138, v80
v_mul_f32 v139, v139, v80
v_mul_f32 v140, v140, v81
v_mul_f32 v141, v141, v81
v_mul_f32 v142, v142, v81
v_mul_f32 v143, v143, v81
v_mul_f32 v144, v144, v80
v_mul_f32 v145, v145, v80
v_mul_f32 v146, v146, v80
v_mul_f32 v147, v147, v80
v_mul_f32 v148, v148, v81
v_mul_f32 v149, v149, v81
v_mul_f32 v150, v150, v81
v_mul_f32 v151, v151, v81
v_mul_f32 v152, v152, v80
v_mul_f32 v153, v153, v80
v_mul_f32 v154, v154, v80
v_mul_f32 v155, v155, v80
v_mul_f32 v156, v156, v81
v_mul_f32 v157, v157, v81
v_mul_f32 v158, v158, v81
v_mul_f32 v159, v159, v81
v_mul_f32 v160, v160, v80
v_mul_f32 v161, v161, v80
v_mul_f32 v162, v162, v80
v_mul_f32 v163, v163, v80
v_mul_f32 v164, v164, v81
v_mul_f32 v165, v165, v81
v_mul_f32 v166, v166, v81
v_mul_f32 v167, v167, v81
v_mul_f32 v168, v168, v80
v_mul_f32 v169, v169, v80
v_mul_f32 v170, v170, v80
v_mul_f32 v171, v171, v80
v_mul_f32 v172, v172, v81
v_mul_f32 v173, v173, v81
v_mul_f32 v174, v174, v81
v_mul_f32 v175, v175, v81
v_mul_f32 v176, v176, v80
v_mul_f32 v177, v177, v80
v_mul_f32 v178, v178, v80
v_mul_f32 v179, v179, v80
v_mul_f32 v180, v180, v81
v_mul_f32 v181, v181, v81
v_mul_f32 v182, v182, v81
v_mul_f32 v183, v183, v81
v_mul_f32 v184, v184, v80
v_mul_f32 v185, v185, v80
v_mul_f32 v186, v186, v80
v_mul_f32 v187, v187, v80
v_mul_f32 v188, v188, v81
v_mul_f32 v189, v189, v81
v_mul_f32 v190, v190, v81
v_mul_f32 v191, v191, v81
v_mul_f32 v192, v192, v80
v_mul_f32 v193, v193, v80
v_mul_f32 v194, v194, v80
v_mul_f32 v195, v195, v80
v_mul_f32 v196, v196, v81
v_mul_f32 v197, v197, v81
v_mul_f32 v198, v198, v81
v_mul_f32 v199, v199, v81
v_mul_f32 v200, v200, v80
v_mul_f32 v201, v201, v80
v_mul_f32 v202, v202, v80
v_mul_f32 v203, v203, v80
v_mul_f32 v204, v204, v81
v_mul_f32 v205, v205, v81
v_mul_f32 v206, v206, v81
v_mul_f32 v207, v207, v81
v_mul_f32 v208, v208, v80
v_mul_f32 v209, v209, v80
v_mul_f32 v210, v210, v80
v_mul_f32 v211, v211, v80
v_mul_f32 v212, v212, v81
v_mul_f32 v213, v213, v81
v_mul_f32 v214, v214, v81
v_mul_f32 v215, v215, v81
v_mul_f32 v216, v216, v80
v_mul_f32 v217, v217, v80
v_mul_f32 v218, v218, v80
v_mul_f32 v219, v219, v80
v_mul_f32 v220, v220, v81
v_mul_f32 v221, v221, v81
v_mul_f32 v222, v222, v81
v_mul_f32 v223, v223, v81
v_accvgpr_read_b32 v78, a192
v_mul_f32 v78, v78, v80
v_accvgpr_write_b32 a192, v78
v_accvgpr_read_b32 v79, a196
v_mul_f32 v79, v79, v81
v_accvgpr_write_b32 a196, v79
s_mov_b32 s84, 0
s_nop 4
s_branch L_backB4
L_rareA5:
s_nop 7
v_mov_b32 v250, v248
s_nop 1
v_permlane16_swap_b32 v248, v250
v_max_f32 v248, v248, v250
v_mov_b32 v250, v248
s_nop 1
v_permlane32_swap_b32 v248, v250
v_max_f32 v248, v248, v250
v_max_f32 v248, 0, v248
v_add_f32 v76, v76, v248
v_sub_f32 v80, 0, v248
v_exp_f32 v80, v80
v_sub_f32 v32, v32, v248
v_sub_f32 v33, v33, v248
v_sub_f32 v34, v34, v248
v_sub_f32 v35, v35, v248
v_sub_f32 v40, v40, v248
v_sub_f32 v41, v41, v248
v_sub_f32 v42, v42, v248
v_sub_f32 v43, v43, v248
v_sub_f32 v48, v48, v248
v_sub_f32 v49, v49, v248
v_sub_f32 v50, v50, v248
v_sub_f32 v51, v51, v248
v_sub_f32 v56, v56, v248
v_sub_f32 v57, v57, v248
v_sub_f32 v58, v58, v248
v_sub_f32 v59, v59, v248
v_sub_f32 v64, 0, v76
v_sub_f32 v65, 0, v76
v_sub_f32 v66, 0, v76
v_sub_f32 v67, 0, v76
v_mov_b32 v250, v249
s_nop 1
v_permlane16_swap_b32 v249, v250
v_max_f32 v249, v249, v250
v_mov_b32 v250, v249
s_nop 1
v_permlane32_swap_b32 v249, v250
v_max_f32 v249, v249, v250
v_max_f32 v249, 0, v249
v_add_f32 v77, v77, v249
v_sub_f32 v81, 0, v249
v_exp_f32 v81, v81
v_sub_f32 v36, v36, v249
v_sub_f32 v37, v37, v249
v_sub_f32 v38, v38, v249
v_sub_f32 v39, v39, v249
v_sub_f32 v44, v44, v249
v_sub_f32 v45, v45, v249
v_sub_f32 v46, v46, v249
v_sub_f32 v47, v47, v249
v_sub_f32 v52, v52, v249
v_sub_f32 v53, v53, v249
v_sub_f32 v54, v54, v249
v_sub_f32 v55, v55, v249
v_sub_f32 v60, v60, v249
v_sub_f32 v61, v61, v249
v_sub_f32 v62, v62, v249
v_sub_f32 v63, v63, v249
v_sub_f32 v68, 0, v77
v_sub_f32 v69, 0, v77
v_sub_f32 v70, 0, v77
v_sub_f32 v71, 0, v77
s_mov_b32 s84, 1
s_nop 4
s_branch L_backA5
L_rareB5:
s_nop 15
v_mul_f32 v96, v96, v80
v_mul_f32 v97, v97, v80
v_mul_f32 v98, v98, v80
v_mul_f32 v99, v99, v80
v_mul_f32 v100, v100, v81
v_mul_f32 v101, v101, v81
v_mul_f32 v102, v102, v81
v_mul_f32 v103, v103, v81
v_mul_f32 v104, v104, v80
v_mul_f32 v105, v105, v80
v_mul_f32 v106, v106, v80
v_mul_f32 v107, v107, v80
v_mul_f32 v108, v108, v81
v_mul_f32 v109, v109, v81
v_mul_f32 v110, v110, v81
v_mul_f32 v111, v111, v81
v_mul_f32 v112, v112, v80
v_mul_f32 v113, v113, v80
v_mul_f32 v114, v114, v80
v_mul_f32 v115, v115, v80
v_mul_f32 v116, v116, v81
v_mul_f32 v117, v117, v81
v_mul_f32 v118, v118, v81
v_mul_f32 v119, v119, v81
v_mul_f32 v120, v120, v80
v_mul_f32 v121, v121, v80
v_mul_f32 v122, v122, v80
v_mul_f32 v123, v123, v80
v_mul_f32 v124, v124, v81
v_mul_f32 v125, v125, v81
v_mul_f32 v126, v126, v81
v_mul_f32 v127, v127, v81
v_mul_f32 v128, v128, v80
v_mul_f32 v129, v129, v80
v_mul_f32 v130, v130, v80
v_mul_f32 v131, v131, v80
v_mul_f32 v132, v132, v81
v_mul_f32 v133, v133, v81
v_mul_f32 v134, v134, v81
v_mul_f32 v135, v135, v81
v_mul_f32 v136, v136, v80
v_mul_f32 v137, v137, v80
v_mul_f32 v138, v138, v80
v_mul_f32 v139, v139, v80
v_mul_f32 v140, v140, v81
v_mul_f32 v141, v141, v81
v_mul_f32 v142, v142, v81
v_mul_f32 v143, v143, v81
v_mul_f32 v144, v144, v80
v_mul_f32 v145, v145, v80
v_mul_f32 v146, v146, v80
v_mul_f32 v147, v147, v80
v_mul_f32 v148, v148, v81
v_mul_f32 v149, v149, v81
v_mul_f32 v150, v150, v81
v_mul_f32 v151, v151, v81
v_mul_f32 v152, v152, v80
v_mul_f32 v153, v153, v80
v_mul_f32 v154, v154, v80
v_mul_f32 v155, v155, v80
v_mul_f32 v156, v156, v81
v_mul_f32 v157, v157, v81
v_mul_f32 v158, v158, v81
v_mul_f32 v159, v159, v81
v_mul_f32 v160, v160, v80
v_mul_f32 v161, v161, v80
v_mul_f32 v162, v162, v80
v_mul_f32 v163, v163, v80
v_mul_f32 v164, v164, v81
v_mul_f32 v165, v165, v81
v_mul_f32 v166, v166, v81
v_mul_f32 v167, v167, v81
v_mul_f32 v168, v168, v80
v_mul_f32 v169, v169, v80
v_mul_f32 v170, v170, v80
v_mul_f32 v171, v171, v80
v_mul_f32 v172, v172, v81
v_mul_f32 v173, v173, v81
v_mul_f32 v174, v174, v81
v_mul_f32 v175, v175, v81
v_mul_f32 v176, v176, v80
v_mul_f32 v177, v177, v80
v_mul_f32 v178, v178, v80
v_mul_f32 v179, v179, v80
v_mul_f32 v180, v180, v81
v_mul_f32 v181, v181, v81
v_mul_f32 v182, v182, v81
v_mul_f32 v183, v183, v81
v_mul_f32 v184, v184, v80
v_mul_f32 v185, v185, v80
v_mul_f32 v186, v186, v80
v_mul_f32 v187, v187, v80
v_mul_f32 v188, v188, v81
v_mul_f32 v189, v189, v81
v_mul_f32 v190, v190, v81
v_mul_f32 v191, v191, v81
v_mul_f32 v192, v192, v80
v_mul_f32 v193, v193, v80
v_mul_f32 v194, v194, v80
v_mul_f32 v195, v195, v80
v_mul_f32 v196, v196, v81
v_mul_f32 v197, v197, v81
v_mul_f32 v198, v198, v81
v_mul_f32 v199, v199, v81
v_mul_f32 v200, v200, v80
v_mul_f32 v201, v201, v80
v_mul_f32 v202, v202, v80
v_mul_f32 v203, v203, v80
v_mul_f32 v204, v204, v81
v_mul_f32 v205, v205, v81
v_mul_f32 v206, v206, v81
v_mul_f32 v207, v207, v81
v_mul_f32 v208, v208, v80
v_mul_f32 v209, v209, v80
v_mul_f32 v210, v210, v80
v_mul_f32 v211, v211, v80
v_mul_f32 v212, v212, v81
v_mul_f32 v213, v213, v81
v_mul_f32 v214, v214, v81
v_mul_f32 v215, v215, v81
v_mul_f32 v216, v216, v80
v_mul_f32 v217, v217, v80
v_mul_f32 v218, v218, v80
v_mul_f32 v219, v219, v80
v_mul_f32 v220, v220, v81
v_mul_f32 v221, v221, v81
v_mul_f32 v222, v222, v81
v_mul_f32 v223, v223, v81
v_accvgpr_read_b32 v78, a192
v_mul_f32 v78, v78, v80
v_accvgpr_write_b32 a192, v78
v_accvgpr_read_b32 v79, a196
v_mul_f32 v79, v79, v81
v_accvgpr_write_b32 a196, v79
s_mov_b32 s84, 0
s_nop 4
s_branch L_backB5
L_rareA6:
s_nop 7
v_mov_b32 v250, v248
s_nop 1
v_permlane16_swap_b32 v248, v250
v_max_f32 v248, v248, v250
v_mov_b32 v250, v248
s_nop 1
v_permlane32_swap_b32 v248, v250
v_max_f32 v248, v248, v250
v_max_f32 v248, 0, v248
v_add_f32 v76, v76, v248
v_sub_f32 v80, 0, v248
v_exp_f32 v80, v80
v_sub_f32 v0, v0, v248
v_sub_f32 v1, v1, v248
v_sub_f32 v2, v2, v248
v_sub_f32 v3, v3, v248
v_sub_f32 v8, v8, v248
v_sub_f32 v9, v9, v248
v_sub_f32 v10, v10, v248
v_sub_f32 v11, v11, v248
v_sub_f32 v16, v16, v248
v_sub_f32 v17, v17, v248
v_sub_f32 v18, v18, v248
v_sub_f32 v19, v19, v248
v_sub_f32 v24, v24, v248
v_sub_f32 v25, v25, v248
v_sub_f32 v26, v26, v248
v_sub_f32 v27, v27, v248
v_sub_f32 v64, 0, v76
v_sub_f32 v65, 0, v76
v_sub_f32 v66, 0, v76
v_sub_f32 v67, 0, v76
v_mov_b32 v250, v249
s_nop 1
v_permlane16_swap_b32 v249, v250
v_max_f32 v249, v249, v250
v_mov_b32 v250, v249
s_nop 1
v_permlane32_swap_b32 v249, v250
v_max_f32 v249, v249, v250
v_max_f32 v249, 0, v249
v_add_f32 v77, v77, v249
v_sub_f32 v81, 0, v249
v_exp_f32 v81, v81
v_sub_f32 v4, v4, v249
v_sub_f32 v5, v5, v249
v_sub_f32 v6, v6, v249
v_sub_f32 v7, v7, v249
v_sub_f32 v12, v12, v249
v_sub_f32 v13, v13, v249
v_sub_f32 v14, v14, v249
v_sub_f32 v15, v15, v249
v_sub_f32 v20, v20, v249
v_sub_f32 v21, v21, v249
v_sub_f32 v22, v22, v249
v_sub_f32 v23, v23, v249
v_sub_f32 v28, v28, v249
v_sub_f32 v29, v29, v249
v_sub_f32 v30, v30, v249
v_sub_f32 v31, v31, v249
v_sub_f32 v68, 0, v77
v_sub_f32 v69, 0, v77
v_sub_f32 v70, 0, v77
v_sub_f32 v71, 0, v77
s_mov_b32 s84, 1
s_nop 4
s_branch L_backA6
L_rareB6:
s_nop 15
v_mul_f32 v96, v96, v80
v_mul_f32 v97, v97, v80
v_mul_f32 v98, v98, v80
v_mul_f32 v99, v99, v80
v_mul_f32 v100, v100, v81
v_mul_f32 v101, v101, v81
v_mul_f32 v102, v102, v81
v_mul_f32 v103, v103, v81
v_mul_f32 v104, v104, v80
v_mul_f32 v105, v105, v80
v_mul_f32 v106, v106, v80
v_mul_f32 v107, v107, v80
v_mul_f32 v108, v108, v81
v_mul_f32 v109, v109, v81
v_mul_f32 v110, v110, v81
v_mul_f32 v111, v111, v81
v_mul_f32 v112, v112, v80
v_mul_f32 v113, v113, v80
v_mul_f32 v114, v114, v80
v_mul_f32 v115, v115, v80
v_mul_f32 v116, v116, v81
v_mul_f32 v117, v117, v81
v_mul_f32 v118, v118, v81
v_mul_f32 v119, v119, v81
v_mul_f32 v120, v120, v80
v_mul_f32 v121, v121, v80
v_mul_f32 v122, v122, v80
v_mul_f32 v123, v123, v80
v_mul_f32 v124, v124, v81
v_mul_f32 v125, v125, v81
v_mul_f32 v126, v126, v81
v_mul_f32 v127, v127, v81
v_mul_f32 v128, v128, v80
v_mul_f32 v129, v129, v80
v_mul_f32 v130, v130, v80
v_mul_f32 v131, v131, v80
v_mul_f32 v132, v132, v81
v_mul_f32 v133, v133, v81
v_mul_f32 v134, v134, v81
v_mul_f32 v135, v135, v81
v_mul_f32 v136, v136, v80
v_mul_f32 v137, v137, v80
v_mul_f32 v138, v138, v80
v_mul_f32 v139, v139, v80
v_mul_f32 v140, v140, v81
v_mul_f32 v141, v141, v81
v_mul_f32 v142, v142, v81
v_mul_f32 v143, v143, v81
v_mul_f32 v144, v144, v80
v_mul_f32 v145, v145, v80
v_mul_f32 v146, v146, v80
v_mul_f32 v147, v147, v80
v_mul_f32 v148, v148, v81
v_mul_f32 v149, v149, v81
v_mul_f32 v150, v150, v81
v_mul_f32 v151, v151, v81
v_mul_f32 v152, v152, v80
v_mul_f32 v153, v153, v80
v_mul_f32 v154, v154, v80
v_mul_f32 v155, v155, v80
v_mul_f32 v156, v156, v81
v_mul_f32 v157, v157, v81
v_mul_f32 v158, v158, v81
v_mul_f32 v159, v159, v81
v_mul_f32 v160, v160, v80
v_mul_f32 v161, v161, v80
v_mul_f32 v162, v162, v80
v_mul_f32 v163, v163, v80
v_mul_f32 v164, v164, v81
v_mul_f32 v165, v165, v81
v_mul_f32 v166, v166, v81
v_mul_f32 v167, v167, v81
v_mul_f32 v168, v168, v80
v_mul_f32 v169, v169, v80
v_mul_f32 v170, v170, v80
v_mul_f32 v171, v171, v80
v_mul_f32 v172, v172, v81
v_mul_f32 v173, v173, v81
v_mul_f32 v174, v174, v81
v_mul_f32 v175, v175, v81
v_mul_f32 v176, v176, v80
v_mul_f32 v177, v177, v80
v_mul_f32 v178, v178, v80
v_mul_f32 v179, v179, v80
v_mul_f32 v180, v180, v81
v_mul_f32 v181, v181, v81
v_mul_f32 v182, v182, v81
v_mul_f32 v183, v183, v81
v_mul_f32 v184, v184, v80
v_mul_f32 v185, v185, v80
v_mul_f32 v186, v186, v80
v_mul_f32 v187, v187, v80
v_mul_f32 v188, v188, v81
v_mul_f32 v189, v189, v81
v_mul_f32 v190, v190, v81
v_mul_f32 v191, v191, v81
v_mul_f32 v192, v192, v80
v_mul_f32 v193, v193, v80
v_mul_f32 v194, v194, v80
v_mul_f32 v195, v195, v80
v_mul_f32 v196, v196, v81
v_mul_f32 v197, v197, v81
v_mul_f32 v198, v198, v81
v_mul_f32 v199, v199, v81
v_mul_f32 v200, v200, v80
v_mul_f32 v201, v201, v80
v_mul_f32 v202, v202, v80
v_mul_f32 v203, v203, v80
v_mul_f32 v204, v204, v81
v_mul_f32 v205, v205, v81
v_mul_f32 v206, v206, v81
v_mul_f32 v207, v207, v81
v_mul_f32 v208, v208, v80
v_mul_f32 v209, v209, v80
v_mul_f32 v210, v210, v80
v_mul_f32 v211, v211, v80
v_mul_f32 v212, v212, v81
v_mul_f32 v213, v213, v81
v_mul_f32 v214, v214, v81
v_mul_f32 v215, v215, v81
v_mul_f32 v216, v216, v80
v_mul_f32 v217, v217, v80
v_mul_f32 v218, v218, v80
v_mul_f32 v219, v219, v80
v_mul_f32 v220, v220, v81
v_mul_f32 v221, v221, v81
v_mul_f32 v222, v222, v81
v_mul_f32 v223, v223, v81
v_accvgpr_read_b32 v78, a192
v_mul_f32 v78, v78, v80
v_accvgpr_write_b32 a192, v78
v_accvgpr_read_b32 v79, a196
v_mul_f32 v79, v79, v81
v_accvgpr_write_b32 a196, v79
s_mov_b32 s84, 0
s_nop 4
s_branch L_backB6
L_rareA7:
s_nop 7
v_mov_b32 v250, v248
s_nop 1
v_permlane16_swap_b32 v248, v250
v_max_f32 v248, v248, v250
v_mov_b32 v250, v248
s_nop 1
v_permlane32_swap_b32 v248, v250
v_max_f32 v248, v248, v250
v_max_f32 v248, 0, v248
v_add_f32 v76, v76, v248
v_sub_f32 v80, 0, v248
v_exp_f32 v80, v80
v_sub_f32 v32, v32, v248
v_sub_f32 v33, v33, v248
v_sub_f32 v34, v34, v248
v_sub_f32 v35, v35, v248
v_sub_f32 v40, v40, v248
v_sub_f32 v41, v41, v248
v_sub_f32 v42, v42, v248
v_sub_f32 v43, v43, v248
v_sub_f32 v48, v48, v248
v_sub_f32 v49, v49, v248
v_sub_f32 v50, v50, v248
v_sub_f32 v51, v51, v248
v_sub_f32 v56, v56, v248
v_sub_f32 v57, v57, v248
v_sub_f32 v58, v58, v248
v_sub_f32 v59, v59, v248
v_sub_f32 v64, 0, v76
v_sub_f32 v65, 0, v76
v_sub_f32 v66, 0, v76
v_sub_f32 v67, 0, v76
v_mov_b32 v250, v249
s_nop 1
v_permlane16_swap_b32 v249, v250
v_max_f32 v249, v249, v250
v_mov_b32 v250, v249
s_nop 1
v_permlane32_swap_b32 v249, v250
v_max_f32 v249, v249, v250
v_max_f32 v249, 0, v249
v_add_f32 v77, v77, v249
v_sub_f32 v81, 0, v249
v_exp_f32 v81, v81
v_sub_f32 v36, v36, v249
v_sub_f32 v37, v37, v249
v_sub_f32 v38, v38, v249
v_sub_f32 v39, v39, v249
v_sub_f32 v44, v44, v249
v_sub_f32 v45, v45, v249
v_sub_f32 v46, v46, v249
v_sub_f32 v47, v47, v249
v_sub_f32 v52, v52, v249
v_sub_f32 v53, v53, v249
v_sub_f32 v54, v54, v249
v_sub_f32 v55, v55, v249
v_sub_f32 v60, v60, v249
v_sub_f32 v61, v61, v249
v_sub_f32 v62, v62, v249
v_sub_f32 v63, v63, v249
v_sub_f32 v68, 0, v77
v_sub_f32 v69, 0, v77
v_sub_f32 v70, 0, v77
v_sub_f32 v71, 0, v77
s_mov_b32 s84, 1
s_nop 4
s_branch L_backA7
L_rareB7:
s_nop 15
v_mul_f32 v96, v96, v80
v_mul_f32 v97, v97, v80
v_mul_f32 v98, v98, v80
v_mul_f32 v99, v99, v80
v_mul_f32 v100, v100, v81
v_mul_f32 v101, v101, v81
v_mul_f32 v102, v102, v81
v_mul_f32 v103, v103, v81
v_mul_f32 v104, v104, v80
v_mul_f32 v105, v105, v80
v_mul_f32 v106, v106, v80
v_mul_f32 v107, v107, v80
v_mul_f32 v108, v108, v81
v_mul_f32 v109, v109, v81
v_mul_f32 v110, v110, v81
v_mul_f32 v111, v111, v81
v_mul_f32 v112, v112, v80
v_mul_f32 v113, v113, v80
v_mul_f32 v114, v114, v80
v_mul_f32 v115, v115, v80
v_mul_f32 v116, v116, v81
v_mul_f32 v117, v117, v81
v_mul_f32 v118, v118, v81
v_mul_f32 v119, v119, v81
v_mul_f32 v120, v120, v80
v_mul_f32 v121, v121, v80
v_mul_f32 v122, v122, v80
v_mul_f32 v123, v123, v80
v_mul_f32 v124, v124, v81
v_mul_f32 v125, v125, v81
v_mul_f32 v126, v126, v81
v_mul_f32 v127, v127, v81
v_mul_f32 v128, v128, v80
v_mul_f32 v129, v129, v80
v_mul_f32 v130, v130, v80
v_mul_f32 v131, v131, v80
v_mul_f32 v132, v132, v81
v_mul_f32 v133, v133, v81
v_mul_f32 v134, v134, v81
v_mul_f32 v135, v135, v81
v_mul_f32 v136, v136, v80
v_mul_f32 v137, v137, v80
v_mul_f32 v138, v138, v80
v_mul_f32 v139, v139, v80
v_mul_f32 v140, v140, v81
v_mul_f32 v141, v141, v81
v_mul_f32 v142, v142, v81
v_mul_f32 v143, v143, v81
v_mul_f32 v144, v144, v80
v_mul_f32 v145, v145, v80
v_mul_f32 v146, v146, v80
v_mul_f32 v147, v147, v80
v_mul_f32 v148, v148, v81
v_mul_f32 v149, v149, v81
v_mul_f32 v150, v150, v81
v_mul_f32 v151, v151, v81
v_mul_f32 v152, v152, v80
v_mul_f32 v153, v153, v80
v_mul_f32 v154, v154, v80
v_mul_f32 v155, v155, v80
v_mul_f32 v156, v156, v81
v_mul_f32 v157, v157, v81
v_mul_f32 v158, v158, v81
v_mul_f32 v159, v159, v81
v_mul_f32 v160, v160, v80
v_mul_f32 v161, v161, v80
v_mul_f32 v162, v162, v80
v_mul_f32 v163, v163, v80
v_mul_f32 v164, v164, v81
v_mul_f32 v165, v165, v81
v_mul_f32 v166, v166, v81
v_mul_f32 v167, v167, v81
v_mul_f32 v168, v168, v80
v_mul_f32 v169, v169, v80
v_mul_f32 v170, v170, v80
v_mul_f32 v171, v171, v80
v_mul_f32 v172, v172, v81
v_mul_f32 v173, v173, v81
v_mul_f32 v174, v174, v81
v_mul_f32 v175, v175, v81
v_mul_f32 v176, v176, v80
v_mul_f32 v177, v177, v80
v_mul_f32 v178, v178, v80
v_mul_f32 v179, v179, v80
v_mul_f32 v180, v180, v81
v_mul_f32 v181, v181, v81
v_mul_f32 v182, v182, v81
v_mul_f32 v183, v183, v81
v_mul_f32 v184, v184, v80
v_mul_f32 v185, v185, v80
v_mul_f32 v186, v186, v80
v_mul_f32 v187, v187, v80
v_mul_f32 v188, v188, v81
v_mul_f32 v189, v189, v81
v_mul_f32 v190, v190, v81
v_mul_f32 v191, v191, v81
v_mul_f32 v192, v192, v80
v_mul_f32 v193, v193, v80
v_mul_f32 v194, v194, v80
v_mul_f32 v195, v195, v80
v_mul_f32 v196, v196, v81
v_mul_f32 v197, v197, v81
v_mul_f32 v198, v198, v81
v_mul_f32 v199, v199, v81
v_mul_f32 v200, v200, v80
v_mul_f32 v201, v201, v80
v_mul_f32 v202, v202, v80
v_mul_f32 v203, v203, v80
v_mul_f32 v204, v204, v81
v_mul_f32 v205, v205, v81
v_mul_f32 v206, v206, v81
v_mul_f32 v207, v207, v81
v_mul_f32 v208, v208, v80
v_mul_f32 v209, v209, v80
v_mul_f32 v210, v210, v80
v_mul_f32 v211, v211, v80
v_mul_f32 v212, v212, v81
v_mul_f32 v213, v213, v81
v_mul_f32 v214, v214, v81
v_mul_f32 v215, v215, v81
v_mul_f32 v216, v216, v80
v_mul_f32 v217, v217, v80
v_mul_f32 v218, v218, v80
v_mul_f32 v219, v219, v80
v_mul_f32 v220, v220, v81
v_mul_f32 v221, v221, v81
v_mul_f32 v222, v222, v81
v_mul_f32 v223, v223, v81
v_accvgpr_read_b32 v78, a192
v_mul_f32 v78, v78, v80
v_accvgpr_write_b32 a192, v78
v_accvgpr_read_b32 v79, a196
v_mul_f32 v79, v79, v81
v_accvgpr_write_b32 a196, v79
s_mov_b32 s84, 0
s_nop 4
s_branch L_backB7
L_idle:
s_waitcnt vmcnt(0) lgkmcnt(0)
s_branch L_end
L_sps:
s_mov_b32 s97, 200000
L_sps_l:
s_sleep 1
buffer_load_dword v236, v234, s[64:67], s82 offen offset:4 sc1
s_waitcnt vmcnt(0)
v_cmp_ne_u32 vcc, v236, v232
s_sub_u32 s97, s97, 1
s_cbranch_scc1 L_bksx
s_cbranch_vccnz L_sps_l
L_bksx:
s_branch L_bks
L_end:

)ASM" : "+v"(tid) : "s"(__builtin_amdgcn_kernarg_segment_ptr()),"s"(blockIdx.x) : "v0","v1","v2","v3","v4","v5","v6","v7","v8","v9","v10","v11","v12","v13","v14","v15","v16","v17","v18","v19","v20","v21","v22","v23","v24","v25","v26","v27","v28","v29","v30","v31","v32","v33","v34","v35","v36","v37","v38","v39","v40","v41","v42","v43","v44","v45","v46","v47","v48","v49","v50","v51","v52","v53","v54","v55","v56","v57","v58","v59","v60","v61","v62","v63","v64","v65","v66","v67","v68","v69","v70","v71","v72","v73","v74","v75","v76","v77","v78","v79","v80","v81","v82","v83","v84","v85","v86","v87","v88","v89","v90","v91","v92","v93","v94","v95","v96","v97","v98","v99","v100","v101","v102","v103","v104","v105","v106","v107","v108","v109","v110","v111","v112","v113","v114","v115","v116","v117","v118","v119","v120","v121","v122","v123","v124","v125","v126","v127","v128","v129","v130","v131","v132","v133","v134","v135","v136","v137","v138","v139","v140","v141","v142","v143","v144","v145","v146","v147","v148","v149","v150","v151","v152","v153","v154","v155","v156","v157","v158","v159","v160","v161","v162","v163","v164","v165","v166","v167","v168","v169","v170","v171","v172","v173","v174","v175","v176","v177","v178","v179","v180","v181","v182","v183","v184","v185","v186","v187","v188","v189","v190","v191","v192","v193","v194","v195","v196","v197","v198","v199","v200","v201","v202","v203","v204","v205","v206","v207","v208","v209","v210","v211","v212","v213","v214","v215","v216","v217","v218","v219","v220","v221","v222","v223","v224","v225","v226","v227","v228","v229","v230","v231","v232","v233","v234","v235","v236","v237","v238","v239","v240","v241","v242","v243","v244","v245","v246","v247","v248","v249","v250","v251","v252","v253","v254","a0","a1","a2","a3","a4","a5","a6","a7","a8","a9","a10","a11","a12","a13","a14","a15","a16","a17","a18","a19","a20","a21","a22","a23","a24","a25","a26","a27","a28","a29","a30","a31","a32","a33","a34","a35","a36","a37","a38","a39","a40","a41","a42","a43","a44","a45","a46","a47","a48","a49","a50","a51","a52","a53","a54","a55","a56","a57","a58","a59","a60","a61","a62","a63","a64","a65","a66","a67","a68","a69","a70","a71","a72","a73","a74","a75","a76","a77","a78","a79","a80","a81","a82","a83","a84","a85","a86","a87","a88","a89","a90","a91","a92","a93","a94","a95","a96","a97","a98","a99","a100","a101","a102","a103","a104","a105","a106","a107","a108","a109","a110","a111","a112","a113","a114","a115","a116","a117","a118","a119","a120","a121","a122","a123","a124","a125","a126","a127","a128","a129","a130","a131","a132","a133","a134","a135","a136","a137","a138","a139","a140","a141","a142","a143","a144","a145","a146","a147","a148","a149","a150","a151","a152","a153","a154","a155","a156","a157","a158","a159","a160","a161","a162","a163","a164","a165","a166","a167","a168","a169","a170","a171","a172","a173","a174","a175","a176","a177","a178","a179","a180","a181","a182","a183","a184","a185","a186","a187","a188","a189","a190","a191","a192","a193","a194","a195","a196","a197","a198","a199","a200","a201","a202","a203","a204","a205","a206","a207","a208","a209","a210","a211","a212","a213","a214","a215","a216","a217","a218","a219","a220","a221","a222","a223","a224","a225","a226","a227","a228","a229","a230","a231","a232","a233","a234","a235","a236","a237","a238","a239","a240","a241","a242","a243","a244","a245","a246","a247","a248","a249","a250","a251","a252","a253","a254","a255","s4","s5","s6","s7","s8","s9","s10","s11","s12","s13","s14","s15","s16","s17","s18","s19","s20","s21","s22","s23","s24","s25","s26","s27","s28","s29","s30","s31","s34","s35","s36","s37","s38","s39","s40","s41","s42","s43","s44","s45","s46","s47","s48","s49","s50","s51","s52","s53","s54","s55","s56","s57","s58","s59","s60","s61","s62","s63","s64","s65","s66","s67","s68","s69","s70","s71","s72","s73","s74","s75","s76","s77","s78","s79","s80","s81","s82","s83","s84","s85","s86","s87","s88","s89","s90","s91","s92","s93","s94","s95","s96","s97","s98","s99","vcc","scc","memory");
}
extern "C" void kernel_launch(void* const* d_in, const int* in_sizes, int n_in, void* d_out, int out_size, void* d_ws, size_t ws_size, hipStream_t stream) {
  (void)hipFuncSetAttribute((const void*)fused_block, hipFuncAttributeMaxDynamicSharedMemorySize, 163840);
  hipLaunchKernelGGL(fused_block, dim3(256), dim3(256), 163840, stream, d_in[0],d_in[1],d_in[2],d_in[3],d_in[4],d_in[6],d_in[7],d_in[8],d_ws,d_out);
}
```
